```python
import torch

def _softmax(x, axis=-1):
    return torch.softmax(x, dim=axis)
def _gelu(x):
    return torch.nn.functional.gelu(x, approximate="tanh")
def _silu(x):
    return torch.nn.functional.silu(x)
def _sigmoid(x):
    return torch.sigmoid(x)
def _layernorm(x, axis=-1, eps=1e-5):
    m = x.mean(dim=axis, keepdim=True)
    v = x.var(dim=axis, keepdim=True, unbiased=False)
    return (x - m) / torch.sqrt(v + eps)
def _scatter_add(tgt, idx, src, axis=0):
    return tgt.clone().index_add_(axis, idx, src)
def _segment_sum(src, seg, n):
    out = torch.zeros((n,) + src.shape[1:], dtype=src.dtype)
    return out.index_add_(0, seg, src)
def _pad(x, pw):
    flat = tuple(v for pair in reversed(pw) for v in pair)
    return torch.nn.functional.pad(x, flat)

kernel_name = 'gated_mix__grouped_kv_mqa__mixer_token_mlp__prompt'

def setup_inputs():

    _w = (lambda a: (lambda t: t.to(torch.bfloat16) if t.is_floating_point() else t)(torch.as_tensor(a)))
    def _A_setup():
        B, S, D, H = 1, 2048, 1024, 32
        Dh = D // H
        return {
            "x": _w((lambda *s: torch.randn(*s, dtype=torch.bfloat16) * 0.05)(B, S, D)),
            "wq": _w((lambda *s: torch.randn(*s, dtype=torch.bfloat16) * 0.8)(D, H * Dh)),
            "wk": _w((lambda *s: torch.randn(*s, dtype=torch.bfloat16) * 0.8)(D, Dh)),
            "wv": _w((lambda *s: torch.randn(*s, dtype=torch.bfloat16) * 0.05)(D, Dh)),
            "wo": _w((lambda *s: torch.randn(*s, dtype=torch.bfloat16) * 0.05)(H * Dh, D)),
        }
    _A_in = _A_setup()

    def _B_setup():
        B, S, D, F = 1, 2048, 1024, 3584
        return {
            "x": _w((lambda *s: torch.randn(*s, dtype=torch.bfloat16) * 0.05)(B, S, D)),
            "w1": _w((lambda *s: torch.randn(*s, dtype=torch.bfloat16) * 0.4)(S, F) * (S ** -0.5)),
            "b1": _w((lambda *s: torch.randn(*s, dtype=torch.bfloat16) * 3.2)(F) * 0.02),
            "w2": _w((lambda *s: torch.randn(*s, dtype=torch.bfloat16) * 0.1)(F, S) * (F ** -0.5)),
            "b2": _w((lambda *s: torch.randn(*s, dtype=torch.bfloat16) * 0.4)(S) * 0.02),
        }
    _B_in = _B_setup()

    return {
        "A_x": _A_in["x"],
        "A_wq": _A_in["wq"],
        "A_wk": _A_in["wk"],
        "A_wv": _A_in["wv"],
        "A_wo": _A_in["wo"],
        "B_x": _B_in["x"],
        "B_w1": _B_in["w1"],
        "B_b1": _B_in["b1"],
        "B_w2": _B_in["w2"],
        "B_b2": _B_in["b2"],
        "gate_w": _w((lambda *s: torch.randn(*s, dtype=torch.bfloat16) * 0.05)(1024) * 16.0),
    }

def reference(A_x, A_wq, A_wk, A_wv, A_wo, B_x, B_w1, B_b1, B_w2, B_b2, gate_w):
    def _A_ref(x, wq, wk, wv, wo):
        B, S, D = x.shape
        H = 32
        Dh = D // H
        q = (lambda x, *d: x.permute(*d))((x @ wq).reshape(B, S, H, Dh), 0, 2, 1, 3)
        k = x @ wk
        v = x @ wv
        scores = torch.einsum("bhqd,bkd->bhqk", q, k) * (Dh ** -0.5)
        p = _softmax(scores, axis=-1)
        o = torch.einsum("bhqk,bkd->bhqd", p, v)
        return (lambda x, *d: x.permute(*d))(o, 0, 2, 1, 3).reshape(B, S, H * Dh) @ wo

    def _B_ref(x, w1, b1, w2, b2):
        B, S, D = x.shape
        h = _layernorm(x, axis=-1)
        u = torch.einsum("bsd,sf->bdf", h, w1) + b1
        u = _gelu(u)
        y = torch.einsum("bdf,fs->bds", u, w2) + b2
        return (lambda x, *d: x.permute(*d))(y, 0, 2, 1)

    x = A_x
    g = _sigmoid((x * gate_w).sum(-1, keepdims=True))
    a = _A_ref(x, A_wq, A_wk, A_wv, A_wo)
    b = _B_ref(x, B_w1, B_b1, B_w2, B_b2)
    return g * a + (1 - g) * b
```

```cpp
#include <hip/hip_runtime.h>
#include <stdint.h>

typedef unsigned u32;
typedef u32 u32x4 __attribute__((ext_vector_type(4)));
typedef u32 u32x2 __attribute__((ext_vector_type(2)));
typedef float f32x2 __attribute__((ext_vector_type(2)));
typedef float f32x4 __attribute__((ext_vector_type(4)));
typedef float f32x16 __attribute__((ext_vector_type(16)));
typedef __bf16 bf16x2 __attribute__((ext_vector_type(2)));
typedef __bf16 bf16x8 __attribute__((ext_vector_type(8)));
typedef short s16x4 __attribute__((ext_vector_type(4)));
#define LDS __attribute__((address_space(3)))
typedef LDS char* lds_ptr;

constexpr int S = 2048, D = 1024, F = 3584;
constexpr u32 WS_HF = 0, WS_QF = 4u << 20, WS_OF = 8u << 20, WS_UF = 12u << 20, WS_KV = 20u << 20;
constexpr u32 WS_G = WS_KV + (256u << 10), WS_BND = WS_G + (8u << 10);
constexpr float QSCALE = 0.17677669529663687f * 1.4426950408889634f;
constexpr float SAFE_LOG2 = 60.f;
constexpr float SAFE_V = 1.0e12f;

#define SB() __builtin_amdgcn_sched_barrier(0)
#define PIN(x) asm volatile("" : "+v"(x))
#define M16(a, b, c) __builtin_amdgcn_mfma_f32_16x16x32_bf16(__builtin_bit_cast(bf16x8, a), __builtin_bit_cast(bf16x8, b), c, 0, 0, 0)
#define M32(a, b, c) __builtin_amdgcn_mfma_f32_32x32x16_bf16(__builtin_bit_cast(bf16x8, a), __builtin_bit_cast(bf16x8, b), c, 0, 0, 0)
#define EX2(x) __builtin_amdgcn_exp2f(x)

__device__ __forceinline__ u32 cvtpk(float lo, float hi) { f32x2 v = {lo, hi}; return __builtin_bit_cast(u32, __builtin_convertvector(v, bf16x2)); }
__device__ __forceinline__ float bf_lo(u32 v) { return __uint_as_float(v << 16); }
__device__ __forceinline__ float bf_hi(u32 v) { return __uint_as_float(v & 0xffff0000u); }
__device__ __forceinline__ float bf_round(float x) { return bf_lo(cvtpk(x, x)); }
__device__ __forceinline__ u32x4 pack8(const f32x4& a, const f32x4& b) { return u32x4{cvtpk(a[0], a[1]), cvtpk(a[2], a[3]), cvtpk(b[0], b[1]), cvtpk(b[2], b[3])}; }

__device__ __forceinline__ u32x4 make_srd(const void* p, u32 bytes) {
    const uint64_t a = (uint64_t)p;
    return u32x4{(u32)a, (u32)(a >> 32) & 0xffffu, bytes, 0x00020000u};
}
__device__ __forceinline__ void dma16(u32 lds, u32 voff, const u32x4& srd, u32 soff) {
    asm volatile("s_mov_b32 m0, %0\n\ts_nop 0\n\tbuffer_load_dwordx4 %1, %2, %3 offen lds" ::"s"(lds), "v"(voff), "s"(srd), "s"(soff) : "memory");
}
template <int N> __device__ __forceinline__ void wait_bar() { asm volatile("s_waitcnt vmcnt(%0) lgkmcnt(0)\n\ts_barrier" ::"n"(N) : "memory"); }
__device__ __forceinline__ u32x4 lds_rd128(lds_ptr p) { return *(LDS const u32x4*)p; }
__device__ __forceinline__ u32x2 lds_rdtr(lds_ptr p) { return __builtin_bit_cast(u32x2, __builtin_amdgcn_ds_read_tr16_b64_v4i16((LDS s16x4*)p)); }

template <int R, int NR, int KS> struct OpFrag {
    static constexpr int BYTES = KS * 4 * R * 16, PPW = BYTES / 4096, NF = R / 32, RPF = 1;
    u32x4 srd; u32 voff, ldst; lds_ptr rd;
    __device__ __forceinline__ void init(const void* base, u32 bytes, int row0, lds_ptr img, int w, int wsel, int lane) {
        srd = make_srd(base, bytes);
        voff = (u32)(row0 + lane + w * NR) * 16u;
        ldst = (u32)(uintptr_t)img + w * (R * 16);
        rd = img + ((lane >> 4) * R + wsel * (R / 2) + (lane & 15)) * 16;
    }
    __device__ __forceinline__ void issue(int slot, u32 pos, int i) const {
        constexpr int HB = R / 64;
        const int ks = i / HB, half = i % HB;
        dma16(ldst + pos + ks * (4 * R * 16) + half * 1024, voff, srd, (u32)(slot * KS + ks) * (4u * NR * 16u) + half * 1024u);
    }
    __device__ __forceinline__ void read(u32x4& f, int pos, int ks, int i, int) const { f = lds_rd128(rd + pos + ks * (4 * R * 16) + i * 256); }
};

template <int C, int RS, int KS, bool NAT> struct OpTr {
    static constexpr int BYTES = KS * 32 * C * 2, PPW = BYTES / 4096, NF = C / 32, RPF = 2;
    u32x4 srd; u32 voff, ldst; lds_ptr rd[NF];
    __device__ __forceinline__ void init(const void* base, u32 bytes, int col0, lds_ptr img, int w, int wsel, int lane) {
        srd = make_srd(base, bytes);
        ldst = (u32)(uintptr_t)img + w * 1024;
        const int g = lane >> 4, q = (lane >> 2) & 3, p = lane & 3;
        if constexpr (C == 128) {
            const int f = NAT ? (g | (((w >> 1) & 1) << 2)) : (4 * (w & 1) + g);
            voff = (u32)(4 * w + g) * RS + col0 * 2 + ((((lane & 15) >> 1) ^ f) * 32) + (lane & 1) * 16;
            const int row = NAT ? 8 * g + q : 4 * g + q, fr = NAT ? (q | ((g & 1) << 2)) : (row & 7);
#pragma unroll
            for (int i = 0; i < NF; ++i) rd[i] = img + row * 256 + (((wsel * NF + i) ^ fr) * 32) + p * 8;
        } else {
            static_assert(C == 128 || (C == 64 && !NAT), "unsupported transposed operand");
            voff = (u32)(8 * w + (lane >> 3)) * RS + col0 * 2 + ((((lane & 7) >> 1) ^ g) * 32) + (lane & 1) * 16;
            const int row = 4 * g + q;
#pragma unroll
            for (int i = 0; i < NF; ++i) rd[i] = img + row * 128 + (((wsel * NF + i) ^ ((row >> 1) & 3)) * 32) + p * 8;
        }
    }
    __device__ __forceinline__ void issue(int slot, u32 pos, int i) const {
        constexpr int ROWS = C == 128 ? 16 : 32;
        dma16(ldst + pos + i * 4096, voff, srd, (u32)(slot * KS * 32 + i * ROWS) * RS);
    }
    __device__ __forceinline__ void read(u32x4& f, int pos, int ks, int i, int t) const {
        const u32x2 v = lds_rdtr(rd[i] + pos + ks * (32 * C * 2) + t * ((NAT ? 4 : 16) * C * 2));
        f[2 * t] = v[0]; f[2 * t + 1] = v[1];
    }
};

template <int NFW> struct OpRow {
    static constexpr int BYTES = 128 * 128, PPW = 4, NF = NFW, RPF = 1;
    u32x4 srd; u32 voff, ldst; lds_ptr rd[2];
    __device__ __forceinline__ void init(const void* base, u32 bytes, int row0, lds_ptr img, int w, int wrow, int lane) {
        srd = make_srd(base, bytes);
        ldst = (u32)(uintptr_t)img + w * 1024;
        const int g = lane >> 4, c = lane & 15;
        voff = (u32)(row0 + 8 * w + (lane >> 3)) * (D * 2) + (((lane & 7) ^ (4 * (w & 1) + g)) * 16);
#pragma unroll
        for (int ks = 0; ks < 2; ++ks) rd[ks] = img + (wrow + c) * 128 + (((4 * ks + g) ^ (c >> 1)) * 16);
    }
    __device__ __forceinline__ void issue(int slot, u32 pos, int i) const { dma16(ldst + pos + i * 4096, voff, srd, (u32)slot * 128u + i * (32u * D * 2u)); }
    __device__ __forceinline__ void read(u32x4& f, int pos, int ks, int i, int) const { f = lds_rd128(rd[ks] + pos + i * 2048); }
};

struct OpKV {
    static constexpr int BYTES = 8192, PPW = 2, NF = 4, RPF = 2;
    u32x4 srdk, srdv; u32 voff, ldst; lds_ptr rd[2];
    __device__ __forceinline__ void init(const void* wk, const void* wv, lds_ptr img, int w, int lane) {
        srdk = make_srd(wk, D * 64); srdv = make_srd(wv, D * 64);
        ldst = (u32)(uintptr_t)img + w * 1024;
        voff = (u32)(16 * w + (lane >> 2)) * 64 + ((((lane >> 1) & 1) ^ (lane >> 5)) * 32) + (lane & 1) * 16;
        const int g = lane >> 4, q = (lane >> 2) & 3, p = lane & 3;
#pragma unroll
        for (int ch = 0; ch < 2; ++ch) rd[ch] = img + (8 * g + q) * 64 + ((ch ^ (g & 1)) * 32) + p * 8;
    }
    __device__ __forceinline__ void issue(int slot, u32 pos, int i) const { dma16(ldst + pos + i * 4096, voff, i ? srdv : srdk, (u32)slot * 4096u); }
    __device__ __forceinline__ void read(u32x4& f, int pos, int ks, int i, int t) const {
        const u32x2 v = lds_rdtr(rd[i & 1] + pos + (i >> 1) * 4096 + ks * 2048 + t * 256);
        f[2 * t] = v[0]; f[2 * t + 1] = v[1];
    }
};

struct NoIssue { static constexpr int PPW = 0; __device__ __forceinline__ void issue(int, u32, int) const {} };

template <int NA, int NB> struct Frags { u32x4 a[NA], b[NB]; };

template <bool SWAPHI, int NA, int NB, class RA, class RB, class IA, class IB>
__device__ __forceinline__ void gemm_step(f32x4 (&acc)[NA][NB], const Frags<NA, NB>& cur, Frags<NA, NB>& nxt, bool do_read, const RA& ra, const RB& rb,
                                          int rpos, int rks, bool do_issue, const IA& ia, const IB& ib, int islot, u32 ipos) {
    constexpr int NM = NA * NB, NRA = NA * RA::RPF, NRD = NRA + NB * RB::RPF, ND = IA::PPW + IB::PPW;
    constexpr int RPM = (NRD + NM - 1) / NM, DPM = (ND + NM - 1) / NM, DSTEP = ND >= NM || ND == 0 ? 1 : NM / ND;
#pragma unroll
    for (int m = 0; m < NM; ++m) {
        const int i = m % NA, j = m / NA;
        if (SWAPHI && i >= NA / 2) acc[i][j] = M16(cur.b[j], cur.a[i], acc[i][j]);
        else acc[i][j] = M16(cur.a[i], cur.b[j], acc[i][j]);
        if (do_read) {
#pragma unroll
            for (int r = m * RPM; r < (m + 1) * RPM && r < NRD; ++r) {
                if (r < RB::RPF) rb.read(nxt.b[0], rpos, rks, 0, r);
                else if (r < RB::RPF + NRA) ra.read(nxt.a[(r - RB::RPF) / RA::RPF], rpos, rks, (r - RB::RPF) / RA::RPF, (r - RB::RPF) % RA::RPF);
                else rb.read(nxt.b[(r - NRA) / RB::RPF], rpos, rks, (r - NRA) / RB::RPF, (r - NRA) % RB::RPF);
            }
        }
        if (do_issue && m % DSTEP == 0) {
#pragma unroll
            for (int d = (m / DSTEP) * DPM; d < (m / DSTEP + 1) * DPM && d < ND; ++d) {
                if (d < IA::PPW) ia.issue(islot, ipos, d); else ib.issue(islot, ipos, d - IA::PPW);
            }
        }
        SB();
    }
}

template <int NS, int KS, int SLOTB, int PPW, bool LAST, bool SWAPHI, int NA, int NB, class CA, class CB, class XA, class XB, class IA, class IB>
__device__ __forceinline__ void gemm_round(f32x4 (&acc)[NA][NB], Frags<NA, NB>& f0, Frags<NA, NB>& f1, const CA& ca, const CB& cb, const XA& na, const XB& nb,
                                           const IA& ia, const IB& ib, int islot0) {
    static_assert((NS * KS) % 2 == 0, "fragment buffers alternate");
    constexpr bool ISSUE = IA::PPW + IB::PPW > 0;
#pragma unroll
    for (int p = 0; p < NS; ++p) {
#pragma unroll
        for (int ks = 0; ks < KS; ++ks) {
            Frags<NA, NB>& cur = ((p * KS + ks) & 1) ? f1 : f0;
            Frags<NA, NB>& nxt = ((p * KS + ks) & 1) ? f0 : f1;
            if (ks + 1 < KS) {
                gemm_step<SWAPHI>(acc, cur, nxt, true, ca, cb, p * SLOTB, ks + 1, false, ia, ib, 0, 0);
            } else if (LAST && p == NS - 1) {
                gemm_step<SWAPHI>(acc, cur, nxt, false, ca, cb, 0, 0, false, ia, ib, 0, 0);
            } else {
                if (ISSUE) wait_bar<(NS - 2) * PPW>();
                else if (p == 0) wait_bar<(NS - 2) * PPW>();
                else if (p == 1) wait_bar<(NS > 3 ? NS - 3 : 0) * PPW>();
                else if (p == 2) wait_bar<(NS > 4 ? NS - 4 : 0) * PPW>();
                else wait_bar<0>();
                if (p == NS - 1) gemm_step<SWAPHI>(acc, cur, nxt, true, na, nb, 0, 0, ISSUE, ia, ib, islot0 + p, p * SLOTB);
                else gemm_step<SWAPHI>(acc, cur, nxt, true, ca, cb, (p + 1) * SLOTB, 0, ISSUE, ia, ib, islot0 + p, p * SLOTB);
            }
        }
    }
}

template <int NS, int SLOTB, int NA, int NB, class A, class B>
__device__ __forceinline__ void gemm_prologue(Frags<NA, NB>& f0, const A& a, const B& b) {
#pragma unroll
    for (int p = 0; p < NS; ++p) {
#pragma unroll
        for (int i = 0; i < A::PPW; ++i) a.issue(p, p * SLOTB, i);
#pragma unroll
        for (int i = 0; i < B::PPW; ++i) b.issue(p, p * SLOTB, i);
    }
    wait_bar<(NS - 1) * (A::PPW + B::PPW)>();
#pragma unroll
    for (int i = 0; i < NA * A::RPF; ++i) a.read(f0.a[i / A::RPF], 0, 0, i / A::RPF, i % A::RPF);
#pragma unroll
    for (int i = 0; i < NB * B::RPF; ++i) b.read(f0.b[i / B::RPF], 0, 0, i / B::RPF, i % B::RPF);
    SB();
}

template <int NS, int KS, int NSLOTS, bool SWAPHI, int NA, int NB, class A, class B>
__device__ __forceinline__ void gemm_simple(f32x4 (&acc)[NA][NB], const A& a, const B& b) {
    constexpr int SLOTB = A::BYTES + B::BYTES, PPW = A::PPW + B::PPW;
    static_assert(NSLOTS % NS == 0 && NS <= 5, "ring shape");
    Frags<NA, NB> f0, f1;
    const NoIssue none;
    gemm_prologue<NS, SLOTB>(f0, a, b);
    for (int r = 1; r < NSLOTS / NS; ++r) gemm_round<NS, KS, SLOTB, PPW, false, SWAPHI>(acc, f0, f1, a, b, a, b, a, b, r * NS);
    gemm_round<NS, KS, SLOTB, PPW, true, SWAPHI>(acc, f0, f1, a, b, a, b, none, none, 0);
}

template <int NA, int NB> __device__ __forceinline__ void zero_acc(f32x4 (&acc)[NA][NB]) {
#pragma unroll
    for (int i = 0; i < NA; ++i)
#pragma unroll
        for (int j = 0; j < NB; ++j) acc[i][j] = f32x4{};
}

constexpr int PRE_Q = 128, PRE_KV = 16, PRE_LN = 64, PRE_LDS = 4 * 32768;

__device__ __forceinline__ void q_tile(int b, const void* x, const void* wq, char* ws, lds_ptr lds, int w, int lane) {
    const int s0 = ((b & 7) * 2 + ((b >> 3) & 1)) * 128, n0 = (b >> 4) * 128;
    const int wa = w & 1, wb = w >> 1;
    OpTr<128, D * 2, 2, true> a; a.init(wq, D * D * 2, n0, lds, w, wa, lane);
    OpRow<4> bb; bb.init(x, S * D * 2, s0, lds + a.BYTES, w, wb * 64, lane);
    f32x4 acc[4][4]; zero_acc(acc);
    gemm_simple<4, 2, D / 64, false>(acc, a, bb);
    const int g = lane >> 4, c = lane & 15;
#pragma unroll
    for (int ih = 0; ih < 2; ++ih)
#pragma unroll
        for (int j = 0; j < 4; ++j) {
            const int head = (n0 + wa * 64) / 32 + ih, s = s0 + wb * 64 + j * 16 + c;
            *(u32x4*)(ws + WS_QF + ((head * 64 + (s >> 5)) * 128 + g * 32 + (s & 31)) * 16) = pack8(acc[2 * ih][j] * QSCALE, acc[2 * ih + 1][j] * QSCALE);
        }
}

__device__ __forceinline__ void kv_tile(int t, const void* x, const void* wk, const void* wv, char* ws, lds_ptr lds, int w, int lane) {
    OpKV a; a.init(wk, wv, lds, w, lane);
    OpRow<2> bb; bb.init(x, S * D * 2, t * 128, lds + a.BYTES, w, w * 32, lane);
    f32x4 acc[4][2]; zero_acc(acc);
    gemm_simple<4, 2, D / 64, true>(acc, a, bb);
    const int g = lane >> 4, c = lane & 15;
    char* blk = ws + WS_KV + (t * 4 + w) * 4096;
    float k2 = 0.f, vm = 0.f;
#pragma unroll
    for (int j = 0; j < 2; ++j) {
        const int row = ((c >> 3) << 4) | (j << 3) | (c & 7);
        *(u32x4*)(blk + (g * 32 + row) * 16) = pack8(acc[0][j], acc[1][j]);
        float n = 0.f;
#pragma unroll
        for (int r = 0; r < 4; ++r) n += acc[0][j][r] * acc[0][j][r] + acc[1][j][r] * acc[1][j][r];
        n += __shfl_xor(n, 16); n += __shfl_xor(n, 32);
        k2 = n > k2 || n != n ? n : k2;
    }
#pragma unroll
    for (int i = 0; i < 2; ++i) {
        *(u32x4*)(blk + 2048 + (g * 32 + i * 16 + c) * 16) = pack8(acc[2 + i][0], acc[2 + i][1]);
#pragma unroll
        for (int r = 0; r < 4; ++r) {
            const float a0 = fabsf(acc[2 + i][0][r]), a1 = fabsf(acc[2 + i][1][r]);
            vm = a0 > vm || a0 != a0 ? a0 : vm; vm = a1 > vm || a1 != a1 ? a1 : vm;
        }
    }
    if (!(vm <= SAFE_V) || k2 != k2) k2 = __builtin_inff();
#pragma unroll
    for (int o = 1; o < 64; o <<= 1) k2 = fmaxf(k2, __shfl_xor(k2, o));
    if (lane == 0) ((float*)(ws + WS_BND))[t * 4 + w] = k2;
}

__device__ __forceinline__ void ln_rows(int j, const char* x, const char* gw, char* ws, int w, int lane) {
    u32x4 xv[8][2], gv[2];
#pragma unroll
    for (int e = 0; e < 8; ++e)
#pragma unroll
        for (int h = 0; h < 2; ++h) xv[e][h] = *(const u32x4*)(x + (size_t)(32 * j + 4 * w + (e & 3) + 16 * (e >> 2)) * (D * 2) + h * 1024 + lane * 16);
#pragma unroll
    for (int h = 0; h < 2; ++h) gv[h] = *(const u32x4*)(gw + h * 1024 + lane * 16);
    float mean[8], dot[8], rs[8];
#pragma unroll
    for (int e = 0; e < 8; ++e) {
        float s = 0.f, d = 0.f;
#pragma unroll
        for (int h = 0; h < 2; ++h)
#pragma unroll
            for (int m = 0; m < 4; ++m) {
                const float a = bf_lo(xv[e][h][m]), b = bf_hi(xv[e][h][m]);
                s += a + b; d += a * bf_lo(gv[h][m]) + b * bf_hi(gv[h][m]);
            }
        mean[e] = s; dot[e] = d;
    }
#pragma unroll
    for (int o = 1; o < 64; o <<= 1)
#pragma unroll
        for (int e = 0; e < 8; ++e) { mean[e] += __shfl_xor(mean[e], o); dot[e] += __shfl_xor(dot[e], o); }
#pragma unroll
    for (int e = 0; e < 8; ++e) {
        mean[e] *= 1.f / D;
        float v = 0.f;
#pragma unroll
        for (int h = 0; h < 2; ++h)
#pragma unroll
            for (int m = 0; m < 4; ++m) {
                const float a = bf_lo(xv[e][h][m]) - mean[e], b = bf_hi(xv[e][h][m]) - mean[e];
                v += a * a + b * b;
            }
        rs[e] = v;
    }
#pragma unroll
    for (int o = 1; o < 64; o <<= 1)
#pragma unroll
        for (int e = 0; e < 8; ++e) rs[e] += __shfl_xor(rs[e], o);
    float gsel = 0.f;
#pragma unroll
    for (int e = 0; e < 8; ++e) {
        rs[e] = 1.f / bf_round(sqrtf(bf_round(bf_round(rs[e] * (1.f / D)) + 1e-5f)));
        mean[e] = bf_round(mean[e]);
        gsel = lane == e ? dot[e] : gsel;
    }
    if (lane < 8) {
        const float z = bf_round(gsel);
        ((float*)(ws + WS_G))[32 * j + 4 * w + (lane & 3) + 16 * (lane >> 2)] = bf_round(1.f / (1.f + __expf(-z)));
    }
    char* dst = ws + WS_HF + (size_t)(j * 4 + w) * (D * 16) + lane * 128;
#pragma unroll
    for (int h = 0; h < 2; ++h)
#pragma unroll
        for (int m = 0; m < 4; ++m) {
            float lo[8], hi[8];
#pragma unroll
            for (int e = 0; e < 8; ++e) { lo[e] = (bf_lo(xv[e][h][m]) - mean[e]) * rs[e]; hi[e] = (bf_hi(xv[e][h][m]) - mean[e]) * rs[e]; }
            *(u32x4*)(dst + h * 8192 + m * 32) = u32x4{cvtpk(lo[0], lo[1]), cvtpk(lo[2], lo[3]), cvtpk(lo[4], lo[5]), cvtpk(lo[6], lo[7])};
            *(u32x4*)(dst + h * 8192 + m * 32 + 16) = u32x4{cvtpk(hi[0], hi[1]), cvtpk(hi[2], hi[3]), cvtpk(hi[4], hi[5]), cvtpk(hi[6], hi[7])};
        }
}

__global__ void __launch_bounds__(256, 2) k_pre(const char* __restrict__ x, const char* __restrict__ wq, const char* __restrict__ wk, const char* __restrict__ wv,
                                                const char* __restrict__ gw, char* __restrict__ ws) {
    extern __shared__ __attribute__((aligned(16))) char smem[];
    const int lane = threadIdx.x & 63, w = __builtin_amdgcn_readfirstlane(threadIdx.x >> 6), b = blockIdx.x;
    if (b < PRE_Q) q_tile(b, x, wq, ws, (lds_ptr)smem, w, lane);
    else if (b < PRE_Q + PRE_KV) kv_tile(b - PRE_Q, x, wk, wv, ws, (lds_ptr)smem, w, lane);
    else ln_rows(b - PRE_Q - PRE_KV, x, gw, ws, w, lane);
}

constexpr int MID_ATT = 256, MID_G1 = 224, MID_LDS = 4 * 16384;
constexpr int KV_STAGE = 16384, KV_NSTAGE = 16;

__device__ __forceinline__ void attention(int b, char* ws, lds_ptr lds, int w, int lane) {
    const int head = b >> 3, s0 = (b & 7) * 256 + w * 64;
    const u32x4 srd = make_srd(ws + WS_KV, S * 128);
    const u32 voff = w * 4096 + lane * 16, ldst = (u32)(uintptr_t)lds + w * 4096;
    auto dma = [&](int st) {
#pragma unroll
        for (int p = 0; p < 4; ++p) dma16(ldst + (st & 1) * KV_STAGE + p * 1024, voff, srd, st * KV_STAGE + p * 1024);
    };
    u32x4 q[2][2];
#pragma unroll
    for (int i = 0; i < 2; ++i)
#pragma unroll
        for (int t = 0; t < 2; ++t) q[i][t] = *(const u32x4*)(ws + WS_QF + ((head * 64 + (s0 >> 5) + i) * 128 + t * 64 + lane) * 16);
    float gate[2];
#pragma unroll
    for (int i = 0; i < 2; ++i) gate[i] = ((const float*)(ws + WS_G))[s0 + i * 32 + (lane & 31)];
    float k2 = ((const float*)(ws + WS_BND))[lane];
    float q2 = 0.f;
#pragma unroll
    for (int i = 0; i < 2; ++i) {
        float n = 0.f;
#pragma unroll
        for (int t = 0; t < 2; ++t)
#pragma unroll
            for (int m = 0; m < 4; ++m) { const float a = bf_lo(q[i][t][m]), c = bf_hi(q[i][t][m]); n += a * a + c * c; }
        n += __shfl_xor(n, 32);
        q2 = n > q2 || n != n ? n : q2;
    }
    PIN(gate[0]); PIN(gate[1]);
    dma(0);
#pragma unroll
    for (int o = 1; o < 64; o <<= 1) k2 = fmaxf(k2, __shfl_xor(k2, o));
    const bool safe = __all(q2 * k2 * 1.02f <= SAFE_LOG2 * SAFE_LOG2);

    f32x16 o[2] = {f32x16{}, f32x16{}};
    float lsum[2];
    const lds_ptr base = lds + lane * 16;
    const f32x16 z = {};
    wait_bar<0>();
    dma(1);
    if (safe) {
        f32x4 l[2] = {f32x4{}, f32x4{}};
        const u32 one = (lane & 15) == ((lane >> 4) & 1) ? 0x3f803f80u : 0u;
        const u32x4 ones = {one, one, one, one};
        u32x4 k0 = lds_rd128(base), k1 = lds_rd128(base + 1024), v0 = lds_rd128(base + 2048), v1 = lds_rd128(base + 3072);
        u32x4 pv0 = {}, pv1 = {}, pa = {}, pb = {};
        f32x16 s = M32(k0, q[0][0], z); s = M32(k1, q[0][1], s);
        for (int st = 0; st < KV_NSTAGE; ++st) {
            const lds_ptr cur = base + (st & 1) * KV_STAGE, nxt = base + ((st & 1) ^ 1) * KV_STAGE;
#pragma unroll
            for (int kb = 0; kb < 4; ++kb) {
                if (kb == 3) { wait_bar<0>(); if (st + 2 < KV_NSTAGE) dma(st + 2); }
                const lds_ptr p = kb == 3 ? nxt : cur + (kb + 1) * 4096;
                const u32x4 nk0 = lds_rd128(p), nk1 = lds_rd128(p + 1024), nv0 = lds_rd128(p + 2048), nv1 = lds_rd128(p + 3072);
#pragma unroll
                for (int i = 0; i < 2; ++i) {
                    const int ip = i ^ 1;
                    const u32x4 w0 = i == 0 ? pv0 : v0, w1 = i == 0 ? pv1 : v1, x0 = i == 1 ? nk0 : k0, x1 = i == 1 ? nk1 : k1;
                    float e[16]; u32 c[8];
#define E1(j) e[j] = EX2(s[j]); PIN(e[j]);
#define C1(j) c[j] = cvtpk(e[2 * (j)], e[2 * (j) + 1]); PIN(c[j]);
                    f32x16 n = M32(x0, q[ip][0], z); SB();
                    E1(0) E1(1) E1(2) SB();
                    o[ip] = M32(w0, pa, o[ip]); SB();
                    E1(3) C1(0) E1(4) E1(5) SB();
                    l[ip] = M16(ones, pa, l[ip]); SB();
                    C1(1) E1(6) E1(7) SB();
                    n = M32(x1, q[ip][1], n); SB();
                    C1(2) E1(8) E1(9) C1(3) E1(10) SB();
                    o[ip] = M32(w1, pb, o[ip]); SB();
                    E1(11) C1(4) E1(12) E1(13) C1(5) SB();
                    l[ip] = M16(ones, pb, l[ip]); SB();
                    E1(14) E1(15) C1(6) C1(7) SB();
#undef E1
#undef C1
                    pa = u32x4{c[0], c[1], c[2], c[3]}; pb = u32x4{c[4], c[5], c[6], c[7]}; s = n;
                }
                pv0 = v0; pv1 = v1; k0 = nk0; k1 = nk1; v0 = nv0; v1 = nv1;
            }
        }
        o[1] = M32(pv0, pa, o[1]); o[1] = M32(pv1, pb, o[1]);
        l[1] = M16(ones, pa, l[1]); l[1] = M16(ones, pb, l[1]);
#pragma unroll
        for (int i = 0; i < 2; ++i) { const float a = __shfl(l[i][0], lane & 15), c = __shfl(l[i][1], lane & 15); lsum[i] = (lane & 16) ? c : a; }
    } else {
        float mx[2] = {-__builtin_inff(), -__builtin_inff()};
        lsum[0] = lsum[1] = 0.f;
        for (int st = 0; st < KV_NSTAGE; ++st) {
            const lds_ptr cur = base + (st & 1) * KV_STAGE;
#pragma unroll 1
            for (int kb = 0; kb < 4; ++kb) {
                const lds_ptr p = cur + kb * 4096;
                const u32x4 k0 = lds_rd128(p), k1 = lds_rd128(p + 1024), v0 = lds_rd128(p + 2048), v1 = lds_rd128(p + 3072);
                if (kb == 3) { wait_bar<0>(); if (st + 2 < KV_NSTAGE) dma(st + 2); }
#pragma unroll
                for (int i = 0; i < 2; ++i) {
                    f32x16 s = M32(k0, q[i][0], z); s = M32(k1, q[i][1], s);
                    float m = mx[i];
#pragma unroll
                    for (int r = 0; r < 16; ++r) m = fmaxf(m, s[r]);
                    m = fmaxf(m, __shfl_xor(m, 32));
                    const float alpha = EX2(mx[i] - m);
                    mx[i] = m;
                    float e[16], t = 0.f;
#pragma unroll
                    for (int r = 0; r < 16; ++r) { e[r] = EX2(s[r] - m); t += e[r]; }
                    lsum[i] = lsum[i] * alpha + t;
                    o[i] *= alpha;
                    o[i] = M32(v0, (u32x4{cvtpk(e[0], e[1]), cvtpk(e[2], e[3]), cvtpk(e[4], e[5]), cvtpk(e[6], e[7])}), o[i]);
                    o[i] = M32(v1, (u32x4{cvtpk(e[8], e[9]), cvtpk(e[10], e[11]), cvtpk(e[12], e[13]), cvtpk(e[14], e[15])}), o[i]);
                }
            }
        }
#pragma unroll
        for (int i = 0; i < 2; ++i) lsum[i] += __shfl_xor(lsum[i], 32);
    }
#pragma unroll
    for (int i = 0; i < 2; ++i) {
        const float r = gate[i] / lsum[i];
        char* dst = ws + WS_OF + ((size_t)(head * 4 + (lane >> 5)) * S + s0 + i * 32 + (lane & 31)) * 16;
        const f32x16 v = o[i] * r;
        *(u32x4*)dst = u32x4{cvtpk(v[0], v[1]), cvtpk(v[2], v[3]), cvtpk(v[8], v[9]), cvtpk(v[10], v[11])};
        *(u32x4*)(dst + 2 * S * 16) = u32x4{cvtpk(v[4], v[5]), cvtpk(v[6], v[7]), cvtpk(v[12], v[13]), cvtpk(v[14], v[15])};
    }
}

__device__ __forceinline__ float gelu_tanh(float x) {
    const float y2 = x * (-2.f * 0.7978845608028654f * 1.4426950408889634f) * (1.f + 0.044715f * x * x);
    return x * __builtin_amdgcn_rcpf(1.f + EX2(y2));
}

__device__ __forceinline__ void gemm1_tile(int b, const void* w1, const char* b1, char* ws, lds_ptr lds, int w, int lane) {
    const int x = b & 7, t = b >> 3;
    const int d0 = ((x & 1) * 4 + (t & 3)) * 128, f0 = ((x >> 1) * 7 + (t >> 2)) * 128;
    const int wa = w & 1, wb = w >> 1, g = lane >> 4, c = lane & 15;
    OpTr<128, F * 2, 1, false> a; a.init(w1, S * F * 2, f0, lds, w, wa, lane);
    OpFrag<128, D, 1> bb; bb.init(ws + WS_HF, S * D * 2, d0, lds + a.BYTES, w, wb, lane);
    f32x4 acc[4][4]; zero_acc(acc);
    gemm_simple<4, 1, S / 32, false>(acc, a, bb);
    u32x2 bias[4];
#pragma unroll
    for (int i = 0; i < 4; ++i) bias[i] = *(const u32x2*)(b1 + (f0 + wa * 64 + i * 16 + g * 4) * 2);
#pragma unroll
    for (int ih = 0; ih < 2; ++ih)
#pragma unroll
        for (int j = 0; j < 4; ++j) {
            f32x4 u[2];
#pragma unroll
            for (int h = 0; h < 2; ++h) {
                const u32x2 bv = bias[2 * ih + h];
                const f32x4 v = acc[2 * ih + h][j] + f32x4{bf_lo(bv[0]), bf_hi(bv[0]), bf_lo(bv[1]), bf_hi(bv[1])};
                u[h] = f32x4{gelu_tanh(v[0]), gelu_tanh(v[1]), gelu_tanh(v[2]), gelu_tanh(v[3])};
            }
            *(u32x4*)(ws + WS_UF + ((size_t)(((f0 + wa * 64) / 32 + ih) * 4 + g) * D + d0 + wb * 64 + j * 16 + c) * 16) = pack8(u[0], u[1]);
        }
}

__global__ void __launch_bounds__(256, 2) k_mid(const char* __restrict__ w1, const char* __restrict__ b1, char* __restrict__ ws) {
    extern __shared__ __attribute__((aligned(16))) char smem[];
    const int lane = threadIdx.x & 63, w = __builtin_amdgcn_readfirstlane(threadIdx.x >> 6), b = blockIdx.x;
    if (b < MID_ATT) attention(b, ws, (lds_ptr)smem, w, lane);
    else gemm1_tile(b - MID_ATT, w1, b1, ws, (lds_ptr)smem, w, lane);
}

constexpr int POST_NS = 4, POST_SLOT = 24576, POST_LDS = POST_NS * POST_SLOT;

__global__ void __launch_bounds__(256, 2) k_post(const char* __restrict__ w2, const char* __restrict__ b2, const char* __restrict__ wo,
                                                 char* __restrict__ ws, char* __restrict__ out) {
    extern __shared__ __attribute__((aligned(16))) char smem[];
    const lds_ptr lds = (lds_ptr)smem;
    const int lane = threadIdx.x & 63, w = __builtin_amdgcn_readfirstlane(threadIdx.x >> 6), b = blockIdx.x;
    const int x = b & 7, t = b >> 3;
    const int s0 = ((x >> 1) * 4 + (t & 3)) * 128, d0 = ((x & 1) * 8 + (t >> 2)) * 64;
    const int wa = w & 1, wb = w >> 1, g = lane >> 4, c = lane & 15;
    float omg[4], bs[4];
#pragma unroll
    for (int j = 0; j < 4; ++j) {
        const int s = s0 + wb * 64 + j * 16 + c;
        omg[j] = bf_round(1.f - ((const float*)(ws + WS_G))[s]);
        bs[j] = __uint_as_float((u32)((const unsigned short*)b2)[s] << 16);
        PIN(omg[j]); PIN(bs[j]);
    }
    OpFrag<64, D, 2> a1; a1.init(ws + WS_UF, F * D * 2, d0, lds, w, wa, lane);
    OpTr<128, S * 2, 2, false> b1; b1.init(w2, F * S * 2, s0, lds + a1.BYTES, w, wb, lane);
    OpTr<64, D * 2, 2, false> a2; a2.init(wo, D * D * 2, d0, lds, w, wa, lane);
    OpFrag<128, S, 2> bb2; bb2.init(ws + WS_OF, S * D * 2, s0, lds + a2.BYTES, w, wb, lane);
    constexpr int NS = POST_NS, PPW = 6, R1 = F / 64 / NS, R2 = D / 64 / NS;
    f32x4 acc[2][4]; zero_acc(acc);
    Frags<2, 4> f0, f1;
    const NoIssue none;
    gemm_prologue<NS, POST_SLOT>(f0, a1, b1);
    for (int r = 1; r < R1; ++r) gemm_round<NS, 2, POST_SLOT, PPW, false, false>(acc, f0, f1, a1, b1, a1, b1, a1, b1, r * NS);
    gemm_round<NS, 2, POST_SLOT, PPW, false, false>(acc, f0, f1, a1, b1, a2, bb2, a2, bb2, 0);
#pragma unroll
    for (int i = 0; i < 2; ++i)
#pragma unroll
        for (int j = 0; j < 4; ++j) acc[i][j] = (acc[i][j] + bs[j]) * omg[j];
    SB();
    for (int r = 1; r < R2; ++r) gemm_round<NS, 2, POST_SLOT, PPW, false, false>(acc, f0, f1, a2, bb2, a2, bb2, a2, bb2, r * NS);
    gemm_round<NS, 2, POST_SLOT, PPW, true, false>(acc, f0, f1, a2, bb2, a2, bb2, none, none, 0);
#pragma unroll
    for (int j = 0; j < 4; ++j) {
        const auto r0 = __builtin_amdgcn_permlane16_swap(cvtpk(acc[0][j][0], acc[0][j][1]), cvtpk(acc[1][j][0], acc[1][j][1]), false, false);
        const auto r1 = __builtin_amdgcn_permlane16_swap(cvtpk(acc[0][j][2], acc[0][j][3]), cvtpk(acc[1][j][2], acc[1][j][3]), false, false);
        const int s = s0 + wb * 64 + j * 16 + c, d = d0 + wa * 32 + (g & 1) * 16 + (g >> 1) * 8;
        *(u32x4*)(out + ((size_t)s * D + d) * 2) = u32x4{r0[0], r1[0], r0[1], r1[1]};
    }
}

extern "C" void kernel_launch(void* const* d_in, const int* in_sizes, int n_in, void* d_out, int out_size, void* d_ws, size_t ws_size,
                              hipStream_t stream) {
    const char* x = (const char*)d_in[0];
    const char *wq = (const char*)d_in[1], *wk = (const char*)d_in[2], *wv = (const char*)d_in[3], *wo = (const char*)d_in[4];
    const char *w1 = (const char*)d_in[6], *b1 = (const char*)d_in[7], *w2 = (const char*)d_in[8], *b2 = (const char*)d_in[9];
    const char* gw = (const char*)d_in[10];
    char* ws = (char*)d_ws;
    (void)hipFuncSetAttribute((const void*)k_pre, hipFuncAttributeMaxDynamicSharedMemorySize, PRE_LDS);
    (void)hipFuncSetAttribute((const void*)k_mid, hipFuncAttributeMaxDynamicSharedMemorySize, MID_LDS);
    (void)hipFuncSetAttribute((const void*)k_post, hipFuncAttributeMaxDynamicSharedMemorySize, POST_LDS);
    k_pre<<<PRE_Q + PRE_KV + PRE_LN, 256, PRE_LDS, stream>>>(x, wq, wk, wv, gw, ws);
    k_mid<<<MID_ATT + MID_G1, 256, MID_LDS, stream>>>(w1, b1, ws);
    k_post<<<256, 256, POST_LDS, stream>>>(w2, b2, wo, ws, (char*)d_out);
}
```

```cpp
#include <hip/hip_runtime.h>
#include <stdint.h>

typedef unsigned u32;
typedef u32 u32x4 __attribute__((ext_vector_type(4)));
typedef u32 u32x2 __attribute__((ext_vector_type(2)));
typedef float f32x2 __attribute__((ext_vector_type(2)));
typedef float f32x4 __attribute__((ext_vector_type(4)));
typedef float f32x16 __attribute__((ext_vector_type(16)));
typedef __bf16 bf16x2 __attribute__((ext_vector_type(2)));
typedef __bf16 bf16x8 __attribute__((ext_vector_type(8)));
typedef short s16x4 __attribute__((ext_vector_type(4)));
#define LDS __attribute__((address_space(3)))
typedef LDS char* lds_ptr;

constexpr int S = 2048, D = 1024, F = 3584;
constexpr u32 WS_HF = 0, WS_QF = 4u << 20, WS_OF = 8u << 20, WS_UF = 12u << 20, WS_KV = 20u << 20;
constexpr u32 WS_G = WS_KV + (256u << 10), WS_BND = WS_G + (8u << 10);
constexpr float QSCALE = 0.17677669529663687f * 1.4426950408889634f;
constexpr float SAFE_LOG2 = 60.f;
constexpr float SAFE_V = 1.0e12f;

#define SB() __builtin_amdgcn_sched_barrier(0)
#define PIN(x) asm volatile("" : "+v"(x))
#define M16(a, b, c) __builtin_amdgcn_mfma_f32_16x16x32_bf16(__builtin_bit_cast(bf16x8, a), __builtin_bit_cast(bf16x8, b), c, 0, 0, 0)
#define M32(a, b, c) __builtin_amdgcn_mfma_f32_32x32x16_bf16(__builtin_bit_cast(bf16x8, a), __builtin_bit_cast(bf16x8, b), c, 0, 0, 0)
#define EX2(x) __builtin_amdgcn_exp2f(x)

__device__ __forceinline__ u32 cvtpk(float lo, float hi) { f32x2 v = {lo, hi}; return __builtin_bit_cast(u32, __builtin_convertvector(v, bf16x2)); }
__device__ __forceinline__ float bf_lo(u32 v) { return __uint_as_float(v << 16); }
__device__ __forceinline__ float bf_hi(u32 v) { return __uint_as_float(v & 0xffff0000u); }
__device__ __forceinline__ float bf_round(float x) { return bf_lo(cvtpk(x, x)); }
__device__ __forceinline__ u32x4 pack8(const f32x4& a, const f32x4& b) { return u32x4{cvtpk(a[0], a[1]), cvtpk(a[2], a[3]), cvtpk(b[0], b[1]), cvtpk(b[2], b[3])}; }

__device__ __forceinline__ u32x4 make_srd(const void* p, u32 bytes) {
    const uint64_t a = (uint64_t)p;
    return u32x4{(u32)a, (u32)(a >> 32) & 0xffffu, bytes, 0x00020000u};
}
__device__ __forceinline__ void dma16(u32 lds, u32 voff, const u32x4& srd, u32 soff) {
    asm volatile("s_mov_b32 m0, %0\n\ts_nop 0\n\tbuffer_load_dwordx4 %1, %2, %3 offen lds" ::"s"(lds), "v"(voff), "s"(srd), "s"(soff) : "memory");
}
template <int N> __device__ __forceinline__ void wait_bar() { asm volatile("s_waitcnt vmcnt(%0) lgkmcnt(0)\n\ts_barrier" ::"n"(N) : "memory"); }
__device__ __forceinline__ u32x4 lds_rd128(lds_ptr p) { return *(LDS const u32x4*)p; }
__device__ __forceinline__ u32x2 lds_rdtr(lds_ptr p) { return __builtin_bit_cast(u32x2, __builtin_amdgcn_ds_read_tr16_b64_v4i16((LDS s16x4*)p)); }

#define STAMP(k, i)

template <int R, int NR, int KS> struct OpFrag {
    static constexpr int BYTES = KS * 4 * R * 16, PPW = BYTES / 4096, NF = R / 32, RPF = 1;
    u32x4 srd; u32 voff, ldst; lds_ptr rd;
    __device__ __forceinline__ void init(const void* base, u32 bytes, int row0, lds_ptr img, int w, int wsel, int lane) {
        srd = make_srd(base, bytes);
        voff = (u32)(row0 + lane + w * NR) * 16u;
        ldst = (u32)(uintptr_t)img + w * (R * 16);
        rd = img + ((lane >> 4) * R + wsel * (R / 2) + (lane & 15)) * 16;
    }
    __device__ __forceinline__ void issue(int slot, u32 pos, int i) const {
        constexpr int HB = R / 64;
        const int ks = i / HB, half = i % HB;
        dma16(ldst + pos + ks * (4 * R * 16) + half * 1024, voff, srd, (u32)(slot * KS + ks) * (4u * NR * 16u) + half * 1024u);
    }
    __device__ __forceinline__ void read(u32x4& f, int pos, int ks, int i, int) const { f = lds_rd128(rd + pos + ks * (4 * R * 16) + i * 256); }
};

template <int C, int RS, int KS, bool NAT> struct OpTr {
    static constexpr int BYTES = KS * 32 * C * 2, PPW = BYTES / 4096, NF = C / 32, RPF = 2;
    u32x4 srd; u32 voff, ldst; lds_ptr rd[NF];
    __device__ __forceinline__ void init(const void* base, u32 bytes, int col0, lds_ptr img, int w, int wsel, int lane) {
        srd = make_srd(base, bytes);
        ldst = (u32)(uintptr_t)img + w * 1024;
        const int g = lane >> 4, q = (lane >> 2) & 3, p = lane & 3;
        if constexpr (C == 128) {
            const int f = NAT ? (g | (((w >> 1) & 1) << 2)) : (4 * (w & 1) + g);
            voff = (u32)(4 * w + g) * RS + col0 * 2 + ((((lane & 15) >> 1) ^ f) * 32) + (lane & 1) * 16;
            const int row = NAT ? 8 * g + q : 4 * g + q, fr = NAT ? (q | ((g & 1) << 2)) : (row & 7);
#pragma unroll
            for (int i = 0; i < NF; ++i) rd[i] = img + row * 256 + (((wsel * NF + i) ^ fr) * 32) + p * 8;
        } else {
            static_assert(C == 128 || (C == 64 && !NAT), "unsupported transposed operand");
            voff = (u32)(8 * w + (lane >> 3)) * RS + col0 * 2 + ((((lane & 7) >> 1) ^ g) * 32) + (lane & 1) * 16;
            const int row = 4 * g + q;
#pragma unroll
            for (int i = 0; i < NF; ++i) rd[i] = img + row * 128 + (((wsel * NF + i) ^ ((row >> 1) & 3)) * 32) + p * 8;
        }
    }
    __device__ __forceinline__ void issue(int slot, u32 pos, int i) const {
        constexpr int ROWS = C == 128 ? 16 : 32;
        dma16(ldst + pos + i * 4096, voff, srd, (u32)(slot * KS * 32 + i * ROWS) * RS);
    }
    __device__ __forceinline__ void read(u32x4& f, int pos, int ks, int i, int t) const {
        const u32x2 v = lds_rdtr(rd[i] + pos + ks * (32 * C * 2) + t * ((NAT ? 4 : 16) * C * 2));
        f[2 * t] = v[0]; f[2 * t + 1] = v[1];
    }
};

template <int NFW> struct OpRow {
    static constexpr int BYTES = 128 * 128, PPW = 4, NF = NFW, RPF = 1;
    u32x4 srd; u32 voff, ldst; lds_ptr rd[2];
    __device__ __forceinline__ void init(const void* base, u32 bytes, int row0, lds_ptr img, int w, int wrow, int lane) {
        srd = make_srd(base, bytes);
        ldst = (u32)(uintptr_t)img + w * 1024;
        const int g = lane >> 4, c = lane & 15;
        voff = (u32)(row0 + 8 * w + (lane >> 3)) * (D * 2) + (((lane & 7) ^ (4 * (w & 1) + g)) * 16);
#pragma unroll
        for (int ks = 0; ks < 2; ++ks) rd[ks] = img + (wrow + c) * 128 + (((4 * ks + g) ^ (c >> 1)) * 16);
    }
    __device__ __forceinline__ void issue(int slot, u32 pos, int i) const { dma16(ldst + pos + i * 4096, voff, srd, (u32)slot * 128u + i * (32u * D * 2u)); }
    __device__ __forceinline__ void read(u32x4& f, int pos, int ks, int i, int) const { f = lds_rd128(rd[ks] + pos + i * 2048); }
};

struct OpKV {
    static constexpr int BYTES = 8192, PPW = 2, NF = 4, RPF = 2;
    u32x4 srdk, srdv; u32 voff, ldst; lds_ptr rd[2];
    __device__ __forceinline__ void init(const void* wk, const void* wv, lds_ptr img, int w, int lane) {
        srdk = make_srd(wk, D * 64); srdv = make_srd(wv, D * 64);
        ldst = (u32)(uintptr_t)img + w * 1024;
        voff = (u32)(16 * w + (lane >> 2)) * 64 + ((((lane >> 1) & 1) ^ (lane >> 5)) * 32) + (lane & 1) * 16;
        const int g = lane >> 4, q = (lane >> 2) & 3, p = lane & 3;
#pragma unroll
        for (int ch = 0; ch < 2; ++ch) rd[ch] = img + (8 * g + q) * 64 + ((ch ^ (g & 1)) * 32) + p * 8;
    }
    __device__ __forceinline__ void issue(int slot, u32 pos, int i) const { dma16(ldst + pos + i * 4096, voff, i ? srdv : srdk, (u32)slot * 4096u); }
    __device__ __forceinline__ void read(u32x4& f, int pos, int ks, int i, int t) const {
        const u32x2 v = lds_rdtr(rd[i & 1] + pos + (i >> 1) * 4096 + ks * 2048 + t * 256);
        f[2 * t] = v[0]; f[2 * t + 1] = v[1];
    }
};

struct NoIssue { static constexpr int PPW = 0; __device__ __forceinline__ void issue(int, u32, int) const {} };

template <int NA, int NB> struct Frags { u32x4 a[NA], b[NB]; };

template <bool SWAPHI, int NA, int NB, class RA, class RB, class IA, class IB>
__device__ __forceinline__ void gemm_step(f32x4 (&acc)[NA][NB], const Frags<NA, NB>& cur, Frags<NA, NB>& nxt, bool do_read, const RA& ra, const RB& rb,
                                          int rpos, int rks, bool do_issue, const IA& ia, const IB& ib, int islot, u32 ipos) {
    constexpr int NM = NA * NB, NRA = NA * RA::RPF, NRD = NRA + NB * RB::RPF, ND = IA::PPW + IB::PPW;
    constexpr int RPM = (NRD + NM - 1) / NM, DPM = (ND + NM - 1) / NM, DSTEP = ND >= NM || ND == 0 ? 1 : NM / ND;
#pragma unroll
    for (int m = 0; m < NM; ++m) {
        const int i = m % NA, j = m / NA;
        if (SWAPHI && i >= NA / 2) acc[i][j] = M16(cur.b[j], cur.a[i], acc[i][j]);
        else acc[i][j] = M16(cur.a[i], cur.b[j], acc[i][j]);
        if (do_read) {
#pragma unroll
            for (int r = m * RPM; r < (m + 1) * RPM && r < NRD; ++r) {
                if (r < RB::RPF) rb.read(nxt.b[0], rpos, rks, 0, r);
                else if (r < RB::RPF + NRA) ra.read(nxt.a[(r - RB::RPF) / RA::RPF], rpos, rks, (r - RB::RPF) / RA::RPF, (r - RB::RPF) % RA::RPF);
                else rb.read(nxt.b[(r - NRA) / RB::RPF], rpos, rks, (r - NRA) / RB::RPF, (r - NRA) % RB::RPF);
            }
        }
        if (do_issue && m % DSTEP == 0) {
#pragma unroll
            for (int d = (m / DSTEP) * DPM; d < (m / DSTEP + 1) * DPM && d < ND; ++d) {
                if (d < IA::PPW) ia.issue(islot, ipos, d); else ib.issue(islot, ipos, d - IA::PPW);
            }
        }
        SB();
    }
}

template <int NS, int KS, int SLOTB, int PPW, bool LAST, bool SWAPHI, int NA, int NB, class CA, class CB, class XA, class XB, class IA, class IB>
__device__ __forceinline__ void gemm_round(f32x4 (&acc)[NA][NB], Frags<NA, NB>& f0, Frags<NA, NB>& f1, const CA& ca, const CB& cb, const XA& na, const XB& nb,
                                           const IA& ia, const IB& ib, int islot0) {
    static_assert((NS * KS) % 2 == 0, "fragment buffers alternate");
    constexpr bool ISSUE = IA::PPW + IB::PPW > 0;
#pragma unroll
    for (int p = 0; p < NS; ++p) {
#pragma unroll
        for (int ks = 0; ks < KS; ++ks) {
            Frags<NA, NB>& cur = ((p * KS + ks) & 1) ? f1 : f0;
            Frags<NA, NB>& nxt = ((p * KS + ks) & 1) ? f0 : f1;
            if (ks + 1 < KS) {
                gemm_step<SWAPHI>(acc, cur, nxt, true, ca, cb, p * SLOTB, ks + 1, false, ia, ib, 0, 0);
            } else if (LAST && p == NS - 1) {
                gemm_step<SWAPHI>(acc, cur, nxt, false, ca, cb, 0, 0, false, ia, ib, 0, 0);
            } else {
                if (ISSUE) wait_bar<(NS - 2) * PPW>();
                else if (p == 0) wait_bar<(NS - 2) * PPW>();
                else if (p == 1) wait_bar<(NS > 3 ? NS - 3 : 0) * PPW>();
                else if (p == 2) wait_bar<(NS > 4 ? NS - 4 : 0) * PPW>();
                else if (p == 3) wait_bar<(NS > 5 ? NS - 5 : 0) * PPW>();
                else if (p == 4) wait_bar<(NS > 6 ? NS - 6 : 0) * PPW>();
                else if (p == 5) wait_bar<(NS > 7 ? NS - 7 : 0) * PPW>();
                else wait_bar<0>();
                if (p == NS - 1) gemm_step<SWAPHI>(acc, cur, nxt, true, na, nb, 0, 0, ISSUE, ia, ib, islot0 + p, p * SLOTB);
                else gemm_step<SWAPHI>(acc, cur, nxt, true, ca, cb, (p + 1) * SLOTB, 0, ISSUE, ia, ib, islot0 + p, p * SLOTB);
            }
        }
    }
}

template <int NS, int SLOTB, int NA, int NB, class A, class B>
__device__ __forceinline__ void gemm_prologue(Frags<NA, NB>& f0, const A& a, const B& b) {
#pragma unroll
    for (int p = 0; p < NS; ++p) {
#pragma unroll
        for (int i = 0; i < A::PPW; ++i) a.issue(p, p * SLOTB, i);
#pragma unroll
        for (int i = 0; i < B::PPW; ++i) b.issue(p, p * SLOTB, i);
    }
    wait_bar<(NS - 1) * (A::PPW + B::PPW)>();
#pragma unroll
    for (int i = 0; i < NA * A::RPF; ++i) a.read(f0.a[i / A::RPF], 0, 0, i / A::RPF, i % A::RPF);
#pragma unroll
    for (int i = 0; i < NB * B::RPF; ++i) b.read(f0.b[i / B::RPF], 0, 0, i / B::RPF, i % B::RPF);
    SB();
}

template <int NS, int KS, int NSLOTS, bool SWAPHI, int NA, int NB, class A, class B>
__device__ __forceinline__ void gemm_simple(f32x4 (&acc)[NA][NB], const A& a, const B& b) {
    constexpr int SLOTB = A::BYTES + B::BYTES, PPW = A::PPW + B::PPW;
    static_assert(NSLOTS % NS == 0 && NS <= 8, "ring shape");
    Frags<NA, NB> f0, f1;
    const NoIssue none;
    gemm_prologue<NS, SLOTB>(f0, a, b);
    for (int r = 1; r < NSLOTS / NS; ++r) gemm_round<NS, KS, SLOTB, PPW, false, SWAPHI>(acc, f0, f1, a, b, a, b, a, b, r * NS);
    gemm_round<NS, KS, SLOTB, PPW, true, SWAPHI>(acc, f0, f1, a, b, a, b, none, none, 0);
}

template <int NA, int NB> __device__ __forceinline__ void zero_acc(f32x4 (&acc)[NA][NB]) {
#pragma unroll
    for (int i = 0; i < NA; ++i)
#pragma unroll
        for (int j = 0; j < NB; ++j) acc[i][j] = f32x4{};
}

constexpr int PRE_Q = 128, PRE_KV = 16, PRE_LN = 64, PRE_LDS = 4 * 32768;

__device__ __forceinline__ void q_tile(int b, const void* x, const void* wq, char* ws, lds_ptr lds, int w, int lane) {
    const int s0 = ((b & 7) * 2 + ((b >> 3) & 1)) * 128, n0 = (b >> 4) * 128;
    const int wa = w & 1, wb = w >> 1;
    OpTr<128, D * 2, 2, true> a; a.init(wq, D * D * 2, n0, lds, w, wa, lane);
    OpRow<4> bb; bb.init(x, S * D * 2, s0, lds + a.BYTES, w, wb * 64, lane);
    f32x4 acc[4][4]; zero_acc(acc);
    gemm_simple<4, 2, D / 64, false>(acc, a, bb);
    STAMP(0, 6);
    const int g = lane >> 4, c = lane & 15;
#pragma unroll
    for (int ih = 0; ih < 2; ++ih)
#pragma unroll
        for (int j = 0; j < 4; ++j) {
            const int head = (n0 + wa * 64) / 32 + ih, s = s0 + wb * 64 + j * 16 + c;
            *(u32x4*)(ws + WS_QF + ((head * 64 + (s >> 5)) * 128 + g * 32 + (s & 31)) * 16) = pack8(acc[2 * ih][j] * QSCALE, acc[2 * ih + 1][j] * QSCALE);
        }
}

__device__ __forceinline__ void kv_tile(int t, const void* x, const void* wk, const void* wv, char* ws, lds_ptr lds, int w, int lane) {
    OpKV a; a.init(wk, wv, lds, w, lane);
    OpRow<2> bb; bb.init(x, S * D * 2, t * 128, lds + a.BYTES, w, w * 32, lane);
    f32x4 acc[4][2]; zero_acc(acc);
    gemm_simple<4, 2, D / 64, true>(acc, a, bb);
    STAMP(0, 6);
    const int g = lane >> 4, c = lane & 15;
    char* blk = ws + WS_KV + (t * 4 + w) * 4096;
    float k2 = 0.f, vm = 0.f;
#pragma unroll
    for (int j = 0; j < 2; ++j) {
        const int row = ((c >> 3) << 4) | (j << 3) | (c & 7);
        *(u32x4*)(blk + (g * 32 + row) * 16) = pack8(acc[0][j], acc[1][j]);
        float n = 0.f;
#pragma unroll
        for (int r = 0; r < 4; ++r) n += acc[0][j][r] * acc[0][j][r] + acc[1][j][r] * acc[1][j][r];
        n += __shfl_xor(n, 16); n += __shfl_xor(n, 32);
        k2 = n > k2 || n != n ? n : k2;
    }
#pragma unroll
    for (int i = 0; i < 2; ++i) {
        *(u32x4*)(blk + 2048 + (g * 32 + i * 16 + c) * 16) = pack8(acc[2 + i][0], acc[2 + i][1]);
#pragma unroll
        for (int r = 0; r < 4; ++r) {
            const float a0 = fabsf(acc[2 + i][0][r]), a1 = fabsf(acc[2 + i][1][r]);
            vm = a0 > vm || a0 != a0 ? a0 : vm; vm = a1 > vm || a1 != a1 ? a1 : vm;
        }
    }
    if (!(vm <= SAFE_V) || k2 != k2) k2 = __builtin_inff();
#pragma unroll
    for (int o = 1; o < 64; o <<= 1) k2 = fmaxf(k2, __shfl_xor(k2, o));
    if (lane == 0) ((float*)(ws + WS_BND))[t * 4 + w] = k2;
}

__device__ __forceinline__ void ln_rows(int j, const char* x, const char* gw, char* ws, int w, int lane) {
    u32x4 xv[8][2], gv[2];
#pragma unroll
    for (int e = 0; e < 8; ++e)
#pragma unroll
        for (int h = 0; h < 2; ++h) xv[e][h] = *(const u32x4*)(x + (size_t)(32 * j + 4 * w + (e & 3) + 16 * (e >> 2)) * (D * 2) + h * 1024 + lane * 16);
#pragma unroll
    for (int h = 0; h < 2; ++h) gv[h] = *(const u32x4*)(gw + h * 1024 + lane * 16);
    float mean[8], dot[8], rs[8];
#pragma unroll
    for (int e = 0; e < 8; ++e) {
        float s = 0.f, d = 0.f;
#pragma unroll
        for (int h = 0; h < 2; ++h)
#pragma unroll
            for (int m = 0; m < 4; ++m) {
                const float a = bf_lo(xv[e][h][m]), b = bf_hi(xv[e][h][m]);
                s += a + b; d += a * bf_lo(gv[h][m]) + b * bf_hi(gv[h][m]);
            }
        mean[e] = s; dot[e] = d;
    }
#pragma unroll
    for (int o = 1; o < 64; o <<= 1)
#pragma unroll
        for (int e = 0; e < 8; ++e) { mean[e] += __shfl_xor(mean[e], o); dot[e] += __shfl_xor(dot[e], o); }
#pragma unroll
    for (int e = 0; e < 8; ++e) {
        mean[e] *= 1.f / D;
        float v = 0.f;
#pragma unroll
        for (int h = 0; h < 2; ++h)
#pragma unroll
            for (int m = 0; m < 4; ++m) {
                const float a = bf_lo(xv[e][h][m]) - mean[e], b = bf_hi(xv[e][h][m]) - mean[e];
                v += a * a + b * b;
            }
        rs[e] = v;
    }
#pragma unroll
    for (int o = 1; o < 64; o <<= 1)
#pragma unroll
        for (int e = 0; e < 8; ++e) rs[e] += __shfl_xor(rs[e], o);
    STAMP(0, 6);
    float gsel = 0.f;
#pragma unroll
    for (int e = 0; e < 8; ++e) {
        rs[e] = 1.f / bf_round(sqrtf(bf_round(bf_round(rs[e] * (1.f / D)) + 1e-5f)));
        mean[e] = bf_round(mean[e]);
        gsel = lane == e ? dot[e] : gsel;
    }
    if (lane < 8) {
        const float z = bf_round(gsel);
        ((float*)(ws + WS_G))[32 * j + 4 * w + (lane & 3) + 16 * (lane >> 2)] = bf_round(1.f / (1.f + __expf(-z)));
    }
    char* dst = ws + WS_HF + (size_t)(j * 4 + w) * (D * 16) + lane * 128;
#pragma unroll
    for (int h = 0; h < 2; ++h)
#pragma unroll
        for (int m = 0; m < 4; ++m) {
            float lo[8], hi[8];
#pragma unroll
            for (int e = 0; e < 8; ++e) { lo[e] = (bf_lo(xv[e][h][m]) - mean[e]) * rs[e]; hi[e] = (bf_hi(xv[e][h][m]) - mean[e]) * rs[e]; }
            *(u32x4*)(dst + h * 8192 + m * 32) = u32x4{cvtpk(lo[0], lo[1]), cvtpk(lo[2], lo[3]), cvtpk(lo[4], lo[5]), cvtpk(lo[6], lo[7])};
            *(u32x4*)(dst + h * 8192 + m * 32 + 16) = u32x4{cvtpk(hi[0], hi[1]), cvtpk(hi[2], hi[3]), cvtpk(hi[4], hi[5]), cvtpk(hi[6], hi[7])};
        }
}

__global__ void __launch_bounds__(256, 2) k_pre(const char* __restrict__ x, const char* __restrict__ wq, const char* __restrict__ wk, const char* __restrict__ wv,
                                                const char* __restrict__ gw, char* __restrict__ ws) {
    extern __shared__ __attribute__((aligned(16))) char smem[];
    const int lane = threadIdx.x & 63, w = __builtin_amdgcn_readfirstlane(threadIdx.x >> 6), b = blockIdx.x;
    STAMP(0, 0);
    if (b < PRE_Q) q_tile(b, x, wq, ws, (lds_ptr)smem, w, lane);
    else if (b < PRE_Q + PRE_KV) kv_tile(b - PRE_Q, x, wk, wv, ws, (lds_ptr)smem, w, lane);
    else ln_rows(b - PRE_Q - PRE_KV, x, gw, ws, w, lane);
    STAMP(0, 7);
}

constexpr int MID_ATT = 256, MID_G1 = 224, MID_LDS = 4 * 16384;
constexpr int KV_STAGE = 16384, KV_NSTAGE = 16;

__device__ __forceinline__ void attention(int b, char* ws, lds_ptr lds, int w, int lane) {
    const int head = b >> 3, s0 = (b & 7) * 256 + w * 64;
    const u32x4 srd = make_srd(ws + WS_KV, S * 128);
    const u32 voff = w * 4096 + lane * 16, ldst = (u32)(uintptr_t)lds + w * 4096;
    auto dma = [&](int st) {
#pragma unroll
        for (int p = 0; p < 4; ++p) dma16(ldst + (st & 1) * KV_STAGE + p * 1024, voff, srd, st * KV_STAGE + p * 1024);
    };
    u32x4 q[2][2];
#pragma unroll
    for (int i = 0; i < 2; ++i)
#pragma unroll
        for (int t = 0; t < 2; ++t) q[i][t] = *(const u32x4*)(ws + WS_QF + ((head * 64 + (s0 >> 5) + i) * 128 + t * 64 + lane) * 16);
    float gate[2];
#pragma unroll
    for (int i = 0; i < 2; ++i) gate[i] = ((const float*)(ws + WS_G))[s0 + i * 32 + (lane & 31)];
    float k2 = ((const float*)(ws + WS_BND))[lane];
    float q2 = 0.f;
#pragma unroll
    for (int i = 0; i < 2; ++i) {
        float n = 0.f;
#pragma unroll
        for (int t = 0; t < 2; ++t)
#pragma unroll
            for (int m = 0; m < 4; ++m) { const float a = bf_lo(q[i][t][m]), c = bf_hi(q[i][t][m]); n += a * a + c * c; }
        n += __shfl_xor(n, 32);
        q2 = n > q2 || n != n ? n : q2;
    }
    PIN(gate[0]); PIN(gate[1]);
    dma(0);
#pragma unroll
    for (int o = 1; o < 64; o <<= 1) k2 = fmaxf(k2, __shfl_xor(k2, o));
    const bool safe = __all(q2 * k2 * 1.02f <= SAFE_LOG2 * SAFE_LOG2);

    f32x16 o[2] = {f32x16{}, f32x16{}};
    float lsum[2];
    const lds_ptr base = lds + lane * 16;
    const f32x16 z = {};
    wait_bar<0>();
    STAMP(1, 1);
    dma(1);
    if (safe) {
        f32x4 l[2] = {f32x4{}, f32x4{}};
        const u32 one = (lane & 15) == ((lane >> 4) & 1) ? 0x3f803f80u : 0u;
        const u32x4 ones = {one, one, one, one};
        u32x4 k0 = lds_rd128(base), k1 = lds_rd128(base + 1024), v0 = lds_rd128(base + 2048), v1 = lds_rd128(base + 3072);
        u32x4 pv0 = {}, pv1 = {}, pa = {}, pb = {};
        f32x16 s = M32(k0, q[0][0], z); s = M32(k1, q[0][1], s);
        for (int st = 0; st < KV_NSTAGE; ++st) {
            const lds_ptr cur = base + (st & 1) * KV_STAGE, nxt = base + ((st & 1) ^ 1) * KV_STAGE;
#pragma unroll
            for (int kb = 0; kb < 4; ++kb) {
                if (kb == 3) { wait_bar<0>(); if (st + 2 < KV_NSTAGE) dma(st + 2); }
                const lds_ptr p = kb == 3 ? nxt : cur + (kb + 1) * 4096;
                const u32x4 nk0 = lds_rd128(p), nk1 = lds_rd128(p + 1024), nv0 = lds_rd128(p + 2048), nv1 = lds_rd128(p + 3072);
#pragma unroll
                for (int i = 0; i < 2; ++i) {
                    const int ip = i ^ 1;
                    const u32x4 w0 = i == 0 ? pv0 : v0, w1 = i == 0 ? pv1 : v1, x0 = i == 1 ? nk0 : k0, x1 = i == 1 ? nk1 : k1;
                    float e[16]; u32 c[8];
#define E1(j) e[j] = EX2(s[j]); PIN(e[j]);
#define C1(j) c[j] = cvtpk(e[2 * (j)], e[2 * (j) + 1]); PIN(c[j]);
                    f32x16 n = M32(x0, q[ip][0], z); SB();
                    E1(0) E1(1) E1(2) SB();
                    o[ip] = M32(w0, pa, o[ip]); SB();
                    E1(3) C1(0) E1(4) E1(5) SB();
                    l[ip] = M16(ones, pa, l[ip]); SB();
                    C1(1) E1(6) E1(7) SB();
                    n = M32(x1, q[ip][1], n); SB();
                    C1(2) E1(8) E1(9) C1(3) E1(10) SB();
                    o[ip] = M32(w1, pb, o[ip]); SB();
                    E1(11) C1(4) E1(12) E1(13) C1(5) SB();
                    l[ip] = M16(ones, pb, l[ip]); SB();
                    E1(14) E1(15) C1(6) C1(7) SB();
#undef E1
#undef C1
                    pa = u32x4{c[0], c[1], c[2], c[3]}; pb = u32x4{c[4], c[5], c[6], c[7]}; s = n;
                }
                pv0 = v0; pv1 = v1; k0 = nk0; k1 = nk1; v0 = nv0; v1 = nv1;
            }
        }
        o[1] = M32(pv0, pa, o[1]); o[1] = M32(pv1, pb, o[1]);
        l[1] = M16(ones, pa, l[1]); l[1] = M16(ones, pb, l[1]);
#pragma unroll
        for (int i = 0; i < 2; ++i) { const float a = __shfl(l[i][0], lane & 15), c = __shfl(l[i][1], lane & 15); lsum[i] = (lane & 16) ? c : a; }
    } else {
        float mx[2] = {-__builtin_inff(), -__builtin_inff()};
        lsum[0] = lsum[1] = 0.f;
        for (int st = 0; st < KV_NSTAGE; ++st) {
            const lds_ptr cur = base + (st & 1) * KV_STAGE;
#pragma unroll 1
            for (int kb = 0; kb < 4; ++kb) {
                const lds_ptr p = cur + kb * 4096;
                const u32x4 k0 = lds_rd128(p), k1 = lds_rd128(p + 1024), v0 = lds_rd128(p + 2048), v1 = lds_rd128(p + 3072);
                if (kb == 3) { wait_bar<0>(); if (st + 2 < KV_NSTAGE) dma(st + 2); }
#pragma unroll
                for (int i = 0; i < 2; ++i) {
                    f32x16 s = M32(k0, q[i][0], z); s = M32(k1, q[i][1], s);
                    float m = mx[i];
#pragma unroll
                    for (int r = 0; r < 16; ++r) m = fmaxf(m, s[r]);
                    m = fmaxf(m, __shfl_xor(m, 32));
                    const float alpha = EX2(mx[i] - m);
                    mx[i] = m;
                    float e[16], t = 0.f;
#pragma unroll
                    for (int r = 0; r < 16; ++r) { e[r] = EX2(s[r] - m); t += e[r]; }
                    lsum[i] = lsum[i] * alpha + t;
                    o[i] *= alpha;
                    o[i] = M32(v0, (u32x4{cvtpk(e[0], e[1]), cvtpk(e[2], e[3]), cvtpk(e[4], e[5]), cvtpk(e[6], e[7])}), o[i]);
                    o[i] = M32(v1, (u32x4{cvtpk(e[8], e[9]), cvtpk(e[10], e[11]), cvtpk(e[12], e[13]), cvtpk(e[14], e[15])}), o[i]);
                }
            }
        }
#pragma unroll
        for (int i = 0; i < 2; ++i) lsum[i] += __shfl_xor(lsum[i], 32);
    }
    STAMP(1, 6);
#pragma unroll
    for (int i = 0; i < 2; ++i) {
        const float r = gate[i] / lsum[i];
        char* dst = ws + WS_OF + ((size_t)(head * 4 + (lane >> 5)) * S + s0 + i * 32 + (lane & 31)) * 16;
        const f32x16 v = o[i] * r;
        *(u32x4*)dst = u32x4{cvtpk(v[0], v[1]), cvtpk(v[2], v[3]), cvtpk(v[8], v[9]), cvtpk(v[10], v[11])};
        *(u32x4*)(dst + 2 * S * 16) = u32x4{cvtpk(v[4], v[5]), cvtpk(v[6], v[7]), cvtpk(v[12], v[13]), cvtpk(v[14], v[15])};
    }
}

__device__ __forceinline__ float gelu_tanh(float x) {
    const float y2 = x * (-2.f * 0.7978845608028654f * 1.4426950408889634f) * (1.f + 0.044715f * x * x);
    return x * __builtin_amdgcn_rcpf(1.f + EX2(y2));
}

__device__ __forceinline__ void gemm1_tile(int b, const void* w1, const char* b1, char* ws, lds_ptr lds, int w, int lane) {
    const int x = b & 7, t = b >> 3;
    const int d0 = ((x & 1) * 4 + (t & 3)) * 128, f0 = ((x >> 1) * 7 + (t >> 2)) * 128;
    const int wa = w & 1, wb = w >> 1, g = lane >> 4, c = lane & 15;
    OpTr<128, F * 2, 1, false> a; a.init(w1, S * F * 2, f0, lds, w, wa, lane);
    OpFrag<128, D, 1> bb; bb.init(ws + WS_HF, S * D * 2, d0, lds + a.BYTES, w, wb, lane);
    f32x4 acc[4][4]; zero_acc(acc);
    gemm_simple<4, 1, S / 32, false>(acc, a, bb);
    STAMP(1, 6);
    u32x2 bias[4];
#pragma unroll
    for (int i = 0; i < 4; ++i) bias[i] = *(const u32x2*)(b1 + (f0 + wa * 64 + i * 16 + g * 4) * 2);
#pragma unroll
    for (int ih = 0; ih < 2; ++ih)
#pragma unroll
        for (int j = 0; j < 4; ++j) {
            f32x4 u[2];
#pragma unroll
            for (int h = 0; h < 2; ++h) {
                const u32x2 bv = bias[2 * ih + h];
                const f32x4 v = acc[2 * ih + h][j] + f32x4{bf_lo(bv[0]), bf_hi(bv[0]), bf_lo(bv[1]), bf_hi(bv[1])};
                u[h] = f32x4{gelu_tanh(v[0]), gelu_tanh(v[1]), gelu_tanh(v[2]), gelu_tanh(v[3])};
            }
            *(u32x4*)(ws + WS_UF + ((size_t)(((f0 + wa * 64) / 32 + ih) * 4 + g) * D + d0 + wb * 64 + j * 16 + c) * 16) = pack8(u[0], u[1]);
        }
}

__global__ void __launch_bounds__(256, 2) k_mid(const char* __restrict__ w1, const char* __restrict__ b1, char* __restrict__ ws) {
    extern __shared__ __attribute__((aligned(16))) char smem[];
    const int lane = threadIdx.x & 63, w = __builtin_amdgcn_readfirstlane(threadIdx.x >> 6), b = blockIdx.x;
    STAMP(1, 0);
    if (b < MID_ATT) attention(b, ws, (lds_ptr)smem, w, lane);
    else gemm1_tile(b - MID_ATT, w1, b1, ws, (lds_ptr)smem, w, lane);
    STAMP(1, 7);
}

#define POST_NS_ 4
constexpr int POST_NS = POST_NS_, POST_SLOT = 24576, POST_LDS = POST_NS * POST_SLOT;

__global__ void __launch_bounds__(256, 2) k_post(const char* __restrict__ w2, const char* __restrict__ b2, const char* __restrict__ wo,
                                                 char* __restrict__ ws, char* __restrict__ out) {
    extern __shared__ __attribute__((aligned(16))) char smem[];
    const lds_ptr lds = (lds_ptr)smem;
    const int lane = threadIdx.x & 63, w = __builtin_amdgcn_readfirstlane(threadIdx.x >> 6), b = blockIdx.x;
    const int x = b & 7, t = b >> 3;
    const int s0 = ((x >> 1) * 4 + (t & 3)) * 128, d0 = ((x & 1) * 8 + (t >> 2)) * 64;
    const int wa = w & 1, wb = w >> 1, g = lane >> 4, c = lane & 15;
    STAMP(2, 0);
    float omg[4], bs[4];
#pragma unroll
    for (int j = 0; j < 4; ++j) {
        const int s = s0 + wb * 64 + j * 16 + c;
        omg[j] = bf_round(1.f - ((const float*)(ws + WS_G))[s]);
        bs[j] = __uint_as_float((u32)((const unsigned short*)b2)[s] << 16);
        PIN(omg[j]); PIN(bs[j]);
    }
    OpFrag<64, D, 2> a1; a1.init(ws + WS_UF, F * D * 2, d0, lds, w, wa, lane);
    OpTr<128, S * 2, 2, false> b1; b1.init(w2, F * S * 2, s0, lds + a1.BYTES, w, wb, lane);
    OpTr<64, D * 2, 2, false> a2; a2.init(wo, D * D * 2, d0, lds, w, wa, lane);
    OpFrag<128, S, 2> bb2; bb2.init(ws + WS_OF, S * D * 2, s0, lds + a2.BYTES, w, wb, lane);
    constexpr int NS = POST_NS, PPW = 6, R1 = F / 64 / NS, R2 = D / 64 / NS;
    f32x4 acc[2][4]; zero_acc(acc);
    Frags<2, 4> f0, f1;
    const NoIssue none;
    gemm_prologue<NS, POST_SLOT>(f0, a1, b1);
    for (int r = 1; r < R1; ++r) gemm_round<NS, 2, POST_SLOT, PPW, false, false>(acc, f0, f1, a1, b1, a1, b1, a1, b1, r * NS);
    gemm_round<NS, 2, POST_SLOT, PPW, false, false>(acc, f0, f1, a1, b1, a2, bb2, a2, bb2, 0);
#pragma unroll
    for (int i = 0; i < 2; ++i)
#pragma unroll
        for (int j = 0; j < 4; ++j) acc[i][j] = (acc[i][j] + bs[j]) * omg[j];
    SB();
    for (int r = 1; r < R2; ++r) gemm_round<NS, 2, POST_SLOT, PPW, false, false>(acc, f0, f1, a2, bb2, a2, bb2, a2, bb2, r * NS);
    gemm_round<NS, 2, POST_SLOT, PPW, true, false>(acc, f0, f1, a2, bb2, a2, bb2, none, none, 0);
    STAMP(2, 6);
#pragma unroll
    for (int j = 0; j < 4; ++j) {
        const auto r0 = __builtin_amdgcn_permlane16_swap(cvtpk(acc[0][j][0], acc[0][j][1]), cvtpk(acc[1][j][0], acc[1][j][1]), false, false);
        const auto r1 = __builtin_amdgcn_permlane16_swap(cvtpk(acc[0][j][2], acc[0][j][3]), cvtpk(acc[1][j][2], acc[1][j][3]), false, false);
        const int s = s0 + wb * 64 + j * 16 + c, d = d0 + wa * 32 + (g & 1) * 16 + (g >> 1) * 8;
        *(u32x4*)(out + ((size_t)s * D + d) * 2) = u32x4{r0[0], r1[0], r0[1], r1[1]};
    }
    STAMP(2, 7);
}

extern "C" void kernel_launch(void* const* d_in, const int* in_sizes, int n_in, void* d_out, int out_size, void* d_ws, size_t ws_size,
                              hipStream_t stream) {
    const char* x = (const char*)d_in[0];
    const char *wq = (const char*)d_in[1], *wk = (const char*)d_in[2], *wv = (const char*)d_in[3], *wo = (const char*)d_in[4];
    const char *w1 = (const char*)d_in[6], *b1 = (const char*)d_in[7], *w2 = (const char*)d_in[8], *b2 = (const char*)d_in[9];
    const char* gw = (const char*)d_in[10];
    char* ws = (char*)d_ws;
    (void)hipFuncSetAttribute((const void*)k_pre, hipFuncAttributeMaxDynamicSharedMemorySize, PRE_LDS);
    (void)hipFuncSetAttribute((const void*)k_mid, hipFuncAttributeMaxDynamicSharedMemorySize, MID_LDS);
    (void)hipFuncSetAttribute((const void*)k_post, hipFuncAttributeMaxDynamicSharedMemorySize, POST_LDS);
    k_pre<<<PRE_Q + PRE_KV + PRE_LN, 256, PRE_LDS, stream>>>(x, wq, wk, wv, gw, ws);
    k_mid<<<MID_ATT + MID_G1, 256, MID_LDS, stream>>>(w1, b1, ws);
    k_post<<<256, 256, POST_LDS, stream>>>(w2, b2, wo, ws, (char*)d_out);
}
```

```cpp
#include <hip/hip_runtime.h>
#include <stdint.h>

typedef unsigned u32;
typedef u32 u32x4 __attribute__((ext_vector_type(4)));
typedef u32 u32x2 __attribute__((ext_vector_type(2)));
typedef float f32x2 __attribute__((ext_vector_type(2)));
typedef float f32x4 __attribute__((ext_vector_type(4)));
typedef float f32x16 __attribute__((ext_vector_type(16)));
typedef __bf16 bf16x2 __attribute__((ext_vector_type(2)));
typedef __bf16 bf16x8 __attribute__((ext_vector_type(8)));
typedef short s16x4 __attribute__((ext_vector_type(4)));
#define LDS __attribute__((address_space(3)))
typedef LDS char* lds_ptr;

constexpr int S = 2048, D = 1024, F = 3584;
constexpr u32 WS_HF = 0, WS_QF = 4u << 20, WS_OF = 8u << 20, WS_UF = 12u << 20, WS_KV = 20u << 20;
constexpr u32 WS_G = WS_KV + (256u << 10), WS_BND = WS_G + (8u << 10), WS_CNT = WS_BND + (4u << 10);
constexpr int CNT_U = 0, CNT_O = 8, CNT_N = 16, CNT_STRIDE = 256;
constexpr float QSCALE = 0.17677669529663687f * 1.4426950408889634f;
constexpr float SAFE_LOG2 = 60.f;
constexpr float SAFE_V = 1.0e12f;

#define SB() __builtin_amdgcn_sched_barrier(0)
#define PIN(x) asm volatile("" : "+v"(x))
#define M16(a, b, c) __builtin_amdgcn_mfma_f32_16x16x32_bf16(__builtin_bit_cast(bf16x8, a), __builtin_bit_cast(bf16x8, b), c, 0, 0, 0)
#define M32(a, b, c) __builtin_amdgcn_mfma_f32_32x32x16_bf16(__builtin_bit_cast(bf16x8, a), __builtin_bit_cast(bf16x8, b), c, 0, 0, 0)
__device__ __forceinline__ void mma16(f32x4& c, const u32x4& a, const u32x4& b) { asm volatile("v_mfma_f32_16x16x32_bf16 %0, %1, %2, %0" : "+v"(c) : "v"(a), "v"(b)); }
__device__ __forceinline__ void mfma_settle() { asm volatile("s_nop 7\n\ts_nop 1" ::: "memory"); SB(); }
#define EX2(x) __builtin_amdgcn_exp2f(x)

__device__ __forceinline__ u32 cvtpk(float lo, float hi) { f32x2 v = {lo, hi}; return __builtin_bit_cast(u32, __builtin_convertvector(v, bf16x2)); }
__device__ __forceinline__ float bf_lo(u32 v) { return __uint_as_float(v << 16); }
__device__ __forceinline__ float bf_hi(u32 v) { return __uint_as_float(v & 0xffff0000u); }
__device__ __forceinline__ float bf_round(float x) { return bf_lo(cvtpk(x, x)); }
__device__ __forceinline__ u32x4 pack8(const f32x4& a, const f32x4& b) { return u32x4{cvtpk(a[0], a[1]), cvtpk(a[2], a[3]), cvtpk(b[0], b[1]), cvtpk(b[2], b[3])}; }

__device__ __forceinline__ u32x4 make_srd(const void* p, u32 bytes) {
    const uint64_t a = (uint64_t)p;
    return u32x4{(u32)a, (u32)(a >> 32) & 0xffffu, bytes, 0x00020000u};
}
__device__ __forceinline__ void dma16(u32 lds, u32 voff, const u32x4& srd, u32 soff) {
    asm volatile("s_mov_b32 m0, %0\n\ts_nop 0\n\tbuffer_load_dwordx4 %1, %2, %3 offen lds" ::"s"(lds), "v"(voff), "s"(srd), "s"(soff) : "memory");
}
__device__ __forceinline__ void dma16_coh(u32 lds, u32 voff, const u32x4& srd, u32 soff) {
    asm volatile("s_mov_b32 m0, %0\n\ts_nop 0\n\tbuffer_load_dwordx4 %1, %2, %3 offen sc1 lds" ::"s"(lds), "v"(voff), "s"(srd), "s"(soff) : "memory");
}
template <int N> __device__ __forceinline__ void wait_bar() {
    __builtin_amdgcn_s_waitcnt(0xc07f);
    asm volatile("s_waitcnt vmcnt(%0)\n\ts_barrier" ::"n"(N) : "memory");
}
__device__ __forceinline__ u32x4 lds_rd128(lds_ptr p) { return *(LDS const u32x4*)p; }
__device__ __forceinline__ u32x2 lds_rdtr(lds_ptr p) { return __builtin_bit_cast(u32x2, __builtin_amdgcn_ds_read_tr16_b64_v4i16((LDS s16x4*)p)); }

typedef __attribute__((address_space(1))) u32 gu32;
__device__ __forceinline__ void store_wt(char* base, u32 bytes, u32 off, const u32x4& v) {
    __builtin_amdgcn_raw_buffer_store_b128(v, __builtin_amdgcn_make_buffer_rsrc(base, 0, bytes, 0x00020000), off, 0, 16);
}
__device__ __forceinline__ void publish(char* ws, int cnt) {
    asm volatile("s_waitcnt vmcnt(0)\n\ts_barrier" ::: "memory");
    if (threadIdx.x == 0) __hip_atomic_fetch_add((gu32*)(ws + WS_CNT + cnt * CNT_STRIDE), 1u, __ATOMIC_RELAXED, __HIP_MEMORY_SCOPE_AGENT);
}
__device__ __forceinline__ void await(char* ws, int cnt, u32 target) {
    gu32* p = (gu32*)(ws + WS_CNT + cnt * CNT_STRIDE);
    for (int spins = 0; spins < (1 << 20); ++spins) {
        if (__builtin_amdgcn_readfirstlane(__hip_atomic_load(p, __ATOMIC_RELAXED, __HIP_MEMORY_SCOPE_AGENT)) >= target) break;
        __builtin_amdgcn_s_sleep(1);
    }
    asm volatile("" ::: "memory");
}

#define STAMP(k, i)

template <int R, int NR, int KSTR> struct OpFrag {
    static constexpr int BYTES = 4 * R * 16, PPW = BYTES / 4096, RPF = 1;
    static constexpr bool REG = false;
    u32x4 srd; u32 voff, ldst; lds_ptr rd;
    __device__ __forceinline__ void init(const void* base, u32 bytes, int row0, int k0, lds_ptr img, int w, int wrow, int lane) {
        srd = make_srd(base, bytes);
        voff = (u32)(row0 + lane + (w + 4 * k0) * NR) * 16u;
        ldst = (u32)(uintptr_t)img + w * (R * 16);
        rd = img + ((lane >> 4) * R + wrow + (lane & 15)) * 16;
    }
    __device__ __forceinline__ void issue(int slot, u32 pos, int i) const { dma16_coh(ldst + pos + i * 1024, voff, srd, (u32)slot * (KSTR * 4u * NR * 16u) + i * 1024u); }
    __device__ __forceinline__ void read(u32x4& f, int pos, int i, int) const { f = lds_rd128(rd + pos + i * 256); }
};

template <int C, int RS, int KSTR, bool NAT, int NF> struct OpTr {
    static constexpr int BYTES = 32 * C * 2, PPW = BYTES / 4096, RPF = 2;
    static constexpr bool REG = false;
    u32x4 srd; u32 voff, ldst; lds_ptr rd[NF];
    __device__ __forceinline__ void init(const void* base, u32 bytes, int col0, int k0, lds_ptr img, int w, int wch, int lane) {
        srd = make_srd(base, bytes);
        ldst = (u32)(uintptr_t)img + w * 1024;
        const int g = lane >> 4, q = (lane >> 2) & 3, p = lane & 3;
        if constexpr (C == 128) {
            const int f = NAT ? (g | (((w >> 1) & 1) << 2)) : (4 * (w & 1) + g);
            voff = (u32)(32 * k0 + 4 * w + g) * RS + col0 * 2 + ((((lane & 15) >> 1) ^ f) * 32) + (lane & 1) * 16;
            const int row = NAT ? 8 * g + q : 4 * g + q, fr = NAT ? (q | ((g & 1) << 2)) : (row & 7);
#pragma unroll
            for (int i = 0; i < NF; ++i) rd[i] = img + row * 256 + (((wch + i) ^ fr) * 32) + p * 8;
        } else {
            static_assert(C == 128 || (C == 64 && !NAT), "unsupported transposed operand");
            voff = (u32)(32 * k0 + 8 * w + (lane >> 3)) * RS + col0 * 2 + ((((lane & 7) >> 1) ^ g) * 32) + (lane & 1) * 16;
            const int row = 4 * g + q;
#pragma unroll
            for (int i = 0; i < NF; ++i) rd[i] = img + row * 128 + (((wch + i) ^ ((row >> 1) & 3)) * 32) + p * 8;
        }
    }
    __device__ __forceinline__ void issue(int slot, u32 pos, int i) const { dma16(ldst + pos + i * 4096, voff, srd, (u32)(slot * KSTR * 32 + i * 16) * RS); }
    __device__ __forceinline__ void read(u32x4& f, int pos, int i, int t) const {
        const u32x2 v = lds_rdtr(rd[i] + pos + t * ((NAT ? 4 : 16) * C * 2));
        f[2 * t] = v[0]; f[2 * t + 1] = v[1];
    }
};

struct OpRow {
    static constexpr int BYTES = 128 * 128, PPW = 2, RPF = 1;
    static constexpr bool REG = false;
    u32x4 srd; u32 voff, ldst; lds_ptr rd;
    __device__ __forceinline__ void init(const void* base, u32 bytes, int row0, int kg, lds_ptr img, int w8, int wrow, int lane) {
        srd = make_srd(base, bytes);
        ldst = (u32)(uintptr_t)img + w8 * 1024;
        const int g = lane >> 4, c = lane & 15;
        voff = (u32)(row0 + 8 * w8 + (lane >> 3)) * (D * 2) + (((lane & 7) ^ (4 * (w8 & 1) + g)) * 16);
        rd = img + (wrow + c) * 128 + (((4 * kg + g) ^ (c >> 1)) * 16);
    }
    __device__ __forceinline__ void issue(int slot, u32 pos, int i) const { dma16(ldst + pos + i * 8192, voff, srd, (u32)slot * 128u + i * (64u * D * 2u)); }
    __device__ __forceinline__ void read(u32x4& f, int pos, int i, int) const { f = lds_rd128(rd + pos + i * 2048); }
};

struct OpKV {
    static constexpr int BYTES = 8192, PPW = 1, RPF = 2;
    static constexpr bool REG = false;
    u32x4 srd; u32 voff, ldst; lds_ptr rd[2];
    __device__ __forceinline__ void init(const void* wk, const void* wv, int kg, lds_ptr img, int w, int lane) {
        srd = make_srd(kg ? wv : wk, D * 64);
        ldst = (u32)(uintptr_t)img + kg * 4096 + w * 1024;
        voff = (u32)(16 * w + (lane >> 2)) * 64 + ((((lane >> 1) & 1) ^ (lane >> 5)) * 32) + (lane & 1) * 16;
        const int g = lane >> 4, q = (lane >> 2) & 3, p = lane & 3;
#pragma unroll
        for (int ch = 0; ch < 2; ++ch) rd[ch] = img + kg * 2048 + (8 * g + q) * 64 + ((ch ^ (g & 1)) * 32) + p * 8;
    }
    __device__ __forceinline__ void issue(int slot, u32 pos, int) const { dma16(ldst + pos, voff, srd, (u32)slot * 4096u); }
    __device__ __forceinline__ void read(u32x4& f, int pos, int i, int t) const {
        const u32x2 v = lds_rdtr(rd[i & 1] + pos + (i >> 1) * 4096 + t * 256);
        f[2 * t] = v[0]; f[2 * t + 1] = v[1];
    }
};

template <int NF, int NR, int KSTR, bool COH> struct OpReg {
    static constexpr int PPW = NF, RPF = 0;
    static constexpr bool REG = true;
    u32x4 srd; u32 voff;
    __device__ __forceinline__ void init(const void* base, u32 bytes, int row0, int k0, int lane) {
        srd = make_srd(base, bytes);
        voff = (u32)(row0 + (lane & 15) + ((lane >> 4) + 4 * k0) * NR) * 16u;
    }
    __device__ __forceinline__ void load(u32x4& f, int slot, int i) const {
        const u32 soff = (u32)slot * (KSTR * 4u * NR * 16u);
        if (COH) asm volatile("buffer_load_dwordx4 %0, %1, %2, %3 offen offset:%4 sc1" : "+v"(f) : "v"(voff), "s"(srd), "s"(soff), "n"(i * 256) : "memory");
        else asm volatile("buffer_load_dwordx4 %0, %1, %2, %3 offen offset:%4" : "+v"(f) : "v"(voff), "s"(srd), "s"(soff), "n"(i * 256) : "memory");
    }
};

template <int NA, int NB> struct Frags { u32x4 a[NA], b[NB]; };
template <int NS, int NA, int NB> struct Engine { f32x4 acc[NA][NB]; Frags<NA, NB> f[2]; u32x4 ring[NS][NA]; };

template <int I, int SWAP, bool READ, bool ISSUE, int NA, int NB, class CA, class RA, class RB, class IA, class IB>
__device__ __forceinline__ void gemm_step(f32x4 (&acc)[NA][NB], const Frags<NA, NB>& cur, Frags<NA, NB>& nxt, u32x4 (&ring)[NA], const RA& ra, const RB& rb, int rpos,
                                          const IA& ia, const IB& ib, int islot, u32 ipos) {
    constexpr int NM = NA * NB, NRA = NA * RA::RPF, NRD = NRA + NB * RB::RPF, NDA = IA::REG ? 0 : IA::PPW, ND = NDA + IB::PPW;
    constexpr int RPM = (NRD + NM - 1) / NM, DSTEP = NM / ND;
    if constexpr (CA::REG) {
#pragma unroll
        for (int i = 0; i < NA; ++i) PIN(ring[i]);
    }
#pragma unroll
    for (int m = 0; m < NM; ++m) {
        const int i = m % NA, j = m / NA;
        const u32x4 av = CA::REG ? ring[i] : cur.a[i];
        if (i >= SWAP) mma16(acc[i][j], cur.b[j], av);
        else mma16(acc[i][j], av, cur.b[j]);
        if constexpr (READ) {
#pragma unroll
            for (int r = m * RPM; r < (m + 1) * RPM && r < NRD; ++r) {
                if (r < RB::RPF) rb.read(nxt.b[0], rpos, 0, r);
                else if (r >= RB::RPF + NRA) rb.read(nxt.b[(r - NRA) / RB::RPF], rpos, (r - NRA) / RB::RPF, (r - NRA) % RB::RPF);
                else if constexpr (!RA::REG) ra.read(nxt.a[(r - RB::RPF) / RA::RPF], rpos, (r - RB::RPF) / RA::RPF, (r - RB::RPF) % RA::RPF);
            }
        }
        if (ISSUE && m % DSTEP == DSTEP - 1 - (ND == 1 ? NM / 2 : 0) && m / DSTEP < ND) {
            const int d = m / DSTEP;
            if (d >= NDA) ib.issue(islot, ipos, d - NDA);
            else if constexpr (!IA::REG) ia.issue(islot, ipos, d);
        }
        SB();
    }
    if constexpr (ISSUE && IA::REG) {
#pragma unroll
        for (int i = 0; i < NA; ++i) ia.load(ring[i], islot, i);
    }
}

template <int P, int COUNT, int REM, int SLOTB, int CSW, int ISW, int SWAP, int NS, int NA, int NB, class A1, class B1, class A2, class B2, class Hook, class Gate>
__device__ __forceinline__ void gemm_steps(Engine<NS, NA, NB>& e, const A1& a1, const B1& b1, const A2& a2, const B2& b2, int islot1, int islot2, const Hook& hook, const Gate& gate) {
    constexpr int PPW = A1::PPW + B1::PPW, POS = P % NS * SLOTB, RPOS = (P + 1) % NS * SLOTB;
    constexpr bool READ = REM > 1, ISSUE = REM > NS;
    static_assert(PPW == A2::PPW + B2::PPW, "both operand sets issue the same number of loads");
    if constexpr (P == CSW) { mfma_settle(); hook(); }
    if constexpr (READ) wait_bar<(REM - 2 < NS - 2 ? REM - 2 : NS - 2) * PPW>();
    if constexpr (P == ISW) gate();
    Frags<NA, NB>& cur = e.f[P & 1];
    Frags<NA, NB>& nxt = e.f[(P & 1) ^ 1];
    if constexpr (P + 1 < CSW && P < ISW) gemm_step<P, SWAP, READ, ISSUE, NA, NB, A1>(e.acc, cur, nxt, e.ring[P % NS], a1, b1, RPOS, a1, b1, islot1 + P, POS);
    else if constexpr (P + 1 < CSW) gemm_step<P, SWAP, READ, ISSUE, NA, NB, A1>(e.acc, cur, nxt, e.ring[P % NS], a1, b1, RPOS, a2, b2, islot2 + P, POS);
    else if constexpr (P < CSW) gemm_step<P, SWAP, READ, ISSUE, NA, NB, A1>(e.acc, cur, nxt, e.ring[P % NS], a2, b2, RPOS, a2, b2, islot2 + P, POS);
    else gemm_step<P, SWAP, READ, ISSUE, NA, NB, A2>(e.acc, cur, nxt, e.ring[P % NS], a2, b2, RPOS, a2, b2, islot2 + P, POS);
    if constexpr (COUNT > 1) gemm_steps<P + 1, COUNT - 1, REM - 1, SLOTB, CSW, ISW, SWAP>(e, a1, b1, a2, b2, islot1, islot2, hook, gate);
    else if constexpr (REM == 1) mfma_settle();
}

template <int SLOTB, int NS, int NA, int NB, class A, class B, class Gate>
__device__ __forceinline__ void gemm_start(Engine<NS, NA, NB>& e, const A& a, const B& b, const Gate& gate) {
#pragma unroll
    for (int i = 0; i < NA; ++i)
#pragma unroll
        for (int j = 0; j < NB; ++j) e.acc[i][j] = f32x4{};
#pragma unroll
    for (int p = 0; p < NS; ++p) {
#pragma unroll
        for (int i = 0; i < B::PPW; ++i) b.issue(p, p * SLOTB, i);
        if constexpr (!A::REG) {
#pragma unroll
            for (int i = 0; i < A::PPW; ++i) a.issue(p, p * SLOTB, i);
        }
    }
    if constexpr (A::REG) {
        gate();
#pragma unroll
        for (int p = 0; p < NS; ++p)
#pragma unroll
            for (int i = 0; i < NA; ++i) { e.ring[p][i] = u32x4{}; a.load(e.ring[p][i], p, i); }
        constexpr int PPW = A::PPW + B::PPW, SHORT = ((NS - 2) * PPW - NA * (NS - 1) + B::PPW - 1) / B::PPW;
        wait_bar<NA * (NS - (SHORT > 1 ? SHORT : 1))>();
    } else {
        wait_bar<(NS - 1) * (A::PPW + B::PPW)>();
#pragma unroll
        for (int i = 0; i < NA * A::RPF; ++i) a.read(e.f[0].a[i / A::RPF], 0, i / A::RPF, i % A::RPF);
    }
#pragma unroll
    for (int i = 0; i < NB * B::RPF; ++i) b.read(e.f[0].b[i / B::RPF], 0, i / B::RPF, i % B::RPF);
    SB();
}

constexpr int MANY = 1 << 20;
template <int SLOTB, int NSLOTS, int SWAP, int NS, int NA, int NB, class A, class B>
__device__ __forceinline__ void gemm_simple(Engine<NS, NA, NB>& e, const A& a, const B& b) {
    constexpr int RL = NS % 2 ? 2 * NS : NS, ROUNDS = (NSLOTS - NS) / RL, TAIL = NSLOTS - ROUNDS * RL;
    auto none = [] {};
    gemm_start<SLOTB>(e, a, b, none);
    for (int r = 0; r < ROUNDS; ++r) gemm_steps<0, RL, MANY, SLOTB, -1, -1, SWAP>(e, a, b, a, b, 0, r * RL + NS, none, none);
    gemm_steps<0, TAIL, TAIL, SLOTB, -1, -1, SWAP>(e, a, b, a, b, 0, ROUNDS * RL + NS, none, none);
}

template <int N> __device__ __forceinline__ void pair_reduce(f32x4 (&keep)[N], const f32x4 (&give)[N], lds_ptr buf, int w8, int lane) {
#pragma unroll
    for (int i = 0; i < N; ++i) *(LDS f32x4*)(buf + (w8 * N + i) * 1024 + lane * 16) = give[i];
    wait_bar<0>();
#pragma unroll
    for (int i = 0; i < N; ++i) keep[i] += *(LDS const f32x4*)(buf + ((w8 ^ 4) * N + i) * 1024 + lane * 16);
}

constexpr int PRE_Q = 128, PRE_KV = 16, PRE_LN = 64, PRE_PF = 48, PRE_LDS = 4 * 32768;

__device__ __forceinline__ void q_tile(int b, const void* x, const void* wq, char* ws, lds_ptr lds, int w8, int lane) {
    const int s0 = ((b & 7) * 2 + ((b >> 3) & 1)) * 128, n0 = (b >> 4) * 128;
    const int kg = w8 >> 2, w = w8 & 3, wa = w & 1, wb = w >> 1;
    OpTr<128, D * 2, 2, true, 4> a; a.init(wq, D * D * 2, n0, kg, lds + kg * 8192, w, wa * 4, lane);
    OpRow bb; bb.init(x, S * D * 2, s0, kg, lds + 16384, w8, wb * 64, lane);
    Engine<4, 4, 4> e;
    gemm_simple<32768, D / 64, 4>(e, a, bb);
    auto& acc = e.acc;
    STAMP(0, 6);
    f32x4 keep[8], give[8];
#pragma unroll
    for (int i = 0; i < 8; ++i) { keep[i] = kg ? acc[2 + (i >> 2)][i & 3] : acc[i >> 2][i & 3]; give[i] = kg ? acc[i >> 2][i & 3] : acc[2 + (i >> 2)][i & 3]; }
    pair_reduce(keep, give, lds, w8, lane);
    const int g = lane >> 4, c = lane & 15, head = (n0 + wa * 64) / 32 + kg;
#pragma unroll
    for (int j = 0; j < 4; ++j) {
        const int s = s0 + wb * 64 + j * 16 + c;
        *(u32x4*)(ws + WS_QF + ((head * 64 + (s >> 5)) * 128 + g * 32 + (s & 31)) * 16) = pack8(keep[j] * QSCALE, keep[4 + j] * QSCALE);
    }
}

__device__ __forceinline__ void kv_tile(int t, const void* x, const void* wk, const void* wv, char* ws, lds_ptr lds, int w8, int lane) {
    const int kg = w8 >> 2, w = w8 & 3;
    OpKV a; a.init(wk, wv, kg, lds, w, lane);
    OpRow bb; bb.init(x, S * D * 2, t * 128, kg, lds + 8192, w8, w * 32, lane);
    Engine<4, 4, 2> e;
    gemm_simple<24576, D / 64, 2>(e, a, bb);
    auto& acc = e.acc;
    STAMP(0, 6);
    f32x4 keep[4], give[4];
#pragma unroll
    for (int i = 0; i < 4; ++i) { keep[i] = kg ? acc[2 + (i >> 1)][i & 1] : acc[i >> 1][i & 1]; give[i] = kg ? acc[i >> 1][i & 1] : acc[2 + (i >> 1)][i & 1]; }
    pair_reduce(keep, give, lds, w8, lane);
    const int g = lane >> 4, c = lane & 15;
    char* blk = ws + WS_KV + (t * 4 + w) * 4096;
    float bnd = 0.f;
    if (kg == 0) {
#pragma unroll
        for (int j = 0; j < 2; ++j) {
            const int row = ((c >> 3) << 4) | (j << 3) | (c & 7);
            *(u32x4*)(blk + (g * 32 + row) * 16) = pack8(keep[j], keep[2 + j]);
            float n = 0.f;
#pragma unroll
            for (int r = 0; r < 4; ++r) n += keep[j][r] * keep[j][r] + keep[2 + j][r] * keep[2 + j][r];
            n += __shfl_xor(n, 16); n += __shfl_xor(n, 32);
            bnd = n == n ? fmaxf(bnd, n) : __builtin_inff();
        }
    } else {
#pragma unroll
        for (int i = 0; i < 2; ++i) {
            *(u32x4*)(blk + 2048 + (g * 32 + i * 16 + c) * 16) = pack8(keep[2 * i], keep[2 * i + 1]);
#pragma unroll
            for (int r = 0; r < 4; ++r)
                if (!(fabsf(keep[2 * i][r]) <= SAFE_V && fabsf(keep[2 * i + 1][r]) <= SAFE_V)) bnd = __builtin_inff();
        }
    }
#pragma unroll
    for (int o = 1; o < 64; o <<= 1) bnd = fmaxf(bnd, __shfl_xor(bnd, o));
    if (lane == 0) ((float*)(ws + WS_BND))[t * 8 + w8] = bnd;
}

template <int CTRL> __device__ __forceinline__ float dpp_add(float v) {
    return v + __builtin_bit_cast(float, __builtin_amdgcn_update_dpp(0, __builtin_bit_cast(int, v), CTRL, 0xf, 0xf, true));
}
__device__ __forceinline__ float wave_sum(float v) {
    v = dpp_add<0xb1>(v); v = dpp_add<0x4e>(v); v = dpp_add<0x141>(v); v = dpp_add<0x140>(v);
    const auto a = __builtin_amdgcn_permlane16_swap(__float_as_uint(v), __float_as_uint(v), false, false);
    v = __uint_as_float(a[0]) + __uint_as_float(a[1]);
    const auto b = __builtin_amdgcn_permlane32_swap(__float_as_uint(v), __float_as_uint(v), false, false);
    return __uint_as_float(b[0]) + __uint_as_float(b[1]);
}

template <int CTRL> __device__ __forceinline__ float dpp_max(float v) {
    return fmaxf(v, __builtin_bit_cast(float, __builtin_amdgcn_update_dpp(0, __builtin_bit_cast(int, v), CTRL, 0xf, 0xf, true)));
}
__device__ __forceinline__ float wave_max(float v) {
    v = dpp_max<0xb1>(v); v = dpp_max<0x4e>(v); v = dpp_max<0x141>(v); v = dpp_max<0x140>(v);
    const auto a = __builtin_amdgcn_permlane16_swap(__float_as_uint(v), __float_as_uint(v), false, false);
    v = fmaxf(__uint_as_float(a[0]), __uint_as_float(a[1]));
    const auto b = __builtin_amdgcn_permlane32_swap(__float_as_uint(v), __float_as_uint(v), false, false);
    return fmaxf(__uint_as_float(b[0]), __uint_as_float(b[1]));
}

template <u32 BYTES, int NB, int NTHR> __device__ __forceinline__ void touch(const char* p, int b, u32 scratch) {
    constexpr u32 LINES = BYTES / 128, PER = (LINES + NB - 1) / NB, N = (PER + NTHR - 1) / NTHR;
    const u32x4 srd = make_srd(p, BYTES);
#pragma unroll
    for (u32 i = 0; i < N; ++i) {
        const u32 l = i * NTHR + threadIdx.x, off = l < PER ? (b * PER + l) * 128u : 0x80000000u;
        asm volatile("s_mov_b32 m0, %0\n\ts_nop 0\n\tbuffer_load_dword %1, %2, 0 offen lds" ::"s"(scratch), "v"(off), "s"(srd) : "memory");
    }
}

__device__ __forceinline__ void ln_rows(int j, const char* x, const char* gw, const char* w1, char* ws, lds_ptr lds, u32 scratch, int w8, int lane) {
    const int hf = w8 >> 2, w = w8 & 3;
    u32x4 xv[8];
#pragma unroll
    for (int e = 0; e < 8; ++e) xv[e] = *(const u32x4*)(x + (size_t)(32 * j + 4 * w + (e & 3) + 16 * (e >> 2)) * (D * 2) + hf * 1024 + lane * 16);
    u32x4 gv = *(const u32x4*)(gw + hf * 1024 + lane * 16);
    PIN(gv);
#pragma unroll
    for (int e = 0; e < 8; ++e) PIN(xv[e]);
    touch<S * F * 2, PRE_LN + PRE_PF, 512>(w1, j, scratch);
    LDS float* red = (LDS float*)(lds + 65536);
    float mean[8], rs[8], dsel = 0.f, ssel = 0.f;
#pragma unroll
    for (int e = 0; e < 8; ++e) {
        float s = 0.f, d = 0.f;
#pragma unroll
        for (int m = 0; m < 4; ++m) {
            const float a = bf_lo(xv[e][m]), b = bf_hi(xv[e][m]);
            s += a + b; d += a * bf_lo(gv[m]) + b * bf_hi(gv[m]);
        }
        s = wave_sum(s); d = wave_sum(d);
        mean[e] = s;
        ssel = lane == e ? s : ssel; dsel = lane == e ? d : dsel;
    }
    if (lane < 8) { red[w8 * 16 + lane] = ssel; red[w8 * 16 + 8 + lane] = dsel; }
    wait_bar<63>();
    float vsel = 0.f;
#pragma unroll
    for (int e = 0; e < 8; ++e) {
        mean[e] = (mean[e] + red[(w8 ^ 4) * 16 + e]) * (1.f / D);
        float v = 0.f;
#pragma unroll
        for (int m = 0; m < 4; ++m) { const float a = bf_lo(xv[e][m]) - mean[e], b = bf_hi(xv[e][m]) - mean[e]; v += a * a + b * b; }
        rs[e] = wave_sum(v);
        vsel = lane == e ? rs[e] : vsel;
    }
    if (lane < 8) {
        red[128 + w8 * 16 + lane] = vsel;
        if (hf == 0) {
            const float z = bf_round(dsel + red[(w8 ^ 4) * 16 + 8 + lane]);
            ((float*)(ws + WS_G))[32 * j + 4 * w + (lane & 3) + 16 * (lane >> 2)] = bf_round(1.f / (1.f + __expf(-z)));
        }
    }
    wait_bar<63>();
    STAMP(0, 6);
#pragma unroll
    for (int e = 0; e < 8; ++e) {
        rs[e] = 1.f / bf_round(sqrtf(bf_round(bf_round((rs[e] + red[128 + (w8 ^ 4) * 16 + e]) * (1.f / D)) + 1e-5f)));
        mean[e] = bf_round(mean[e]);
    }
    const lds_ptr tb = lds + w8 * 8192;
#pragma unroll
    for (int m = 0; m < 4; ++m) {
        float lo[8], hi[8];
#pragma unroll
        for (int e = 0; e < 8; ++e) { lo[e] = (bf_lo(xv[e][m]) - mean[e]) * rs[e]; hi[e] = (bf_hi(xv[e][m]) - mean[e]) * rs[e]; }
        *(LDS u32x4*)(tb + lane * 128 + (((2 * m) ^ (lane & 7)) * 16)) = u32x4{cvtpk(lo[0], lo[1]), cvtpk(lo[2], lo[3]), cvtpk(lo[4], lo[5]), cvtpk(lo[6], lo[7])};
        *(LDS u32x4*)(tb + lane * 128 + (((2 * m + 1) ^ (lane & 7)) * 16)) = u32x4{cvtpk(hi[0], hi[1]), cvtpk(hi[2], hi[3]), cvtpk(hi[4], hi[5]), cvtpk(hi[6], hi[7])};
    }
    char* dst = ws + WS_HF + (size_t)(j * 4 + w) * (D * 16) + hf * 8192 + lane * 16;
#pragma unroll
    for (int i = 0; i < 8; ++i) *(u32x4*)(dst + i * 1024) = lds_rd128(tb + (i * 8 + (lane >> 3)) * 128 + (((lane & 7) ^ (lane >> 3)) * 16));
}

__global__ void __launch_bounds__(512, 2) k_pre(const char* __restrict__ x, const char* __restrict__ wq, const char* __restrict__ wk, const char* __restrict__ wv,
                                                const char* __restrict__ gw, const char* __restrict__ w1, char* __restrict__ ws) {
    extern __shared__ __attribute__((aligned(16))) char smem[];
    const int lane = threadIdx.x & 63, w8 = __builtin_amdgcn_readfirstlane(threadIdx.x >> 6), b = blockIdx.x;
    STAMP(0, 0);
    if (b < PRE_Q) q_tile(b, x, wq, ws, (lds_ptr)smem, w8, lane);
    else if (b < PRE_Q + PRE_KV) kv_tile(b - PRE_Q, x, wk, wv, ws, (lds_ptr)smem, w8, lane);
    else {
        const int r = b - PRE_Q - PRE_KV;
        const u32 scratch = (u32)(uintptr_t)(lds_ptr)smem + 98304 + w8 * 256;
        if (r < PRE_LN) ln_rows(r, x, gw, w1, ws, (lds_ptr)smem, scratch, w8, lane);
        else {
            touch<256 * D * 2, PRE_PF / 8, 512>(x + (b & 7) * (256 * D * 2), (r - PRE_LN) >> 3, scratch);
            touch<D * D * 2, PRE_PF / 8, 512>(wq, (r - PRE_LN) >> 3, scratch);
            if (r == PRE_LN && threadIdx.x < CNT_N) *(u32*)(ws + WS_CNT + threadIdx.x * CNT_STRIDE) = 0u;
            touch<S * F * 2, PRE_LN + PRE_PF, 512>(w1, r, scratch);
        }
        asm volatile("s_waitcnt vmcnt(0)" ::: "memory");
    }
    STAMP(0, 7);
}

#define G1_PRIO 1
#define G1_NS_ 6
#define POST_PRIO 0
#define ATT_PRIO 0
constexpr int MID_ATT = 256, MID_G1 = 224, MID_PF = 32, G1_NS = G1_NS_;
constexpr int KV_STAGE = 16384, KV_NSTAGE = 16;

#define ATT_LOOP_ASM \
    "s_mov_b32 s23, 16\n" \
    "s_mov_b32 s24, 0x4000\n" \
    "s_mov_b32 s22, 0x8000\n" \
    "s_add_u32 s20, %[ldst], 0x8c00\n" \
    "s_mov_b32 s21, 0x8c00\n" \
    "ATT_LOOP_%=:\n" \
    "v_mfma_f32_32x32x16_bf16 v[16:31], v[88:91], v[80:83], 0\n" \
    "ds_read_b128 v[96:99], v140 offset:2048\n" \
    "v_exp_f32 v0, v0\n" \
    "v_exp_f32 v1, v1\n" \
    "v_exp_f32 v2, v2\n" \
    "v_mfma_f32_32x32x16_bf16 v[48:63], v[112:115], v[132:135], v[48:63]\n" \
    "ds_read_b128 v[100:103], v140 offset:3072\n" \
    "v_exp_f32 v3, v3\n" \
    "v_cvt_pk_bf16_f32 v124, v0, v1\n" \
    "v_exp_f32 v4, v4\n" \
    "v_exp_f32 v5, v5\n" \
    "v_mfma_f32_16x16x32_bf16 v[68:71], v[120:123], v[132:135], v[68:71]\n" \
    "v_cvt_pk_bf16_f32 v125, v2, v3\n" \
    "v_exp_f32 v6, v6\n" \
    "v_exp_f32 v7, v7\n" \
    "v_mfma_f32_32x32x16_bf16 v[16:31], v[92:95], v[84:87], v[16:31]\n" \
    "s_mov_b32 m0, s20\n" \
    "s_add_u32 s20, s20, 0x400\n" \
    "buffer_load_dwordx4 v142, %[srd], s21 offen lds\n" \
    "s_add_u32 s21, s21, 0x400\n" \
    "s_add_u32 s22, s22, 0x4000\n" \
    "s_and_b32 s22, s22, 0xffff\n" \
    "s_add_u32 s20, s22, %[ldst]\n" \
    "s_add_u32 s21, s21, 0x3000\n" \
    "v_cvt_pk_bf16_f32 v126, v4, v5\n" \
    "v_exp_f32 v8, v8\n" \
    "v_exp_f32 v9, v9\n" \
    "v_cvt_pk_bf16_f32 v127, v6, v7\n" \
    "v_exp_f32 v10, v10\n" \
    "v_mfma_f32_32x32x16_bf16 v[48:63], v[116:119], v[136:139], v[48:63]\n" \
    "v_exp_f32 v11, v11\n" \
    "v_cvt_pk_bf16_f32 v128, v8, v9\n" \
    "v_exp_f32 v12, v12\n" \
    "v_exp_f32 v13, v13\n" \
    "v_cvt_pk_bf16_f32 v129, v10, v11\n" \
    "v_mfma_f32_16x16x32_bf16 v[68:71], v[120:123], v[136:139], v[68:71]\n" \
    "v_exp_f32 v14, v14\n" \
    "v_exp_f32 v15, v15\n" \
    "v_cvt_pk_bf16_f32 v130, v12, v13\n" \
    "v_cvt_pk_bf16_f32 v131, v14, v15\n" \
    "s_waitcnt lgkmcnt(0)\n" \
    "v_mfma_f32_32x32x16_bf16 v[0:15], v[104:107], v[72:75], 0\n" \
    "ds_read_b128 v[88:91], v140 offset:8192\n" \
    "v_exp_f32 v16, v16\n" \
    "v_exp_f32 v17, v17\n" \
    "v_exp_f32 v18, v18\n" \
    "v_mfma_f32_32x32x16_bf16 v[32:47], v[96:99], v[124:127], v[32:47]\n" \
    "ds_read_b128 v[92:95], v140 offset:9216\n" \
    "v_exp_f32 v19, v19\n" \
    "v_cvt_pk_bf16_f32 v132, v16, v17\n" \
    "v_exp_f32 v20, v20\n" \
    "v_exp_f32 v21, v21\n" \
    "v_mfma_f32_16x16x32_bf16 v[64:67], v[120:123], v[124:127], v[64:67]\n" \
    "v_cvt_pk_bf16_f32 v133, v18, v19\n" \
    "v_exp_f32 v22, v22\n" \
    "v_exp_f32 v23, v23\n" \
    "v_mfma_f32_32x32x16_bf16 v[0:15], v[108:111], v[76:79], v[0:15]\n" \
    "v_cvt_pk_bf16_f32 v134, v20, v21\n" \
    "v_exp_f32 v24, v24\n" \
    "v_exp_f32 v25, v25\n" \
    "v_cvt_pk_bf16_f32 v135, v22, v23\n" \
    "v_exp_f32 v26, v26\n" \
    "v_mfma_f32_32x32x16_bf16 v[32:47], v[100:103], v[128:131], v[32:47]\n" \
    "v_exp_f32 v27, v27\n" \
    "v_cvt_pk_bf16_f32 v136, v24, v25\n" \
    "v_exp_f32 v28, v28\n" \
    "v_exp_f32 v29, v29\n" \
    "v_cvt_pk_bf16_f32 v137, v26, v27\n" \
    "v_mfma_f32_16x16x32_bf16 v[64:67], v[120:123], v[128:131], v[64:67]\n" \
    "v_exp_f32 v30, v30\n" \
    "v_exp_f32 v31, v31\n" \
    "v_cvt_pk_bf16_f32 v138, v28, v29\n" \
    "v_cvt_pk_bf16_f32 v139, v30, v31\n" \
    "v_mfma_f32_32x32x16_bf16 v[16:31], v[104:107], v[80:83], 0\n" \
    "ds_read_b128 v[112:115], v140 offset:6144\n" \
    "v_exp_f32 v0, v0\n" \
    "v_exp_f32 v1, v1\n" \
    "v_exp_f32 v2, v2\n" \
    "v_mfma_f32_32x32x16_bf16 v[48:63], v[96:99], v[132:135], v[48:63]\n" \
    "ds_read_b128 v[116:119], v140 offset:7168\n" \
    "v_exp_f32 v3, v3\n" \
    "v_cvt_pk_bf16_f32 v124, v0, v1\n" \
    "v_exp_f32 v4, v4\n" \
    "v_exp_f32 v5, v5\n" \
    "v_mfma_f32_16x16x32_bf16 v[68:71], v[120:123], v[132:135], v[68:71]\n" \
    "v_cvt_pk_bf16_f32 v125, v2, v3\n" \
    "v_exp_f32 v6, v6\n" \
    "v_exp_f32 v7, v7\n" \
    "v_mfma_f32_32x32x16_bf16 v[16:31], v[108:111], v[84:87], v[16:31]\n" \
    "v_cvt_pk_bf16_f32 v126, v4, v5\n" \
    "v_exp_f32 v8, v8\n" \
    "v_exp_f32 v9, v9\n" \
    "v_cvt_pk_bf16_f32 v127, v6, v7\n" \
    "v_exp_f32 v10, v10\n" \
    "v_mfma_f32_32x32x16_bf16 v[48:63], v[100:103], v[136:139], v[48:63]\n" \
    "v_exp_f32 v11, v11\n" \
    "v_cvt_pk_bf16_f32 v128, v8, v9\n" \
    "v_exp_f32 v12, v12\n" \
    "v_exp_f32 v13, v13\n" \
    "v_cvt_pk_bf16_f32 v129, v10, v11\n" \
    "v_mfma_f32_16x16x32_bf16 v[68:71], v[120:123], v[136:139], v[68:71]\n" \
    "v_exp_f32 v14, v14\n" \
    "v_exp_f32 v15, v15\n" \
    "v_cvt_pk_bf16_f32 v130, v12, v13\n" \
    "v_cvt_pk_bf16_f32 v131, v14, v15\n" \
    "s_waitcnt lgkmcnt(0)\n" \
    "v_mfma_f32_32x32x16_bf16 v[0:15], v[88:91], v[72:75], 0\n" \
    "ds_read_b128 v[104:107], v140 offset:12288\n" \
    "v_exp_f32 v16, v16\n" \
    "v_exp_f32 v17, v17\n" \
    "v_exp_f32 v18, v18\n" \
    "v_mfma_f32_32x32x16_bf16 v[32:47], v[112:115], v[124:127], v[32:47]\n" \
    "ds_read_b128 v[108:111], v140 offset:13312\n" \
    "v_exp_f32 v19, v19\n" \
    "v_cvt_pk_bf16_f32 v132, v16, v17\n" \
    "v_exp_f32 v20, v20\n" \
    "v_exp_f32 v21, v21\n" \
    "v_mfma_f32_16x16x32_bf16 v[64:67], v[120:123], v[124:127], v[64:67]\n" \
    "v_cvt_pk_bf16_f32 v133, v18, v19\n" \
    "v_exp_f32 v22, v22\n" \
    "v_exp_f32 v23, v23\n" \
    "v_mfma_f32_32x32x16_bf16 v[0:15], v[92:95], v[76:79], v[0:15]\n" \
    "v_cvt_pk_bf16_f32 v134, v20, v21\n" \
    "v_exp_f32 v24, v24\n" \
    "v_exp_f32 v25, v25\n" \
    "v_cvt_pk_bf16_f32 v135, v22, v23\n" \
    "v_exp_f32 v26, v26\n" \
    "v_mfma_f32_32x32x16_bf16 v[32:47], v[116:119], v[128:131], v[32:47]\n" \
    "v_exp_f32 v27, v27\n" \
    "v_cvt_pk_bf16_f32 v136, v24, v25\n" \
    "v_exp_f32 v28, v28\n" \
    "v_exp_f32 v29, v29\n" \
    "v_cvt_pk_bf16_f32 v137, v26, v27\n" \
    "v_mfma_f32_16x16x32_bf16 v[64:67], v[120:123], v[128:131], v[64:67]\n" \
    "v_exp_f32 v30, v30\n" \
    "v_exp_f32 v31, v31\n" \
    "v_cvt_pk_bf16_f32 v138, v28, v29\n" \
    "v_cvt_pk_bf16_f32 v139, v30, v31\n" \
    "v_mfma_f32_32x32x16_bf16 v[16:31], v[88:91], v[80:83], 0\n" \
    "ds_read_b128 v[96:99], v140 offset:10240\n" \
    "v_exp_f32 v0, v0\n" \
    "v_exp_f32 v1, v1\n" \
    "v_exp_f32 v2, v2\n" \
    "v_mfma_f32_32x32x16_bf16 v[48:63], v[112:115], v[132:135], v[48:63]\n" \
    "ds_read_b128 v[100:103], v140 offset:11264\n" \
    "v_exp_f32 v3, v3\n" \
    "v_cvt_pk_bf16_f32 v124, v0, v1\n" \
    "v_exp_f32 v4, v4\n" \
    "v_exp_f32 v5, v5\n" \
    "v_mfma_f32_16x16x32_bf16 v[68:71], v[120:123], v[132:135], v[68:71]\n" \
    "v_cvt_pk_bf16_f32 v125, v2, v3\n" \
    "v_exp_f32 v6, v6\n" \
    "v_exp_f32 v7, v7\n" \
    "v_mfma_f32_32x32x16_bf16 v[16:31], v[92:95], v[84:87], v[16:31]\n" \
    "v_cvt_pk_bf16_f32 v126, v4, v5\n" \
    "v_exp_f32 v8, v8\n" \
    "v_exp_f32 v9, v9\n" \
    "v_cvt_pk_bf16_f32 v127, v6, v7\n" \
    "v_exp_f32 v10, v10\n" \
    "v_mfma_f32_32x32x16_bf16 v[48:63], v[116:119], v[136:139], v[48:63]\n" \
    "v_exp_f32 v11, v11\n" \
    "v_cvt_pk_bf16_f32 v128, v8, v9\n" \
    "v_exp_f32 v12, v12\n" \
    "v_exp_f32 v13, v13\n" \
    "v_cvt_pk_bf16_f32 v129, v10, v11\n" \
    "v_mfma_f32_16x16x32_bf16 v[68:71], v[120:123], v[136:139], v[68:71]\n" \
    "v_exp_f32 v14, v14\n" \
    "v_exp_f32 v15, v15\n" \
    "v_cvt_pk_bf16_f32 v130, v12, v13\n" \
    "v_cvt_pk_bf16_f32 v131, v14, v15\n" \
    "s_waitcnt lgkmcnt(0)\n" \
    "s_waitcnt vmcnt(4)\n" \
    "s_barrier\n" \
    "v_mfma_f32_32x32x16_bf16 v[0:15], v[104:107], v[72:75], 0\n" \
    "ds_read_b128 v[88:91], v141 offset:0\n" \
    "v_exp_f32 v16, v16\n" \
    "v_exp_f32 v17, v17\n" \
    "v_exp_f32 v18, v18\n" \
    "v_mfma_f32_32x32x16_bf16 v[32:47], v[96:99], v[124:127], v[32:47]\n" \
    "ds_read_b128 v[92:95], v141 offset:1024\n" \
    "v_exp_f32 v19, v19\n" \
    "v_cvt_pk_bf16_f32 v132, v16, v17\n" \
    "v_exp_f32 v20, v20\n" \
    "v_exp_f32 v21, v21\n" \
    "v_mfma_f32_16x16x32_bf16 v[64:67], v[120:123], v[124:127], v[64:67]\n" \
    "v_cvt_pk_bf16_f32 v133, v18, v19\n" \
    "v_exp_f32 v22, v22\n" \
    "v_exp_f32 v23, v23\n" \
    "v_mfma_f32_32x32x16_bf16 v[0:15], v[108:111], v[76:79], v[0:15]\n" \
    "s_mov_b32 m0, s20\n" \
    "s_add_u32 s20, s20, 0x400\n" \
    "buffer_load_dwordx4 v142, %[srd], s21 offen lds\n" \
    "s_add_u32 s21, s21, 0x400\n" \
    "v_cvt_pk_bf16_f32 v134, v20, v21\n" \
    "v_exp_f32 v24, v24\n" \
    "v_exp_f32 v25, v25\n" \
    "v_cvt_pk_bf16_f32 v135, v22, v23\n" \
    "v_exp_f32 v26, v26\n" \
    "v_mfma_f32_32x32x16_bf16 v[32:47], v[100:103], v[128:131], v[32:47]\n" \
    "v_exp_f32 v27, v27\n" \
    "v_cvt_pk_bf16_f32 v136, v24, v25\n" \
    "v_exp_f32 v28, v28\n" \
    "v_exp_f32 v29, v29\n" \
    "v_cvt_pk_bf16_f32 v137, v26, v27\n" \
    "v_mfma_f32_16x16x32_bf16 v[64:67], v[120:123], v[128:131], v[64:67]\n" \
    "v_exp_f32 v30, v30\n" \
    "v_exp_f32 v31, v31\n" \
    "v_cvt_pk_bf16_f32 v138, v28, v29\n" \
    "v_cvt_pk_bf16_f32 v139, v30, v31\n" \
    "v_mfma_f32_32x32x16_bf16 v[16:31], v[104:107], v[80:83], 0\n" \
    "ds_read_b128 v[112:115], v140 offset:14336\n" \
    "v_exp_f32 v0, v0\n" \
    "v_exp_f32 v1, v1\n" \
    "v_exp_f32 v2, v2\n" \
    "v_mfma_f32_32x32x16_bf16 v[48:63], v[96:99], v[132:135], v[48:63]\n" \
    "ds_read_b128 v[116:119], v140 offset:15360\n" \
    "v_exp_f32 v3, v3\n" \
    "v_cvt_pk_bf16_f32 v124, v0, v1\n" \
    "v_exp_f32 v4, v4\n" \
    "v_exp_f32 v5, v5\n" \
    "v_mfma_f32_16x16x32_bf16 v[68:71], v[120:123], v[132:135], v[68:71]\n" \
    "v_cvt_pk_bf16_f32 v125, v2, v3\n" \
    "v_exp_f32 v6, v6\n" \
    "v_exp_f32 v7, v7\n" \
    "v_mfma_f32_32x32x16_bf16 v[16:31], v[108:111], v[84:87], v[16:31]\n" \
    "s_mov_b32 m0, s20\n" \
    "s_add_u32 s20, s20, 0x400\n" \
    "buffer_load_dwordx4 v142, %[srd], s21 offen lds\n" \
    "s_add_u32 s21, s21, 0x400\n" \
    "v_cvt_pk_bf16_f32 v126, v4, v5\n" \
    "v_exp_f32 v8, v8\n" \
    "v_exp_f32 v9, v9\n" \
    "v_cvt_pk_bf16_f32 v127, v6, v7\n" \
    "v_exp_f32 v10, v10\n" \
    "v_mfma_f32_32x32x16_bf16 v[48:63], v[100:103], v[136:139], v[48:63]\n" \
    "v_exp_f32 v11, v11\n" \
    "v_cvt_pk_bf16_f32 v128, v8, v9\n" \
    "v_exp_f32 v12, v12\n" \
    "v_exp_f32 v13, v13\n" \
    "v_cvt_pk_bf16_f32 v129, v10, v11\n" \
    "v_mfma_f32_16x16x32_bf16 v[68:71], v[120:123], v[136:139], v[68:71]\n" \
    "v_exp_f32 v14, v14\n" \
    "v_exp_f32 v15, v15\n" \
    "v_cvt_pk_bf16_f32 v130, v12, v13\n" \
    "v_cvt_pk_bf16_f32 v131, v14, v15\n" \
    "s_waitcnt lgkmcnt(0)\n" \
    "v_mfma_f32_32x32x16_bf16 v[0:15], v[88:91], v[72:75], 0\n" \
    "ds_read_b128 v[104:107], v141 offset:4096\n" \
    "v_exp_f32 v16, v16\n" \
    "v_exp_f32 v17, v17\n" \
    "v_exp_f32 v18, v18\n" \
    "v_mfma_f32_32x32x16_bf16 v[32:47], v[112:115], v[124:127], v[32:47]\n" \
    "ds_read_b128 v[108:111], v141 offset:5120\n" \
    "v_exp_f32 v19, v19\n" \
    "v_cvt_pk_bf16_f32 v132, v16, v17\n" \
    "v_exp_f32 v20, v20\n" \
    "v_exp_f32 v21, v21\n" \
    "v_mfma_f32_16x16x32_bf16 v[64:67], v[120:123], v[124:127], v[64:67]\n" \
    "v_cvt_pk_bf16_f32 v133, v18, v19\n" \
    "v_exp_f32 v22, v22\n" \
    "v_exp_f32 v23, v23\n" \
    "v_mfma_f32_32x32x16_bf16 v[0:15], v[92:95], v[76:79], v[0:15]\n" \
    "s_mov_b32 m0, s20\n" \
    "s_add_u32 s20, s20, 0x400\n" \
    "buffer_load_dwordx4 v142, %[srd], s21 offen lds\n" \
    "s_add_u32 s21, s21, 0x400\n" \
    "v_cvt_pk_bf16_f32 v134, v20, v21\n" \
    "v_exp_f32 v24, v24\n" \
    "v_exp_f32 v25, v25\n" \
    "v_cvt_pk_bf16_f32 v135, v22, v23\n" \
    "v_exp_f32 v26, v26\n" \
    "v_mfma_f32_32x32x16_bf16 v[32:47], v[116:119], v[128:131], v[32:47]\n" \
    "v_exp_f32 v27, v27\n" \
    "v_cvt_pk_bf16_f32 v136, v24, v25\n" \
    "v_exp_f32 v28, v28\n" \
    "v_exp_f32 v29, v29\n" \
    "v_cvt_pk_bf16_f32 v137, v26, v27\n" \
    "v_mfma_f32_16x16x32_bf16 v[64:67], v[120:123], v[128:131], v[64:67]\n" \
    "v_exp_f32 v30, v30\n" \
    "v_exp_f32 v31, v31\n" \
    "v_cvt_pk_bf16_f32 v138, v28, v29\n" \
    "v_cvt_pk_bf16_f32 v139, v30, v31\n" \
    "v_mov_b32 v140, v141\n" \
    "s_add_u32 s24, s24, 0x4000\n" \
    "s_and_b32 s24, s24, 0xffff\n" \
    "v_add_u32 v141, s24, v143\n" \
    "s_sub_u32 s23, s23, 1\n" \
    "s_cmp_lg_u32 s23, 0\n" \
    "s_cbranch_scc1 ATT_LOOP_%=\n" \
    "v_mfma_f32_32x32x16_bf16 v[48:63], v[112:115], v[132:135], v[48:63]\n" \
    "v_mfma_f32_16x16x32_bf16 v[68:71], v[120:123], v[132:135], v[68:71]\n" \
    "v_mfma_f32_32x32x16_bf16 v[48:63], v[116:119], v[136:139], v[48:63]\n" \
    "v_mfma_f32_16x16x32_bf16 v[68:71], v[120:123], v[136:139], v[68:71]\n" \
    "s_waitcnt vmcnt(0) lgkmcnt(0)\n" \
    "s_nop 15\n"

__device__ __forceinline__ void attention(int b, char* ws, lds_ptr lds, int w, int lane) {
    const int head = b >> 3, s0 = (b & 7) * 256 + w * 64;
    const u32x4 srd = make_srd(ws + WS_KV, S * 128);
    const u32 voff = w * 4096 + lane * 16, ldst = (u32)(uintptr_t)lds + w * 4096;
    auto dma = [&](int st, int p0, int p1) {
#pragma unroll
        for (int p = p0; p < p1; ++p) dma16(ldst + (st & 3) * KV_STAGE + p * 1024, voff, srd, st * KV_STAGE + p * 1024);
    };
    dma(0, 0, 4); dma(1, 0, 4); dma(2, 0, 3);
    u32x4 q[2][2];
#pragma unroll
    for (int i = 0; i < 2; ++i)
#pragma unroll
        for (int t = 0; t < 2; ++t) q[i][t] = *(const u32x4*)(ws + WS_QF + ((head * 64 + (s0 >> 5) + i) * 128 + t * 64 + lane) * 16);
    float gate[2];
#pragma unroll
    for (int i = 0; i < 2; ++i) gate[i] = ((const float*)(ws + WS_G))[s0 + i * 32 + (lane & 31)];
    const f32x2 kb2 = ((const f32x2*)(ws + WS_BND))[lane];
    const float k2 = wave_max(fmaxf(kb2[0], kb2[1]));
    bool ok = true;
#pragma unroll
    for (int i = 0; i < 2; ++i) {
        float n = 0.f;
#pragma unroll
        for (int t = 0; t < 2; ++t)
#pragma unroll
            for (int m = 0; m < 4; ++m) { const float a = bf_lo(q[i][t][m]), c = bf_hi(q[i][t][m]); n += a * a + c * c; }
        const auto h = __builtin_amdgcn_permlane32_swap(__float_as_uint(n), __float_as_uint(n), false, false);
        ok = ok && (__uint_as_float(h[0]) + __uint_as_float(h[1])) * k2 * 1.02f <= SAFE_LOG2 * SAFE_LOG2;
    }
    PIN(gate[0]); PIN(gate[1]);
    const bool safe = __all(ok);
    f32x16 o[2] = {f32x16{}, f32x16{}};
    float lsum[2];
    const lds_ptr base = lds + lane * 16;
    const f32x16 z = {};
    wait_bar<0>();
    STAMP(1, 1);
    if (safe) {
        f32x4 l0 = {}, l1 = {};
        const u32 one = (lane & 15) == ((lane >> 4) & 1) ? 0x3f803f80u : 0u;
        const u32x4 ones = {one, one, one, one};
        u32x4 k0 = lds_rd128(base), k1 = lds_rd128(base + 1024), n0 = lds_rd128(base + 4096), n1 = lds_rd128(base + 4096 + 1024);
        f32x16 s = M32(k0, q[0][0], z); s = M32(k1, q[0][1], s);
        u32x4 vz0 = {}, vz1 = {}, pz0 = {}, pz1 = {};
        u32 cur = (u32)(uintptr_t)base, nxt = cur + KV_STAGE;
        asm volatile("s_nop 11\n" ATT_LOOP_ASM
                     : "+{v[0:15]}"(s), "+{v[32:47]}"(o[0]), "+{v[48:63]}"(o[1]), "+{v[64:67]}"(l0), "+{v[68:71]}"(l1), "+{v[88:91]}"(k0), "+{v[92:95]}"(k1),
                       "+{v[104:107]}"(n0), "+{v[108:111]}"(n1), "+{v[112:115]}"(vz0), "+{v[116:119]}"(vz1), "+{v[132:135]}"(pz0), "+{v[136:139]}"(pz1),
                       "+{v140}"(cur), "+{v141}"(nxt)
                     : "{v[72:75]}"(q[0][0]), "{v[76:79]}"(q[0][1]), "{v[80:83]}"(q[1][0]), "{v[84:87]}"(q[1][1]), "{v[120:123]}"(ones), "{v142}"(voff),
                       "{v143}"((u32)(uintptr_t)base), [srd] "s"(srd), [ldst] "s"(ldst)
                     : "v16", "v17", "v18", "v19", "v20", "v21", "v22", "v23", "v24", "v25", "v26", "v27", "v28", "v29", "v30", "v31", "v96", "v97", "v98", "v99",
                       "v100", "v101", "v102", "v103", "v124", "v125", "v126", "v127", "v128", "v129", "v130", "v131", "s20", "s21", "s22", "s23", "s24", "scc", "memory");
        { const float a = __shfl(l0[0], lane & 15), c = __shfl(l0[1], lane & 15); lsum[0] = (lane & 16) ? c : a; }
        { const float a = __shfl(l1[0], lane & 15), c = __shfl(l1[1], lane & 15); lsum[1] = (lane & 16) ? c : a; }
    } else {
        float mx[2] = {-__builtin_inff(), -__builtin_inff()};
        lsum[0] = lsum[1] = 0.f;
        dma(2, 3, 4);
        for (int st = 0; st < KV_NSTAGE; ++st) {
            wait_bar<4>();
            dma(st + 3, 0, 4);
#pragma unroll 1
            for (int kb = 0; kb < 4; ++kb) {
                const lds_ptr p = base + (st & 3) * KV_STAGE + kb * 4096;
                const u32x4 k0 = lds_rd128(p), k1 = lds_rd128(p + 1024), v0 = lds_rd128(p + 2048), v1 = lds_rd128(p + 3072);
#pragma unroll
                for (int i = 0; i < 2; ++i) {
                    f32x16 s = M32(k0, q[i][0], z); s = M32(k1, q[i][1], s);
                    float m = mx[i];
#pragma unroll
                    for (int r = 0; r < 16; ++r) m = fmaxf(m, s[r]);
                    m = fmaxf(m, __shfl_xor(m, 32));
                    const float alpha = EX2(mx[i] - m);
                    mx[i] = m;
                    float e[16], t = 0.f;
#pragma unroll
                    for (int r = 0; r < 16; ++r) { e[r] = EX2(s[r] - m); t += e[r]; }
                    lsum[i] = lsum[i] * alpha + t;
                    o[i] *= alpha;
                    o[i] = M32(v0, (u32x4{cvtpk(e[0], e[1]), cvtpk(e[2], e[3]), cvtpk(e[4], e[5]), cvtpk(e[6], e[7])}), o[i]);
                    o[i] = M32(v1, (u32x4{cvtpk(e[8], e[9]), cvtpk(e[10], e[11]), cvtpk(e[12], e[13]), cvtpk(e[14], e[15])}), o[i]);
                }
            }
        }
        asm volatile("s_waitcnt vmcnt(0)" ::: "memory");
#pragma unroll
        for (int i = 0; i < 2; ++i) lsum[i] += __shfl_xor(lsum[i], 32);
    }
    STAMP(1, 6);
#pragma unroll
    for (int i = 0; i < 2; ++i) {
        const float r = gate[i] / lsum[i];
        const u32 dst = (u32)((head * 4 + (lane >> 5)) * S + s0 + i * 32 + (lane & 31)) * 16u;
        const f32x16 v = o[i] * r;
        store_wt(ws + WS_OF, S * D * 2, dst, u32x4{cvtpk(v[0], v[1]), cvtpk(v[2], v[3]), cvtpk(v[8], v[9]), cvtpk(v[10], v[11])});
        store_wt(ws + WS_OF, S * D * 2, dst + 2 * S * 16, u32x4{cvtpk(v[4], v[5]), cvtpk(v[6], v[7]), cvtpk(v[12], v[13]), cvtpk(v[14], v[15])});
    }
    publish(ws, CNT_O + (b & 7));
}

__device__ __forceinline__ float gelu_tanh(float x) {
    const float y2 = x * (-2.f * 0.7978845608028654f * 1.4426950408889634f) * (1.f + 0.044715f * x * x);
    return x * __builtin_amdgcn_rcpf(1.f + EX2(y2));
}

__device__ __forceinline__ void gemm1_tile(int b, const void* w1, const char* b1, char* ws, lds_ptr lds, int w, int lane) {
    const int x = b & 7, t = b >> 3;
    const int dt = (x & 1) * 4 + (t & 3), d0 = dt * 128, f0 = ((x >> 1) * 7 + (t >> 2)) * 128;
    const int g = lane >> 4, c = lane & 15;
    u32x2 bias[8];
#pragma unroll
    for (int j = 0; j < 8; ++j) bias[j] = *(const u32x2*)(b1 + (f0 + j * 16 + g * 4) * 2);
    OpReg<2, D, 1, false> a; a.init(ws + WS_HF, S * D * 2, d0 + w * 32, 0, lane);
    OpTr<128, F * 2, 1, false, 8> bb; bb.init(w1, S * F * 2, f0, 0, lds, w, 0, lane);
    Engine<G1_NS, 2, 8> e;
    gemm_simple<8192, S / 32, 0>(e, a, bb);
    auto& acc = e.acc;
    STAMP(1, 1);
#pragma unroll
    for (int jp = 0; jp < 4; ++jp)
#pragma unroll
        for (int i = 0; i < 2; ++i) {
            f32x4 u[2];
#pragma unroll
            for (int h = 0; h < 2; ++h) {
                const u32x2 bv = bias[2 * jp + h];
                const f32x4 v = acc[i][2 * jp + h] + f32x4{bf_lo(bv[0]), bf_hi(bv[0]), bf_lo(bv[1]), bf_hi(bv[1])};
                u[h] = f32x4{gelu_tanh(v[0]), gelu_tanh(v[1]), gelu_tanh(v[2]), gelu_tanh(v[3])};
            }
            store_wt(ws + WS_UF, F * D * 2, (u32)(((f0 / 32 + jp) * 4 + g) * D + d0 + w * 32 + i * 16 + c) * 16u, pack8(u[0], u[1]));
        }
    publish(ws, CNT_U + dt);
}

constexpr int POST_NS = 6, POST_SLOT = 12288;

__device__ __forceinline__ void post_tile(int b, const char* w2, const char* b2, const char* wo, char* ws, char* out, lds_ptr lds, int w, int lane) {
    const int x = b & 7, t = b >> 3;
    const int s0 = ((x >> 1) * 8 + (t & 7)) * 64, d0 = ((x & 1) * 4 + (t >> 3)) * 128;
    const int g = lane >> 4, c = lane & 15;
    OpReg<2, D, 1, true> a1; a1.init(ws + WS_UF, F * D * 2, d0 + w * 32, 0, lane);
    OpTr<64, S * 2, 1, false, 4> b1; b1.init(w2, F * S * 2, s0, 0, lds + 8192, w, 0, lane);
    OpTr<128, D * 2, 1, false, 2> a2; a2.init(wo, D * D * 2, d0, 0, lds, w, w * 2, lane);
    OpFrag<64, S, 1> bb2; bb2.init(ws + WS_OF, S * D * 2, s0, 0, lds + 8192, w, 0, lane);
    constexpr int NS = POST_NS, N1 = F / 32, N2 = D / 32, SW = N1 % NS, R1 = N1 / NS - 1, MID = 2 * NS, R2 = (SW + N2 - 2 * NS) / NS, TAIL = SW + N2 - NS - R2 * NS;
    static_assert(NS % 2 == 0 && SW > 0 && TAIL >= NS, "round structure");
    float omg[4], bs[4];
#pragma unroll
    for (int j = 0; j < 4; ++j) {
        const int s = s0 + j * 16 + c;
        asm volatile("global_load_dword %0, %1, off" : "=v"(omg[j]) : "v"(ws + WS_G + s * 4) : "memory");
        asm volatile("global_load_ushort %0, %1, off" : "=v"(bs[j]) : "v"(b2 + s * 2) : "memory");
    }
    Engine<NS, 2, 4> e;
    auto& acc = e.acc;
    auto none = [] {};
    auto u_ready = [&] { if (w == 0) await(ws, CNT_U + (d0 >> 7), F / 128); __builtin_amdgcn_s_barrier(); STAMP(1, 3); };
    static_assert(SW >= NS - 2, "the early sample completes before the gate");
    u32 seen = 0;
    auto o_ready = [&] { PIN(seen); if (__builtin_amdgcn_readfirstlane(seen) < 32u) await(ws, CNT_O + (s0 >> 8), 32); STAMP(1, 6); };
    auto mix = [&] {
#pragma unroll
        for (int i = 0; i < 2; ++i)
#pragma unroll
            for (int j = 0; j < 4; ++j) acc[i][j] = (acc[i][j] + bs[j]) * omg[j];
        SB();
    };
    gemm_start<POST_SLOT>(e, a1, b1, u_ready);
#pragma unroll
    for (int j = 0; j < 4; ++j) {
        PIN(omg[j]); PIN(bs[j]);
        omg[j] = bf_round(1.f - omg[j]); bs[j] = __uint_as_float(__float_as_uint(bs[j]) << 16);
    }
    for (int r = 0; r < R1; ++r) gemm_steps<0, NS, MANY, POST_SLOT, -1, -1, 2>(e, a1, b1, a1, b1, 0, (r + 1) * NS, none, none);
    STAMP(1, 4);
    asm volatile("global_load_dword %0, %1, off sc1" : "+v"(seen) : "v"(ws + WS_CNT + (CNT_O + (s0 >> 8)) * CNT_STRIDE) : "memory");
    gemm_steps<0, MID, MANY, POST_SLOT, NS + SW, SW, 2>(e, a1, b1, a2, bb2, N1 - SW, -SW, mix, o_ready);
    for (int r = 0; r < R2; ++r) gemm_steps<0, NS, MANY, POST_SLOT, -1, -1, 2>(e, a2, bb2, a2, bb2, 0, MID - SW + r * NS, none, none);
    gemm_steps<0, TAIL, TAIL, POST_SLOT, -1, -1, 2>(e, a2, bb2, a2, bb2, 0, MID - SW + R2 * NS, none, none);
    STAMP(1, 5);
#pragma unroll
    for (int j = 0; j < 4; ++j) {
        const auto r0 = __builtin_amdgcn_permlane16_swap(cvtpk(acc[0][j][0], acc[0][j][1]), cvtpk(acc[1][j][0], acc[1][j][1]), false, false);
        const auto r1 = __builtin_amdgcn_permlane16_swap(cvtpk(acc[0][j][2], acc[0][j][3]), cvtpk(acc[1][j][2], acc[1][j][3]), false, false);
        const int s = s0 + j * 16 + c, d = d0 + w * 32 + (g & 1) * 16 + (g >> 1) * 8;
        *(u32x4*)(out + ((size_t)s * D + d) * 2) = u32x4{r0[0], r1[0], r0[1], r1[1]};
    }
}

constexpr int MAIN_LDS = POST_NS * POST_SLOT;

__global__ void __launch_bounds__(256, 2) k_main(const char* __restrict__ w1, const char* __restrict__ b1, const char* __restrict__ w2, const char* __restrict__ b2,
                                                 const char* __restrict__ wo, char* __restrict__ ws, char* __restrict__ out) {
    extern __shared__ __attribute__((aligned(16))) char smem[];
    const int lane = threadIdx.x & 63, w = __builtin_amdgcn_readfirstlane(threadIdx.x >> 6), b = blockIdx.x;
    STAMP(1, 0);
    if (b < MID_ATT) {
        attention(b, ws, (lds_ptr)smem, w, lane);
    } else {
        const int gb = b - MID_ATT;
        __builtin_amdgcn_s_setprio(G1_PRIO);
        if (gb < MID_G1) gemm1_tile(gb, w1, b1, ws, (lds_ptr)smem, w, lane);
        else {
            const u32 scratch = (u32)(uintptr_t)(lds_ptr)smem + w * 256;
            for (int i = 0; i < 4; ++i) {
                touch<F * S * 2, MID_PF * 4, 256>(w2, (gb - MID_G1) * 4 + i, scratch);
                asm volatile("s_waitcnt vmcnt(0)" ::: "memory");
            }
            touch<D * D * 2, MID_PF, 256>(wo, gb - MID_G1, scratch);
            wait_bar<0>();
        }
        STAMP(1, 2);
        __builtin_amdgcn_s_setprio(POST_PRIO);
        post_tile(gb, w2, b2, wo, ws, out, (lds_ptr)smem, w, lane);
    }
    STAMP(1, 7);
}

extern "C" void kernel_launch(void* const* d_in, const int* in_sizes, int n_in, void* d_out, int out_size, void* d_ws, size_t ws_size,
                              hipStream_t stream) {
    const char* x = (const char*)d_in[0];
    const char *wq = (const char*)d_in[1], *wk = (const char*)d_in[2], *wv = (const char*)d_in[3], *wo = (const char*)d_in[4];
    const char *w1 = (const char*)d_in[6], *b1 = (const char*)d_in[7], *w2 = (const char*)d_in[8], *b2 = (const char*)d_in[9];
    const char* gw = (const char*)d_in[10];
    char* ws = (char*)d_ws;
    (void)hipFuncSetAttribute((const void*)k_pre, hipFuncAttributeMaxDynamicSharedMemorySize, PRE_LDS);
    (void)hipFuncSetAttribute((const void*)k_main, hipFuncAttributeMaxDynamicSharedMemorySize, MAIN_LDS);
    k_pre<<<PRE_Q + PRE_KV + PRE_LN + PRE_PF, 512, PRE_LDS, stream>>>(x, wq, wk, wv, gw, w1, ws);
    k_main<<<MID_ATT + MID_G1 + MID_PF, 256, MAIN_LDS, stream>>>(w1, b1, w2, b2, wo, ws, (char*)d_out);
}
```

```cpp
#include <hip/hip_runtime.h>
#include <stdint.h>

typedef unsigned u32;
typedef u32 u32x4 __attribute__((ext_vector_type(4)));
typedef u32 u32x2 __attribute__((ext_vector_type(2)));
typedef float f32x2 __attribute__((ext_vector_type(2)));
typedef float f32x4 __attribute__((ext_vector_type(4)));
typedef float f32x16 __attribute__((ext_vector_type(16)));
typedef __bf16 bf16x2 __attribute__((ext_vector_type(2)));
typedef __bf16 bf16x8 __attribute__((ext_vector_type(8)));
typedef short s16x4 __attribute__((ext_vector_type(4)));
#define LDS __attribute__((address_space(3)))
typedef LDS char* lds_ptr;

constexpr int S = 2048, D = 1024, F = 3584;
constexpr u32 WS_HF = 0, WS_OF = 8u << 20, WS_UF = 12u << 20, WS_KV = 20u << 20;
constexpr u32 WS_G = WS_KV + (256u << 10), WS_BND = WS_G + (8u << 10), WS_CNT = WS_BND + (4u << 10);
constexpr u32 WS_X = 24u << 20;
constexpr int CNT_U = 0, CNT_O = 16, CNT_X = 32, CNT_N = 288, CNT_STRIDE = 256;
constexpr float QSCALE = 0.17677669529663687f * 1.4426950408889634f;
constexpr float SAFE_LOG2 = 60.f;
constexpr float SAFE_V = 1.0e12f;

#define SB() __builtin_amdgcn_sched_barrier(0)
#define PIN(x) asm volatile("" : "+v"(x))
#define M16(a, b, c) __builtin_amdgcn_mfma_f32_16x16x32_bf16(__builtin_bit_cast(bf16x8, a), __builtin_bit_cast(bf16x8, b), c, 0, 0, 0)
#define M32(a, b, c) __builtin_amdgcn_mfma_f32_32x32x16_bf16(__builtin_bit_cast(bf16x8, a), __builtin_bit_cast(bf16x8, b), c, 0, 0, 0)
__device__ __forceinline__ void mma16(f32x4& c, const u32x4& a, const u32x4& b) { asm volatile("v_mfma_f32_16x16x32_bf16 %0, %1, %2, %0" : "+v"(c) : "v"(a), "v"(b)); }
__device__ __forceinline__ void mfma_settle() { asm volatile("s_nop 7\n\ts_nop 1" ::: "memory"); SB(); }
#define EX2(x) __builtin_amdgcn_exp2f(x)

__device__ __forceinline__ u32 cvtpk(float lo, float hi) { f32x2 v = {lo, hi}; return __builtin_bit_cast(u32, __builtin_convertvector(v, bf16x2)); }
__device__ __forceinline__ float bf_lo(u32 v) { return __uint_as_float(v << 16); }
__device__ __forceinline__ float bf_hi(u32 v) { return __uint_as_float(v & 0xffff0000u); }
__device__ __forceinline__ float bf_round(float x) { return bf_lo(cvtpk(x, x)); }
__device__ __forceinline__ u32x4 pack8(const f32x4& a, const f32x4& b) { return u32x4{cvtpk(a[0], a[1]), cvtpk(a[2], a[3]), cvtpk(b[0], b[1]), cvtpk(b[2], b[3])}; }

__device__ __forceinline__ u32x4 make_srd(const void* p, u32 bytes) {
    const uint64_t a = (uint64_t)p;
    return u32x4{(u32)a, (u32)(a >> 32) & 0xffffu, bytes, 0x00020000u};
}
__device__ __forceinline__ void dma16(u32 lds, u32 voff, const u32x4& srd, u32 soff) {
    asm volatile("s_mov_b32 m0, %0\n\ts_nop 0\n\tbuffer_load_dwordx4 %1, %2, %3 offen lds" ::"s"(lds), "v"(voff), "s"(srd), "s"(soff) : "memory");
}
__device__ __forceinline__ void dma16_coh(u32 lds, u32 voff, const u32x4& srd, u32 soff) {
    asm volatile("s_mov_b32 m0, %0\n\ts_nop 0\n\tbuffer_load_dwordx4 %1, %2, %3 offen sc1 lds" ::"s"(lds), "v"(voff), "s"(srd), "s"(soff) : "memory");
}
template <int N> __device__ __forceinline__ void wait_bar() {
    __builtin_amdgcn_s_waitcnt(0xc07f);
    asm volatile("s_waitcnt vmcnt(%0)\n\ts_barrier" ::"n"(N) : "memory");
}
__device__ __forceinline__ u32x4 lds_rd128(lds_ptr p) { return *(LDS const u32x4*)p; }
__device__ __forceinline__ u32x2 lds_rdtr(lds_ptr p) { return __builtin_bit_cast(u32x2, __builtin_amdgcn_ds_read_tr16_b64_v4i16((LDS s16x4*)p)); }

typedef __attribute__((address_space(1))) u32 gu32;
__device__ __forceinline__ void store_wt(char* base, u32 bytes, u32 off, const u32x4& v) {
    __builtin_amdgcn_raw_buffer_store_b128(v, __builtin_amdgcn_make_buffer_rsrc(base, 0, bytes, 0x00020000), off, 0, 16);
}
__device__ __forceinline__ void load_coh(u32x4& f, u32 voff, const u32x4& srd, u32 soff) {
    asm volatile("buffer_load_dwordx4 %0, %1, %2, %3 offen sc1" : "+v"(f) : "v"(voff), "s"(srd), "s"(soff) : "memory");
}
__device__ __forceinline__ void signal(char* ws, int cnt) {
    if (threadIdx.x == 0) __hip_atomic_fetch_add((gu32*)(ws + WS_CNT + cnt * CNT_STRIDE), 1u, __ATOMIC_RELAXED, __HIP_MEMORY_SCOPE_AGENT);
}
__device__ __forceinline__ void publish(char* ws, int cnt) {
    asm volatile("s_waitcnt vmcnt(0)\n\ts_barrier" ::: "memory");
    signal(ws, cnt);
}
__device__ __forceinline__ void await(char* ws, int cnt, u32 target) {
    gu32* p = (gu32*)(ws + WS_CNT + cnt * CNT_STRIDE);
    for (int spins = 0; spins < (1 << 20); ++spins) {
        if (__builtin_amdgcn_readfirstlane(__hip_atomic_load(p, __ATOMIC_RELAXED, __HIP_MEMORY_SCOPE_AGENT)) >= target) break;
        __builtin_amdgcn_s_sleep(1);
    }
    asm volatile("" ::: "memory");
}

__device__ __forceinline__ void sample(u32& seen, char* ws, int cnt) {
    asm volatile("global_load_dword %0, %1, off sc1" : "+v"(seen) : "v"(ws + WS_CNT + cnt * CNT_STRIDE) : "memory");
}
__device__ __forceinline__ void confirm(u32& seen, char* ws, int cnt, u32 target) {
    PIN(seen);
    if (__builtin_amdgcn_readfirstlane(seen) < target) await(ws, cnt, target);
}

#define STAMP(k, i)

template <int R, int NR, int KSTR> struct OpFrag {
    static constexpr int BYTES = 4 * R * 16, PPW = BYTES / 4096, RPF = 1;
    static constexpr bool REG = false;
    u32x4 srd; u32 voff, ldst; lds_ptr rd;
    __device__ __forceinline__ void init(const void* base, u32 bytes, int row0, int k0, lds_ptr img, int w, int wrow, int lane) {
        srd = make_srd(base, bytes);
        voff = (u32)(row0 + lane + (w + 4 * k0) * NR) * 16u;
        ldst = (u32)(uintptr_t)img + w * (R * 16);
        rd = img + ((lane >> 4) * R + wrow + (lane & 15)) * 16;
    }
    __device__ __forceinline__ void issue(int slot, u32 pos, int i) const { dma16_coh(ldst + pos + i * 1024, voff, srd, (u32)slot * (KSTR * 4u * NR * 16u) + i * 1024u); }
    __device__ __forceinline__ void read(u32x4& f, int pos, int i, int) const { f = lds_rd128(rd + pos + i * 256); }
};

template <int C, int RS, int KS, bool NAT, int NF> struct OpTr {
    static constexpr int BYTES = KS * 32 * C * 2, PPW = BYTES / 4096, RPF = 2;
    static constexpr bool REG = false;
    u32x4 srd; u32 voff, ldst; lds_ptr rd[NF];
    __device__ __forceinline__ void init(const void* base, u32 bytes, int col0, int k0, lds_ptr img, int w, int wch, int lane) {
        srd = make_srd(base, bytes);
        ldst = (u32)(uintptr_t)img + w * 1024;
        const int g = lane >> 4, q = (lane >> 2) & 3, p = lane & 3;
        if constexpr (C == 128) {
            const int f = NAT ? (g | (((w >> 1) & 1) << 2)) : (4 * (w & 1) + g);
            voff = (u32)(32 * k0 + 4 * w + g) * RS + col0 * 2 + ((((lane & 15) >> 1) ^ f) * 32) + (lane & 1) * 16;
            const int row = NAT ? 8 * g + q : 4 * g + q, fr = NAT ? (q | ((g & 1) << 2)) : (row & 7);
#pragma unroll
            for (int i = 0; i < NF; ++i) rd[i] = img + row * 256 + (((wch ^ i) ^ fr) * 32) + p * 8;
        } else {
            static_assert(C == 128 || C == 64, "unsupported transposed operand");
            const int f = NAT ? ((g & 1) | ((w & 1) << 1)) : g;
            voff = (u32)(32 * k0 + 8 * w + (lane >> 3)) * RS + col0 * 2 + ((((lane & 7) >> 1) ^ f) * 32) + (lane & 1) * 16;
            const int row = NAT ? 8 * g + q : 4 * g + q, fr = NAT ? ((q >> 1) | ((g & 1) << 1)) : ((row >> 1) & 3);
#pragma unroll
            for (int i = 0; i < NF; ++i) rd[i] = img + row * 128 + (((wch ^ i) ^ fr) * 32) + p * 8;
        }
    }
    __device__ __forceinline__ void issue(int slot, u32 pos, int i) const { dma16(ldst + pos + i * 4096, voff, srd, (u32)(slot * KS * 32 + i * (2048 / C)) * RS); }
    __device__ __forceinline__ void read(u32x4& f, int pos, int i, int t) const {
        const u32x2 v = lds_rdtr(rd[i % NF] + pos + i / NF * (32 * C * 2) + t * ((NAT ? 4 : 16) * C * 2));
        f[2 * t] = v[0]; f[2 * t + 1] = v[1];
    }
};

struct OpRow {
    static constexpr int BYTES = 128 * 128, PPW = 4, RPF = 1;
    static constexpr bool REG = false;
    u32x4 srd; u32 voff, ldst; lds_ptr rd[2];
    __device__ __forceinline__ void init(const void* base, u32 bytes, int row0, lds_ptr img, int w, int wrow, int lane) {
        srd = make_srd(base, bytes);
        ldst = (u32)(uintptr_t)img + w * 1024;
        const int g = lane >> 4, c = lane & 15;
        voff = (u32)(row0 + 8 * w + (lane >> 3)) * (D * 2) + (((lane & 7) ^ (4 * (w & 1) + g)) * 16);
#pragma unroll
        for (int ks = 0; ks < 2; ++ks) rd[ks] = img + (wrow + c) * 128 + (((4 * ks + g) ^ (c >> 1)) * 16);
    }
    __device__ __forceinline__ void issue(int slot, u32 pos, int i) const { dma16(ldst + pos + i * 4096, voff, srd, (u32)slot * 128u + i * (32u * D * 2u)); }
    __device__ __forceinline__ void read(u32x4& f, int pos, int i, int) const { f = lds_rd128(rd[i >> 2] + pos + (i & 3) * 2048); }
};

template <int NF, int NR, int KSTR, bool COH> struct OpReg {
    static constexpr int PPW = NF, RPF = 0;
    static constexpr bool REG = true;
    u32x4 srd; u32 voff;
    __device__ __forceinline__ void init(const void* base, u32 bytes, int row0, int k0, int lane) {
        srd = make_srd(base, bytes);
        voff = (u32)(row0 + (lane & 15) + ((lane >> 4) + 4 * k0) * NR) * 16u;
    }
    __device__ __forceinline__ void load(u32x4& f, int slot, int i) const {
        const u32 soff = (u32)slot * (KSTR * 4u * NR * 16u);
        if (COH) asm volatile("buffer_load_dwordx4 %0, %1, %2, %3 offen offset:%4 sc1" : "+v"(f) : "v"(voff), "s"(srd), "s"(soff), "n"(i * 256) : "memory");
        else asm volatile("buffer_load_dwordx4 %0, %1, %2, %3 offen offset:%4" : "+v"(f) : "v"(voff), "s"(srd), "s"(soff), "n"(i * 256) : "memory");
    }
};

template <int NA, int NB> struct Frags { u32x4 a[NA], b[NB]; };
template <int NS, int NA, int NB, int KS = 1> struct Engine { f32x4 acc[NA][NB]; Frags<NA * KS, NB * KS> f[2]; u32x4 ring[NS][NA]; };

#define STAG 1
#define RDX 2
template <int KS, int SWAP, bool READ, bool ISSUE, bool RISSUE, int NA, int NB, class A, class B>
__device__ __forceinline__ void gemm_step(f32x4 (&acc)[NA][NB], const Frags<NA * KS, NB * KS>& cur, Frags<NA * KS, NB * KS>& nxt, u32x4 (&ring)[NA],
                                          u32x4 (&prev)[NA], const A& a, const B& b, int rpos, int islot, u32 ipos) {
    constexpr int NM = NA * NB * KS, NRA = NA * A::RPF, NRK = NRA + NB * B::RPF, NRD = NRK * KS, RPM = (RDX * NRD + NM - 1) / NM;
    constexpr int ND = A::PPW + B::PPW, DSTEP = NM / ND, DOFF = STAG % DSTEP;
    if constexpr (A::REG) {
#pragma unroll
        for (int i = 0; i < NA; ++i) PIN(ring[i]);
    }
#pragma unroll
    for (int m = 0; m < NM; ++m) {
        const int i = m % NA, j = m / NA % NB, ks = m / (NA * NB);
        const u32x4 av = A::REG ? ring[i] : cur.a[ks * NA + i];
        if (i >= SWAP) mma16(acc[i][j], cur.b[ks * NB + j], av);
        else mma16(acc[i][j], av, cur.b[ks * NB + j]);
        if constexpr (READ) {
#pragma unroll
            for (int rr = m * RPM; rr < (m + 1) * RPM && rr < NRD; ++rr) {
                const int k = rr / NRK, r = rr % NRK;
                if (r < B::RPF) b.read(nxt.b[k * NB], rpos, k * NB, r);
                else if (r >= B::RPF + NRA) b.read(nxt.b[k * NB + (r - NRA) / B::RPF], rpos, k * NB + (r - NRA) / B::RPF, (r - NRA) % B::RPF);
                else if constexpr (!A::REG) a.read(nxt.a[k * NA + (r - B::RPF) / A::RPF], rpos, k * NA + (r - B::RPF) / A::RPF, (r - B::RPF) % A::RPF);
            }
        }
        if (m % DSTEP == DOFF && m / DSTEP < ND) {
            const int d = m / DSTEP;
            if (d < B::PPW) { if constexpr (ISSUE) b.issue(islot, ipos, d); }
            else if constexpr (A::REG) { if constexpr (RISSUE) a.load(prev[d - B::PPW], islot - 1, d - B::PPW); }
            else if constexpr (ISSUE) a.issue(islot, ipos, d - B::PPW);
        }
        SB();
    }
}

template <int V> struct IC { static constexpr int v = V; };
template <int NS, int PA, int PB, bool REG> constexpr int younger(int rem) {
    if (rem == 2) return 0;
    int n = 0;
    for (int k = 1; k <= NS - 2; ++k) n += (rem + k > NS ? PB + (REG ? 0 : PA) : 0) + (REG && rem + k > NS - 1 ? PA : 0);
    return n;
}
template <int P, int COUNT, int REM, int SLOTB, int SWAP, int XW, int NS, int NA, int NB, int KS, class A, class B, class Hook>
__device__ __forceinline__ void gemm_steps(Engine<NS, NA, NB, KS>& e, const A& a, const B& b, int islot, const Hook& hook) {
    constexpr bool READ = REM > 1;
    if constexpr (READ) wait_bar<younger<NS, A::PPW, B::PPW, A::REG>(REM) + (P < NS - 2 ? XW : 0)>();
    hook(IC<P>{});
    gemm_step<KS, SWAP, READ, (REM > NS), (A::REG && REM > NS - 1)>(e.acc, e.f[P & 1], e.f[(P & 1) ^ 1], e.ring[P % NS], e.ring[(P + NS - 1) % NS], a, b,
                                                                   (P + 1) % NS * SLOTB, islot + P, P % NS * SLOTB);
    if constexpr (COUNT > 1) gemm_steps<P + 1, COUNT - 1, REM - 1, SLOTB, SWAP, XW>(e, a, b, islot, hook);
    else if constexpr (REM == 1) mfma_settle();
}

template <int SLOTB, class A, class B> __device__ __forceinline__ void gemm_fill(const A& a, const B& b, int p0, int p1) {
#pragma unroll
    for (int p = p0; p < p1; ++p) {
#pragma unroll
        for (int i = 0; i < B::PPW; ++i) b.issue(p, p * SLOTB, i);
        if constexpr (!A::REG) {
#pragma unroll
            for (int i = 0; i < A::PPW; ++i) a.issue(p, p * SLOTB, i);
        }
    }
}
template <int NS, int NA, int NB, int KS, class A, class B> __device__ __forceinline__ void gemm_first(Engine<NS, NA, NB, KS>& e, const A& a, const B& b) {
    if constexpr (!A::REG) {
#pragma unroll
        for (int i = 0; i < KS * NA * A::RPF; ++i) a.read(e.f[0].a[i / A::RPF], 0, i / A::RPF, i % A::RPF);
    }
#pragma unroll
    for (int i = 0; i < KS * NB * B::RPF; ++i) b.read(e.f[0].b[i / B::RPF], 0, i / B::RPF, i % B::RPF);
    SB();
}

constexpr int MANY = 1 << 20;
template <int NS, int NSLOTS> struct Rounds { static constexpr int RL = NS % 2 ? 2 * NS : NS, N = (NSLOTS - NS) / RL, TAIL = NSLOTS - N * RL; };
template <int SLOTB, int NSLOTS, int SWAP, int NS, int NA, int NB, int KS, class A, class B, class Gate, class Round, class Tail>
__device__ __forceinline__ void gemm_run(Engine<NS, NA, NB, KS>& e, const A& a, const B& b, const Gate& gate, const Round& round, const Tail& tail) {
    using R = Rounds<NS, NSLOTS>;
#pragma unroll
    for (int i = 0; i < NA; ++i)
#pragma unroll
        for (int j = 0; j < NB; ++j) e.acc[i][j] = f32x4{};
    gemm_fill<SLOTB>(a, b, 0, NS);
    if constexpr (A::REG) {
        gate();
#pragma unroll
        for (int p = 0; p < NS; ++p)
#pragma unroll
            for (int i = 0; i < NA; ++i) { e.ring[p][i] = u32x4{}; if (p < NS - 1) a.load(e.ring[p][i], p, i); }
        wait_bar<NA>();
    } else {
        wait_bar<(NS - 1) * (A::PPW + B::PPW)>();
    }
    gemm_first(e, a, b);
    for (int r = 0; r < R::N; ++r) { round(r); gemm_steps<0, R::RL, MANY, SLOTB, SWAP, 0>(e, a, b, r * R::RL + NS, [](auto) {}); }
    gemm_steps<0, R::TAIL, R::TAIL, SLOTB, SWAP, 0>(e, a, b, R::N * R::RL + NS, tail);
}

constexpr int PRE_KV = 64, PRE_LN = 128, PRE_PF = 64, PRE_LDS = 8 * 16384 + 2048;

template <int CTRL> __device__ __forceinline__ float dpp_add(float v) {
    return v + __builtin_bit_cast(float, __builtin_amdgcn_update_dpp(0, __builtin_bit_cast(int, v), CTRL, 0xf, 0xf, true));
}
__device__ __forceinline__ float group_sum(float v) {
    const auto a = __builtin_amdgcn_permlane16_swap(__float_as_uint(v), __float_as_uint(v), false, false);
    v = __uint_as_float(a[0]) + __uint_as_float(a[1]);
    const auto b = __builtin_amdgcn_permlane32_swap(__float_as_uint(v), __float_as_uint(v), false, false);
    return __uint_as_float(b[0]) + __uint_as_float(b[1]);
}

__device__ __forceinline__ float row_sum(float v) { return dpp_add<0x140>(dpp_add<0x141>(dpp_add<0x4e>(dpp_add<0xb1>(v)))); }
__device__ __forceinline__ float fold32(float a, float b) {
    const auto r = __builtin_amdgcn_permlane32_swap(__float_as_uint(a), __float_as_uint(b), false, false);
    return __uint_as_float(r[0]) + __uint_as_float(r[1]);
}
__device__ __forceinline__ float fold16(float a, float b) {
    const auto r = __builtin_amdgcn_permlane16_swap(__float_as_uint(a), __float_as_uint(b), false, false);
    return __uint_as_float(r[0]) + __uint_as_float(r[1]);
}
template <int CTRL> __device__ __forceinline__ float dpp_max(float v) {
    return fmaxf(v, __builtin_bit_cast(float, __builtin_amdgcn_update_dpp(0, __builtin_bit_cast(int, v), CTRL, 0xf, 0xf, true)));
}
__device__ __forceinline__ float wave_max(float v) {
    v = dpp_max<0xb1>(v); v = dpp_max<0x4e>(v); v = dpp_max<0x141>(v); v = dpp_max<0x140>(v);
    const auto a = __builtin_amdgcn_permlane16_swap(__float_as_uint(v), __float_as_uint(v), false, false);
    v = fmaxf(__uint_as_float(a[0]), __uint_as_float(a[1]));
    const auto b = __builtin_amdgcn_permlane32_swap(__float_as_uint(v), __float_as_uint(v), false, false);
    return fmaxf(__uint_as_float(b[0]), __uint_as_float(b[1]));
}

template <u32 BYTES, int NB, int NTHR> __device__ __forceinline__ void touch(const char* p, int b, u32 scratch) {
    constexpr u32 LINES = BYTES / 128, PER = (LINES + NB - 1) / NB, N = (PER + NTHR - 1) / NTHR;
    const u32x4 srd = make_srd(p, BYTES);
#pragma unroll
    for (u32 i = 0; i < N; ++i) {
        const u32 l = i * NTHR + threadIdx.x, off = l < PER ? (b * PER + l) * 128u : 0x80000000u;
        asm volatile("s_mov_b32 m0, %0\n\ts_nop 0\n\tbuffer_load_dword %1, %2, 0 offen lds" ::"s"(scratch), "v"(off), "s"(srd) : "memory");
    }
}

__device__ __forceinline__ void warm_pre(const char* wq, const char* w1, int b, u32 scratch) {
    touch<D * D * 2, PRE_LN + PRE_PF, 512>(wq, b, scratch);
    touch<S * F * 2, PRE_LN + PRE_PF, 512>(w1, b, scratch);
}

__device__ __forceinline__ void kv_block(int t, const char* x, const char* wk, const char* wv, char* ws, lds_ptr lds, int w8, int lane) {
    const int g = lane >> 4, c = lane & 15, q = (lane >> 2) & 3, p = lane & 3;
    const lds_ptr img = lds + w8 * 16384;
    const u32x4 sk = make_srd(wk, D * 64), sv = make_srd(wv, D * 64);
    const u32 voff = (u32)(lane >> 2) * 64 + ((((lane >> 1) & 1) ^ (lane >> 5)) * 32) + (lane & 1) * 16;
    u32x4 xf[2][4];
#pragma unroll
    for (int ks = 0; ks < 4; ++ks)
#pragma unroll
        for (int j = 0; j < 2; ++j) xf[j][ks] = *(const u32x4*)(x + (size_t)(t * 32 + j * 16 + c) * (D * 2) + (w8 * 128 + ks * 32 + g * 8) * 2);
#pragma unroll
    for (int i = 0; i < 8; ++i) {
        dma16((u32)(uintptr_t)img + i * 1024, voff, sk, w8 * 8192 + i * 1024);
        dma16((u32)(uintptr_t)img + 8192 + i * 1024, voff, sv, w8 * 8192 + i * 1024);
    }
    asm volatile("s_waitcnt vmcnt(0)" ::: "memory");
    f32x4 acc[8];
#pragma unroll
    for (int i = 0; i < 8; ++i) acc[i] = f32x4{};
#pragma unroll
    for (int ks = 0; ks < 4; ++ks)
#pragma unroll
        for (int i = 0; i < 4; ++i) {
            const lds_ptr rd = img + (i >> 1) * 8192 + (ks * 32 + 8 * g + q) * 64 + (((i & 1) ^ (g & 1)) * 32) + p * 8;
            const u32x2 lo = lds_rdtr(rd), hi = lds_rdtr(rd + 256);
            const u32x4 wf = {lo[0], lo[1], hi[0], hi[1]};
#pragma unroll
            for (int j = 0; j < 2; ++j) acc[2 * i + j] = i < 2 ? M16(wf, xf[j][ks], acc[2 * i + j]) : M16(xf[j][ks], wf, acc[2 * i + j]);
        }
#pragma unroll
    for (int i = 0; i < 8; ++i) *(LDS f32x4*)(img + i * 1024 + lane * 16) = acc[i];
    wait_bar<0>();
    STAMP(0, 6);
    if (w8 >= 4) return;
    const int ta = w8 < 2 ? w8 : 2 * w8, tb = w8 < 2 ? w8 + 2 : 2 * w8 + 1;
    f32x4 a = {}, b = {};
#pragma unroll
    for (int v = 0; v < 8; ++v) { a += *(LDS const f32x4*)(lds + v * 16384 + ta * 1024 + lane * 16); b += *(LDS const f32x4*)(lds + v * 16384 + tb * 1024 + lane * 16); }
    *(u32x4*)(ws + WS_KV + t * 4096 + w8 * 1024 + lane * 16) = pack8(a, b);
    float bnd = 0.f;
    if (w8 < 2) {
#pragma unroll
        for (int r = 0; r < 4; ++r) bnd += a[r] * a[r] + b[r] * b[r];
        bnd = group_sum(bnd);
        bnd = bnd == bnd ? bnd : __builtin_inff();
    } else {
#pragma unroll
        for (int r = 0; r < 4; ++r)
            if (!(fabsf(a[r]) <= SAFE_V && fabsf(b[r]) <= SAFE_V)) bnd = __builtin_inff();
    }
    bnd = wave_max(bnd);
    if (lane == 0) ((float*)(ws + WS_BND))[t * 4 + w8] = bnd;
}

__device__ __forceinline__ void ln_rows(int r, const char* x, const char* gw, const char* wq, const char* w1, char* ws, lds_ptr lds, u32 scratch, int w8, int lane) {
    const int u = w8 >> 2, cq = w8 & 3, chunk = 2 * r + u, row0 = 32 * (chunk >> 2) + 4 * (chunk & 3);
    u32x2 xv[8];
    float sh[8];
#pragma unroll
    for (int e = 0; e < 8; ++e) {
        const char* row = x + (size_t)(row0 + (e & 3) + 16 * (e >> 2)) * (D * 2);
        xv[e] = *(const u32x2*)(row + cq * 512 + lane * 8);
        sh[e] = bf_lo(*(const u32*)row);
    }
    u32x2 gv = *(const u32x2*)(gw + cq * 512 + lane * 8);
    PIN(gv);
#pragma unroll
    for (int e = 0; e < 8; ++e) PIN(xv[e]);
    warm_pre(wq, w1, r, scratch);
    float part[3][8];
#pragma unroll
    for (int e = 0; e < 8; ++e) {
        const float a0 = bf_lo(xv[e][0]), a1 = bf_hi(xv[e][0]), a2 = bf_lo(xv[e][1]), a3 = bf_hi(xv[e][1]);
        const float d0 = a0 - sh[e], d1 = a1 - sh[e], d2 = a2 - sh[e], d3 = a3 - sh[e];
        part[0][e] = (d0 + d1) + (d2 + d3);
        part[1][e] = (d0 * d0 + d1 * d1) + (d2 * d2 + d3 * d3);
        part[2][e] = (a0 * bf_lo(gv[0]) + a1 * bf_hi(gv[0])) + (a2 * bf_lo(gv[1]) + a3 * bf_hi(gv[1]));
    }
    LDS float* red = (LDS float*)(lds + 65536);
#pragma unroll
    for (int k = 0; k < 3; ++k)
#pragma unroll
        for (int h = 0; h < 2; ++h) {
            const float v = row_sum(fold16(fold32(part[k][4 * h], part[k][4 * h + 2]), fold32(part[k][4 * h + 1], part[k][4 * h + 3])));
            if ((lane & 15) == 0) red[w8 * 24 + k * 8 + 4 * h + (lane >> 4)] = v;
        }
    wait_bar<63>();
    STAMP(0, 6);
    const int e8 = lane & 7;
    float st[3], mysh = 0.f;
#pragma unroll
    for (int k = 0; k < 3; ++k) st[k] = (red[(4 * u) * 24 + k * 8 + e8] + red[(4 * u + 1) * 24 + k * 8 + e8]) + (red[(4 * u + 2) * 24 + k * 8 + e8] + red[(4 * u + 3) * 24 + k * 8 + e8]);
#pragma unroll
    for (int e = 0; e < 8; ++e) mysh = e8 == e ? sh[e] : mysh;
    const float dm = st[0] * (1.f / D), var = fmaxf(st[1] * (1.f / D) - dm * dm, 0.f);
    const float mean = bf_round(mysh + dm), rs = 1.f / bf_round(sqrtf(bf_round(bf_round(var) + 1e-5f)));
    if (cq == 0 && lane < 8) ((float*)(ws + WS_G))[row0 + (lane & 3) + 16 * (lane >> 2)] = bf_round(1.f / (1.f + __expf(-bf_round(st[2]))));
    float h[4][8];
#pragma unroll
    for (int e = 0; e < 8; ++e) {
        const float m = __uint_as_float(__builtin_amdgcn_readlane(__float_as_uint(mean), e)), q = __uint_as_float(__builtin_amdgcn_readlane(__float_as_uint(rs), e));
        h[0][e] = (bf_lo(xv[e][0]) - m) * q; h[1][e] = (bf_hi(xv[e][0]) - m) * q; h[2][e] = (bf_lo(xv[e][1]) - m) * q; h[3][e] = (bf_hi(xv[e][1]) - m) * q;
    }
    const lds_ptr tb = lds + w8 * 4096;
#pragma unroll
    for (int m = 0; m < 4; ++m)
        *(LDS u32x4*)(tb + lane * 64 + ((m ^ ((lane >> 1) & 3)) * 16)) = u32x4{cvtpk(h[m][0], h[m][1]), cvtpk(h[m][2], h[m][3]), cvtpk(h[m][4], h[m][5]), cvtpk(h[m][6], h[m][7])};
    char* dst = ws + WS_HF + (size_t)chunk * (D * 16) + cq * 4096 + lane * 16;
#pragma unroll
    for (int i = 0; i < 4; ++i) *(u32x4*)(dst + i * 1024) = lds_rd128(tb + (i * 16 + (lane >> 2)) * 64 + (((lane & 3) ^ ((lane >> 3) & 3)) * 16));
}

__global__ void __launch_bounds__(512, 2) k_pre(const char* __restrict__ x, const char* __restrict__ wq, const char* __restrict__ wk, const char* __restrict__ wv,
                                                const char* __restrict__ gw, const char* __restrict__ w1, char* __restrict__ ws) {
    extern __shared__ __attribute__((aligned(16))) char smem[];
    const int lane = threadIdx.x & 63, w8 = __builtin_amdgcn_readfirstlane(threadIdx.x >> 6), b = blockIdx.x;
    STAMP(0, 0);
    if (b < PRE_KV) kv_block(b, x, wk, wv, ws, (lds_ptr)smem, w8, lane);
    else {
        const int r = b - PRE_KV;
        const u32 scratch = (u32)(uintptr_t)(lds_ptr)smem + 8 * 16384 + w8 * 256;
        if (r < PRE_LN) ln_rows(r, x, gw, wq, w1, ws, (lds_ptr)smem, scratch, w8, lane);
        else {
            warm_pre(wq, w1, r, scratch);
            if (r == PRE_LN && threadIdx.x < CNT_N) *(u32*)(ws + WS_CNT + threadIdx.x * CNT_STRIDE) = 0u;
        }
        asm volatile("s_waitcnt vmcnt(0)" ::: "memory");
    }
    STAMP(0, 7);
}

#define G1_PRIO 1
#define G1_NS_ 6
#define POST_PRIO 0
#define G_SLEEP 0
#define PF_SLEEP 64
#define Q_PRIO 2
#define PRIO_SWITCH 99
#define ATT_PRIO 0
constexpr int MID_ATT = 256, MID_G1 = 224, MID_PF = 32, G1_NS = G1_NS_;
constexpr int KV_STAGE = 16384, KV_NSTAGE = 16;

#define ATT_LOOP_ASM \
    "s_mov_b32 s23, 16\n" \
    "s_mov_b32 s24, 0xc000\n" \
    "s_mov_b32 s22, 0\n" \
    "s_add_u32 s20, %[ldst], 0x800\n" \
    "s_mov_b32 s21, 0x8800\n" \
    "ATT_LOOP_%=:\n" \
    "v_mfma_f32_16x16x32_bf16 v[8:11], v[80:83], v[68:71], 0\n" \
    "v_exp_f32 v0, v0\n" \
    "v_exp_f32 v1, v1\n" \
    "v_mfma_f32_16x16x32_bf16 v[12:15], v[84:87], v[68:71], 0\n" \
    "ds_read_b128 v[96:99], v124 offset:4096\n" \
    "v_exp_f32 v2, v2\n" \
    "v_exp_f32 v3, v3\n" \
    "v_mfma_f32_16x16x32_bf16 v[40:43], v[104:107], v[120:123], v[40:43]\n" \
    "v_cvt_pk_bf16_f32 v116, v0, v1\n" \
    "v_exp_f32 v4, v4\n" \
    "v_exp_f32 v5, v5\n" \
    "v_mfma_f32_16x16x32_bf16 v[44:47], v[108:111], v[120:123], v[44:47]\n" \
    "s_mov_b32 m0, s20\n" \
    "s_add_u32 s20, s20, 0x400\n" \
    "buffer_load_dwordx4 v126, %[srd], s21 offen lds\n" \
    "s_add_u32 s21, s21, 0x400\n" \
    "v_cvt_pk_bf16_f32 v117, v2, v3\n" \
    "v_exp_f32 v6, v6\n" \
    "v_exp_f32 v7, v7\n" \
    "v_mfma_f32_16x16x32_bf16 v[60:63], v[112:115], v[120:123], v[60:63]\n" \
    "v_cvt_pk_bf16_f32 v118, v4, v5\n" \
    "v_cvt_pk_bf16_f32 v119, v6, v7\n" \
    "s_waitcnt lgkmcnt(1)\n" \
    "v_mfma_f32_16x16x32_bf16 v[0:3], v[80:83], v[72:75], 0\n" \
    "v_exp_f32 v8, v8\n" \
    "v_exp_f32 v9, v9\n" \
    "v_mfma_f32_16x16x32_bf16 v[4:7], v[84:87], v[72:75], 0\n" \
    "ds_read_b128 v[100:103], v124 offset:5120\n" \
    "v_exp_f32 v10, v10\n" \
    "v_exp_f32 v11, v11\n" \
    "v_mfma_f32_16x16x32_bf16 v[16:19], v[88:91], v[116:119], v[16:19]\n" \
    "v_cvt_pk_bf16_f32 v120, v8, v9\n" \
    "v_exp_f32 v12, v12\n" \
    "v_exp_f32 v13, v13\n" \
    "v_mfma_f32_16x16x32_bf16 v[20:23], v[92:95], v[116:119], v[20:23]\n" \
    "v_cvt_pk_bf16_f32 v121, v10, v11\n" \
    "v_exp_f32 v14, v14\n" \
    "v_exp_f32 v15, v15\n" \
    "v_mfma_f32_16x16x32_bf16 v[48:51], v[112:115], v[116:119], v[48:51]\n" \
    "v_cvt_pk_bf16_f32 v122, v12, v13\n" \
    "v_cvt_pk_bf16_f32 v123, v14, v15\n" \
    "v_mfma_f32_16x16x32_bf16 v[8:11], v[80:83], v[76:79], 0\n" \
    "v_exp_f32 v0, v0\n" \
    "v_exp_f32 v1, v1\n" \
    "v_mfma_f32_16x16x32_bf16 v[12:15], v[84:87], v[76:79], 0\n" \
    "ds_read_b128 v[104:107], v124 offset:6144\n" \
    "v_exp_f32 v2, v2\n" \
    "v_exp_f32 v3, v3\n" \
    "v_mfma_f32_16x16x32_bf16 v[24:27], v[88:91], v[120:123], v[24:27]\n" \
    "v_cvt_pk_bf16_f32 v116, v0, v1\n" \
    "v_exp_f32 v4, v4\n" \
    "v_exp_f32 v5, v5\n" \
    "v_mfma_f32_16x16x32_bf16 v[28:31], v[92:95], v[120:123], v[28:31]\n" \
    "s_mov_b32 m0, s20\n" \
    "s_add_u32 s20, s20, 0x400\n" \
    "buffer_load_dwordx4 v126, %[srd], s21 offen lds\n" \
    "s_add_u32 s21, s21, 0x400\n" \
    "s_add_u32 s22, s22, 0x4000\n" \
    "s_and_b32 s22, s22, 0xffff\n" \
    "s_add_u32 s20, s22, %[ldst]\n" \
    "s_add_u32 s21, s21, 0x3000\n" \
    "v_cvt_pk_bf16_f32 v117, v2, v3\n" \
    "v_exp_f32 v6, v6\n" \
    "v_exp_f32 v7, v7\n" \
    "v_mfma_f32_16x16x32_bf16 v[52:55], v[112:115], v[120:123], v[52:55]\n" \
    "v_cvt_pk_bf16_f32 v118, v4, v5\n" \
    "v_cvt_pk_bf16_f32 v119, v6, v7\n" \
    "s_waitcnt lgkmcnt(1)\n" \
    "v_mfma_f32_16x16x32_bf16 v[0:3], v[96:99], v[64:67], 0\n" \
    "v_exp_f32 v8, v8\n" \
    "v_exp_f32 v9, v9\n" \
    "v_mfma_f32_16x16x32_bf16 v[4:7], v[100:103], v[64:67], 0\n" \
    "ds_read_b128 v[108:111], v124 offset:7168\n" \
    "v_exp_f32 v10, v10\n" \
    "v_exp_f32 v11, v11\n" \
    "v_mfma_f32_16x16x32_bf16 v[32:35], v[88:91], v[116:119], v[32:35]\n" \
    "v_cvt_pk_bf16_f32 v120, v8, v9\n" \
    "v_exp_f32 v12, v12\n" \
    "v_exp_f32 v13, v13\n" \
    "v_mfma_f32_16x16x32_bf16 v[36:39], v[92:95], v[116:119], v[36:39]\n" \
    "v_cvt_pk_bf16_f32 v121, v10, v11\n" \
    "v_exp_f32 v14, v14\n" \
    "v_exp_f32 v15, v15\n" \
    "v_mfma_f32_16x16x32_bf16 v[56:59], v[112:115], v[116:119], v[56:59]\n" \
    "v_cvt_pk_bf16_f32 v122, v12, v13\n" \
    "v_cvt_pk_bf16_f32 v123, v14, v15\n" \
    "v_mfma_f32_16x16x32_bf16 v[8:11], v[96:99], v[68:71], 0\n" \
    "v_exp_f32 v0, v0\n" \
    "v_exp_f32 v1, v1\n" \
    "v_mfma_f32_16x16x32_bf16 v[12:15], v[100:103], v[68:71], 0\n" \
    "ds_read_b128 v[80:83], v124 offset:8192\n" \
    "v_exp_f32 v2, v2\n" \
    "v_exp_f32 v3, v3\n" \
    "v_mfma_f32_16x16x32_bf16 v[40:43], v[88:91], v[120:123], v[40:43]\n" \
    "v_cvt_pk_bf16_f32 v116, v0, v1\n" \
    "v_exp_f32 v4, v4\n" \
    "v_exp_f32 v5, v5\n" \
    "v_mfma_f32_16x16x32_bf16 v[44:47], v[92:95], v[120:123], v[44:47]\n" \
    "v_cvt_pk_bf16_f32 v117, v2, v3\n" \
    "v_exp_f32 v6, v6\n" \
    "v_exp_f32 v7, v7\n" \
    "v_mfma_f32_16x16x32_bf16 v[60:63], v[112:115], v[120:123], v[60:63]\n" \
    "v_cvt_pk_bf16_f32 v118, v4, v5\n" \
    "v_cvt_pk_bf16_f32 v119, v6, v7\n" \
    "s_waitcnt lgkmcnt(1)\n" \
    "v_mfma_f32_16x16x32_bf16 v[0:3], v[96:99], v[72:75], 0\n" \
    "v_exp_f32 v8, v8\n" \
    "v_exp_f32 v9, v9\n" \
    "v_mfma_f32_16x16x32_bf16 v[4:7], v[100:103], v[72:75], 0\n" \
    "ds_read_b128 v[84:87], v124 offset:9216\n" \
    "v_exp_f32 v10, v10\n" \
    "v_exp_f32 v11, v11\n" \
    "v_mfma_f32_16x16x32_bf16 v[16:19], v[104:107], v[116:119], v[16:19]\n" \
    "v_cvt_pk_bf16_f32 v120, v8, v9\n" \
    "v_exp_f32 v12, v12\n" \
    "v_exp_f32 v13, v13\n" \
    "v_mfma_f32_16x16x32_bf16 v[20:23], v[108:111], v[116:119], v[20:23]\n" \
    "v_cvt_pk_bf16_f32 v121, v10, v11\n" \
    "v_exp_f32 v14, v14\n" \
    "v_exp_f32 v15, v15\n" \
    "v_mfma_f32_16x16x32_bf16 v[48:51], v[112:115], v[116:119], v[48:51]\n" \
    "v_cvt_pk_bf16_f32 v122, v12, v13\n" \
    "v_cvt_pk_bf16_f32 v123, v14, v15\n" \
    "v_mfma_f32_16x16x32_bf16 v[8:11], v[96:99], v[76:79], 0\n" \
    "v_exp_f32 v0, v0\n" \
    "v_exp_f32 v1, v1\n" \
    "v_mfma_f32_16x16x32_bf16 v[12:15], v[100:103], v[76:79], 0\n" \
    "ds_read_b128 v[88:91], v124 offset:10240\n" \
    "v_exp_f32 v2, v2\n" \
    "v_exp_f32 v3, v3\n" \
    "v_mfma_f32_16x16x32_bf16 v[24:27], v[104:107], v[120:123], v[24:27]\n" \
    "v_cvt_pk_bf16_f32 v116, v0, v1\n" \
    "v_exp_f32 v4, v4\n" \
    "v_exp_f32 v5, v5\n" \
    "v_mfma_f32_16x16x32_bf16 v[28:31], v[108:111], v[120:123], v[28:31]\n" \
    "v_cvt_pk_bf16_f32 v117, v2, v3\n" \
    "v_exp_f32 v6, v6\n" \
    "v_exp_f32 v7, v7\n" \
    "v_mfma_f32_16x16x32_bf16 v[52:55], v[112:115], v[120:123], v[52:55]\n" \
    "v_cvt_pk_bf16_f32 v118, v4, v5\n" \
    "v_cvt_pk_bf16_f32 v119, v6, v7\n" \
    "s_waitcnt lgkmcnt(1)\n" \
    "v_mfma_f32_16x16x32_bf16 v[0:3], v[80:83], v[64:67], 0\n" \
    "v_exp_f32 v8, v8\n" \
    "v_exp_f32 v9, v9\n" \
    "v_mfma_f32_16x16x32_bf16 v[4:7], v[84:87], v[64:67], 0\n" \
    "ds_read_b128 v[92:95], v124 offset:11264\n" \
    "v_exp_f32 v10, v10\n" \
    "v_exp_f32 v11, v11\n" \
    "v_mfma_f32_16x16x32_bf16 v[32:35], v[104:107], v[116:119], v[32:35]\n" \
    "v_cvt_pk_bf16_f32 v120, v8, v9\n" \
    "v_exp_f32 v12, v12\n" \
    "v_exp_f32 v13, v13\n" \
    "v_mfma_f32_16x16x32_bf16 v[36:39], v[108:111], v[116:119], v[36:39]\n" \
    "v_cvt_pk_bf16_f32 v121, v10, v11\n" \
    "v_exp_f32 v14, v14\n" \
    "v_exp_f32 v15, v15\n" \
    "v_mfma_f32_16x16x32_bf16 v[56:59], v[112:115], v[116:119], v[56:59]\n" \
    "v_cvt_pk_bf16_f32 v122, v12, v13\n" \
    "v_cvt_pk_bf16_f32 v123, v14, v15\n" \
    "v_mfma_f32_16x16x32_bf16 v[8:11], v[80:83], v[68:71], 0\n" \
    "v_exp_f32 v0, v0\n" \
    "v_exp_f32 v1, v1\n" \
    "v_mfma_f32_16x16x32_bf16 v[12:15], v[84:87], v[68:71], 0\n" \
    "ds_read_b128 v[96:99], v124 offset:12288\n" \
    "v_exp_f32 v2, v2\n" \
    "v_exp_f32 v3, v3\n" \
    "v_mfma_f32_16x16x32_bf16 v[40:43], v[104:107], v[120:123], v[40:43]\n" \
    "v_cvt_pk_bf16_f32 v116, v0, v1\n" \
    "v_exp_f32 v4, v4\n" \
    "v_exp_f32 v5, v5\n" \
    "v_mfma_f32_16x16x32_bf16 v[44:47], v[108:111], v[120:123], v[44:47]\n" \
    "v_cvt_pk_bf16_f32 v117, v2, v3\n" \
    "v_exp_f32 v6, v6\n" \
    "v_exp_f32 v7, v7\n" \
    "v_mfma_f32_16x16x32_bf16 v[60:63], v[112:115], v[120:123], v[60:63]\n" \
    "v_cvt_pk_bf16_f32 v118, v4, v5\n" \
    "v_cvt_pk_bf16_f32 v119, v6, v7\n" \
    "s_waitcnt lgkmcnt(1)\n" \
    "v_mfma_f32_16x16x32_bf16 v[0:3], v[80:83], v[72:75], 0\n" \
    "v_exp_f32 v8, v8\n" \
    "v_exp_f32 v9, v9\n" \
    "v_mfma_f32_16x16x32_bf16 v[4:7], v[84:87], v[72:75], 0\n" \
    "ds_read_b128 v[100:103], v124 offset:13312\n" \
    "v_exp_f32 v10, v10\n" \
    "v_exp_f32 v11, v11\n" \
    "v_mfma_f32_16x16x32_bf16 v[16:19], v[88:91], v[116:119], v[16:19]\n" \
    "v_cvt_pk_bf16_f32 v120, v8, v9\n" \
    "v_exp_f32 v12, v12\n" \
    "v_exp_f32 v13, v13\n" \
    "v_mfma_f32_16x16x32_bf16 v[20:23], v[92:95], v[116:119], v[20:23]\n" \
    "v_cvt_pk_bf16_f32 v121, v10, v11\n" \
    "v_exp_f32 v14, v14\n" \
    "v_exp_f32 v15, v15\n" \
    "v_mfma_f32_16x16x32_bf16 v[48:51], v[112:115], v[116:119], v[48:51]\n" \
    "v_cvt_pk_bf16_f32 v122, v12, v13\n" \
    "v_cvt_pk_bf16_f32 v123, v14, v15\n" \
    "v_mfma_f32_16x16x32_bf16 v[8:11], v[80:83], v[76:79], 0\n" \
    "v_exp_f32 v0, v0\n" \
    "v_exp_f32 v1, v1\n" \
    "v_mfma_f32_16x16x32_bf16 v[12:15], v[84:87], v[76:79], 0\n" \
    "ds_read_b128 v[104:107], v124 offset:14336\n" \
    "v_exp_f32 v2, v2\n" \
    "v_exp_f32 v3, v3\n" \
    "v_mfma_f32_16x16x32_bf16 v[24:27], v[88:91], v[120:123], v[24:27]\n" \
    "v_cvt_pk_bf16_f32 v116, v0, v1\n" \
    "v_exp_f32 v4, v4\n" \
    "v_exp_f32 v5, v5\n" \
    "v_mfma_f32_16x16x32_bf16 v[28:31], v[92:95], v[120:123], v[28:31]\n" \
    "v_cvt_pk_bf16_f32 v117, v2, v3\n" \
    "v_exp_f32 v6, v6\n" \
    "v_exp_f32 v7, v7\n" \
    "v_mfma_f32_16x16x32_bf16 v[52:55], v[112:115], v[120:123], v[52:55]\n" \
    "v_cvt_pk_bf16_f32 v118, v4, v5\n" \
    "v_cvt_pk_bf16_f32 v119, v6, v7\n" \
    "s_waitcnt lgkmcnt(1)\n" \
    "v_mfma_f32_16x16x32_bf16 v[0:3], v[96:99], v[64:67], 0\n" \
    "v_exp_f32 v8, v8\n" \
    "v_exp_f32 v9, v9\n" \
    "v_mfma_f32_16x16x32_bf16 v[4:7], v[100:103], v[64:67], 0\n" \
    "ds_read_b128 v[108:111], v124 offset:15360\n" \
    "v_exp_f32 v10, v10\n" \
    "v_exp_f32 v11, v11\n" \
    "v_mfma_f32_16x16x32_bf16 v[32:35], v[88:91], v[116:119], v[32:35]\n" \
    "v_cvt_pk_bf16_f32 v120, v8, v9\n" \
    "v_exp_f32 v12, v12\n" \
    "v_exp_f32 v13, v13\n" \
    "v_mfma_f32_16x16x32_bf16 v[36:39], v[92:95], v[116:119], v[36:39]\n" \
    "v_cvt_pk_bf16_f32 v121, v10, v11\n" \
    "v_exp_f32 v14, v14\n" \
    "v_exp_f32 v15, v15\n" \
    "v_mfma_f32_16x16x32_bf16 v[56:59], v[112:115], v[116:119], v[56:59]\n" \
    "v_cvt_pk_bf16_f32 v122, v12, v13\n" \
    "v_cvt_pk_bf16_f32 v123, v14, v15\n" \
    "s_waitcnt vmcnt(4)\n" \
    "s_barrier\n" \
    "v_mfma_f32_16x16x32_bf16 v[8:11], v[96:99], v[68:71], 0\n" \
    "v_exp_f32 v0, v0\n" \
    "v_exp_f32 v1, v1\n" \
    "v_mfma_f32_16x16x32_bf16 v[12:15], v[100:103], v[68:71], 0\n" \
    "ds_read_b128 v[80:83], v125 offset:0\n" \
    "v_exp_f32 v2, v2\n" \
    "v_exp_f32 v3, v3\n" \
    "v_mfma_f32_16x16x32_bf16 v[40:43], v[88:91], v[120:123], v[40:43]\n" \
    "v_cvt_pk_bf16_f32 v116, v0, v1\n" \
    "v_exp_f32 v4, v4\n" \
    "v_exp_f32 v5, v5\n" \
    "v_mfma_f32_16x16x32_bf16 v[44:47], v[92:95], v[120:123], v[44:47]\n" \
    "s_mov_b32 m0, s20\n" \
    "s_add_u32 s20, s20, 0x400\n" \
    "buffer_load_dwordx4 v126, %[srd], s21 offen lds\n" \
    "s_add_u32 s21, s21, 0x400\n" \
    "v_cvt_pk_bf16_f32 v117, v2, v3\n" \
    "v_exp_f32 v6, v6\n" \
    "v_exp_f32 v7, v7\n" \
    "v_mfma_f32_16x16x32_bf16 v[60:63], v[112:115], v[120:123], v[60:63]\n" \
    "v_cvt_pk_bf16_f32 v118, v4, v5\n" \
    "v_cvt_pk_bf16_f32 v119, v6, v7\n" \
    "s_waitcnt lgkmcnt(1)\n" \
    "v_mfma_f32_16x16x32_bf16 v[0:3], v[96:99], v[72:75], 0\n" \
    "v_exp_f32 v8, v8\n" \
    "v_exp_f32 v9, v9\n" \
    "v_mfma_f32_16x16x32_bf16 v[4:7], v[100:103], v[72:75], 0\n" \
    "ds_read_b128 v[84:87], v125 offset:1024\n" \
    "v_exp_f32 v10, v10\n" \
    "v_exp_f32 v11, v11\n" \
    "v_mfma_f32_16x16x32_bf16 v[16:19], v[104:107], v[116:119], v[16:19]\n" \
    "v_cvt_pk_bf16_f32 v120, v8, v9\n" \
    "v_exp_f32 v12, v12\n" \
    "v_exp_f32 v13, v13\n" \
    "v_mfma_f32_16x16x32_bf16 v[20:23], v[108:111], v[116:119], v[20:23]\n" \
    "v_cvt_pk_bf16_f32 v121, v10, v11\n" \
    "v_exp_f32 v14, v14\n" \
    "v_exp_f32 v15, v15\n" \
    "v_mfma_f32_16x16x32_bf16 v[48:51], v[112:115], v[116:119], v[48:51]\n" \
    "v_cvt_pk_bf16_f32 v122, v12, v13\n" \
    "v_cvt_pk_bf16_f32 v123, v14, v15\n" \
    "v_mfma_f32_16x16x32_bf16 v[8:11], v[96:99], v[76:79], 0\n" \
    "v_exp_f32 v0, v0\n" \
    "v_exp_f32 v1, v1\n" \
    "v_mfma_f32_16x16x32_bf16 v[12:15], v[100:103], v[76:79], 0\n" \
    "ds_read_b128 v[88:91], v125 offset:2048\n" \
    "v_exp_f32 v2, v2\n" \
    "v_exp_f32 v3, v3\n" \
    "v_mfma_f32_16x16x32_bf16 v[24:27], v[104:107], v[120:123], v[24:27]\n" \
    "v_cvt_pk_bf16_f32 v116, v0, v1\n" \
    "v_exp_f32 v4, v4\n" \
    "v_exp_f32 v5, v5\n" \
    "v_mfma_f32_16x16x32_bf16 v[28:31], v[108:111], v[120:123], v[28:31]\n" \
    "s_mov_b32 m0, s20\n" \
    "s_add_u32 s20, s20, 0x400\n" \
    "buffer_load_dwordx4 v126, %[srd], s21 offen lds\n" \
    "s_add_u32 s21, s21, 0x400\n" \
    "v_cvt_pk_bf16_f32 v117, v2, v3\n" \
    "v_exp_f32 v6, v6\n" \
    "v_exp_f32 v7, v7\n" \
    "v_mfma_f32_16x16x32_bf16 v[52:55], v[112:115], v[120:123], v[52:55]\n" \
    "v_cvt_pk_bf16_f32 v118, v4, v5\n" \
    "v_cvt_pk_bf16_f32 v119, v6, v7\n" \
    "s_waitcnt lgkmcnt(1)\n" \
    "v_mfma_f32_16x16x32_bf16 v[0:3], v[80:83], v[64:67], 0\n" \
    "v_exp_f32 v8, v8\n" \
    "v_exp_f32 v9, v9\n" \
    "v_mfma_f32_16x16x32_bf16 v[4:7], v[84:87], v[64:67], 0\n" \
    "ds_read_b128 v[92:95], v125 offset:3072\n" \
    "v_exp_f32 v10, v10\n" \
    "v_exp_f32 v11, v11\n" \
    "v_mfma_f32_16x16x32_bf16 v[32:35], v[104:107], v[116:119], v[32:35]\n" \
    "v_cvt_pk_bf16_f32 v120, v8, v9\n" \
    "v_exp_f32 v12, v12\n" \
    "v_exp_f32 v13, v13\n" \
    "v_mfma_f32_16x16x32_bf16 v[36:39], v[108:111], v[116:119], v[36:39]\n" \
    "v_cvt_pk_bf16_f32 v121, v10, v11\n" \
    "v_exp_f32 v14, v14\n" \
    "v_exp_f32 v15, v15\n" \
    "v_mfma_f32_16x16x32_bf16 v[56:59], v[112:115], v[116:119], v[56:59]\n" \
    "v_cvt_pk_bf16_f32 v122, v12, v13\n" \
    "v_cvt_pk_bf16_f32 v123, v14, v15\n" \
    "v_mov_b32 v124, v125\n" \
    "s_add_u32 s24, s24, 0x4000\n" \
    "s_and_b32 s24, s24, 0xffff\n" \
    "v_add_u32 v125, s24, v127\n" \
    "s_sub_u32 s23, s23, 1\n" \
    "s_cmp_lg_u32 s23, 0\n" \
    "s_cbranch_scc1 ATT_LOOP_%=\n" \
    "v_mfma_f32_16x16x32_bf16 v[40:43], v[104:107], v[120:123], v[40:43]\n" \
    "v_mfma_f32_16x16x32_bf16 v[44:47], v[108:111], v[120:123], v[44:47]\n" \
    "v_mfma_f32_16x16x32_bf16 v[60:63], v[112:115], v[120:123], v[60:63]\n" \
    "s_waitcnt vmcnt(0) lgkmcnt(0)\n" \
    "s_nop 15\n"

constexpr int Q_NS = 3, Q_SLOT = 24576;
__device__ __forceinline__ void attention(int b, const char* x, const char* wq, char* ws, lds_ptr lds, int w, int lane) {
    const int xc = b & 7, t = b >> 3, st128 = (xc >> 1) * 4 + (t & 3), hp = (xc & 1) * 8 + (t >> 2);
    const int head = 2 * hp + (w & 1), s0 = st128 * 128 + (w >> 1) * 64, g = lane >> 4, c = lane & 15;
    const u32x4 srd = make_srd(ws + WS_KV, S * 128);
    const u32 voff = w * 4096 + lane * 16, ldst = (u32)(uintptr_t)lds + w * 4096;
    auto dma = [&](int st, int p0, int p1) {
#pragma unroll
        for (int p = p0; p < p1; ++p) dma16(ldst + ((st + 2) & 3) * KV_STAGE + p * 1024, voff, srd, st * KV_STAGE + p * 1024);
    };
    __builtin_amdgcn_s_setprio(Q_PRIO);
    float gate[4];
#pragma unroll
    for (int i = 0; i < 4; ++i) gate[i] = ((const float*)(ws + WS_G))[s0 + i * 16 + c];
    const f32x4 kb2 = ((const f32x4*)(ws + WS_BND))[lane];
    u32x4 q[4];
    {
        OpTr<64, D * 2, 2, true, 2> a; a.init(wq, D * D * 2, hp * 64, 0, lds, w, (w & 1) * 2, lane);
        OpRow bb; bb.init(x, S * D * 2, st128 * 128, lds + a.BYTES, w, (w >> 1) * 64, lane);
        Engine<Q_NS, 2, 4, 2> e;
        static_assert((D / 64 - 1) % Q_NS == 0 && Q_SLOT <= 2 * KV_STAGE, "the last Q slot lies below the first two K/V stages");
        gemm_run<Q_SLOT, D / 64, 2>(e, a, bb, [] {}, [](int) {}, [&](auto p) { if constexpr (p.v == Rounds<Q_NS, D / 64>::TAIL - 2) { dma(0, 0, 4); dma(1, 0, 4); } });
        wait_bar<4>();
        dma(2, 0, 2);
#pragma unroll
        for (int i = 0; i < 4; ++i) q[i] = pack8(e.acc[0][i] * QSCALE, e.acc[1][i] * QSCALE);
    }
    const float k2 = wave_max(fmaxf(fmaxf(kb2[0], kb2[1]), fmaxf(kb2[2], kb2[3])));
    bool ok = true;
#pragma unroll
    for (int i = 0; i < 4; ++i) {
        float n = 0.f;
#pragma unroll
        for (int m = 0; m < 4; ++m) { const float a = bf_lo(q[i][m]), h = bf_hi(q[i][m]); n += a * a + h * h; }
        ok = ok && group_sum(n) * k2 * 1.02f <= SAFE_LOG2 * SAFE_LOG2;
        PIN(gate[i]);
    }
    const bool safe = __all(ok);
    __builtin_amdgcn_s_setprio(ATT_PRIO);
    f32x4 o[8];
#pragma unroll
    for (int i = 0; i < 8; ++i) o[i] = f32x4{};
    float lsum[4];
    const lds_ptr base = lds + lane * 16;
    const f32x4 z = {};
    STAMP(1, 1);
    if (safe) {
        const u32 one = c == 0 ? 0x3f803f80u : 0u;
        const u32x4 ones = {one, one, one, one};
        const lds_ptr first = base + 2 * KV_STAGE;
        u32x4 ka = lds_rd128(first), kb = lds_rd128(first + 1024), v0 = lds_rd128(first + 2048), v1 = lds_rd128(first + 3072);
        f32x4 sa = M16(ka, q[0], z), sb = M16(kb, q[0], z);
        f32x16 oa = {}, ob = {}, l = {};
        u32x4 vz0 = {}, vz1 = {}, pz = {};
        u32 cur = (u32)(uintptr_t)first, nxt = cur + KV_STAGE;
        asm volatile("s_nop 7\n" ATT_LOOP_ASM
                     : "+{v[0:3]}"(sa), "+{v[4:7]}"(sb), "+{v[16:31]}"(oa), "+{v[32:47]}"(ob), "+{v[48:63]}"(l), "+{v[80:83]}"(ka), "+{v[84:87]}"(kb),
                       "+{v[88:91]}"(v0), "+{v[92:95]}"(v1), "+{v[104:107]}"(vz0), "+{v[108:111]}"(vz1), "+{v[120:123]}"(pz), "+{v124}"(cur), "+{v125}"(nxt)
                     : "{v[64:67]}"(q[0]), "{v[68:71]}"(q[1]), "{v[72:75]}"(q[2]), "{v[76:79]}"(q[3]), "{v[112:115]}"(ones), "{v126}"(voff),
                       "{v127}"((u32)(uintptr_t)base), [srd] "s"(srd), [ldst] "s"(ldst)
                     : "v8", "v9", "v10", "v11", "v12", "v13", "v14", "v15", "v96", "v97", "v98", "v99", "v100", "v101", "v102", "v103",
                       "v116", "v117", "v118", "v119", "s20", "s21", "s22", "s23", "s24", "scc", "memory");
#pragma unroll
        for (int i = 0; i < 4; ++i) {
            lsum[i] = __shfl(l[4 * i], c);
            o[i] = f32x4{oa[4 * i], oa[4 * i + 1], oa[4 * i + 2], oa[4 * i + 3]};
            o[4 + i] = f32x4{ob[4 * i], ob[4 * i + 1], ob[4 * i + 2], ob[4 * i + 3]};
        }
    } else {
        float mx[4];
#pragma unroll
        for (int i = 0; i < 4; ++i) { mx[i] = -__builtin_inff(); lsum[i] = 0.f; }
        dma(2, 2, 4);
        for (int st = 0; st < KV_NSTAGE; ++st) {
            wait_bar<4>();
            dma(st + 3, 0, 4);
#pragma unroll 1
            for (int kb = 0; kb < 4; ++kb) {
                const lds_ptr p = base + ((st + 2) & 3) * KV_STAGE + kb * 4096;
                const u32x4 k0 = lds_rd128(p), k1 = lds_rd128(p + 1024), v0 = lds_rd128(p + 2048), v1 = lds_rd128(p + 3072);
#pragma unroll
                for (int i = 0; i < 4; ++i) {
                    f32x4 sa = M16(k0, q[i], z), sb = M16(k1, q[i], z);
                    float m = mx[i];
#pragma unroll
                    for (int r = 0; r < 4; ++r) m = fmaxf(m, fmaxf(sa[r], sb[r]));
                    m = fmaxf(m, __shfl_xor(m, 16)); m = fmaxf(m, __shfl_xor(m, 32));
                    const float alpha = EX2(mx[i] - m);
                    mx[i] = m;
                    float t = 0.f;
#pragma unroll
                    for (int r = 0; r < 4; ++r) { sa[r] = EX2(sa[r] - m); sb[r] = EX2(sb[r] - m); t += sa[r] + sb[r]; }
                    lsum[i] = lsum[i] * alpha + t;
                    const u32x4 pp = pack8(sa, sb);
                    o[2 * i] = M16(v0, pp, o[2 * i] * alpha);
                    o[2 * i + 1] = M16(v1, pp, o[2 * i + 1] * alpha);
                }
            }
        }
        asm volatile("s_waitcnt vmcnt(0)" ::: "memory");
#pragma unroll
        for (int i = 0; i < 4; ++i) lsum[i] = group_sum(lsum[i]);
    }
    STAMP(1, 6);
#pragma unroll
    for (int i = 0; i < 4; ++i) {
        const float r = gate[i] / lsum[i];
        store_wt(ws + WS_OF, S * D * 2, (u32)((head * 4 + g) * S + s0 + i * 16 + c) * 16u, pack8(o[2 * i] * r, o[2 * i + 1] * r));
    }
    publish(ws, CNT_O + st128);
}

__device__ __forceinline__ f32x2 gelu_tanh(f32x2 x) {
    constexpr float C0 = -2.f * 0.7978845608028654f * 1.4426950408889634f, C1 = C0 * 0.044715f;
    const f32x2 y = x * __builtin_elementwise_fma(x * x, f32x2{C1, C1}, f32x2{C0, C0});
    const f32x2 d = f32x2{EX2(y[0]), EX2(y[1])} + 1.f;
    return x * f32x2{__builtin_amdgcn_rcpf(d[0]), __builtin_amdgcn_rcpf(d[1])};
}

__device__ __forceinline__ void gemm1_tile(int b, const void* w1, const char* b1, char* ws, lds_ptr lds, int w, int lane) {
    const int x = b & 7, t = b >> 3;
    const int dt = (x & 1) * 4 + (t & 3), d0 = dt * 128, f0 = ((x >> 1) * 7 + (t >> 2)) * 128;
    const int g = lane >> 4, c = lane & 15;
    u32x2 bias[8];
#pragma unroll
    for (int j = 0; j < 8; ++j) bias[j] = *(const u32x2*)(b1 + (f0 + j * 16 + g * 4) * 2);
    OpReg<2, D, 1, false> a; a.init(ws + WS_HF, S * D * 2, d0 + w * 32, 0, lane);
    OpTr<128, F * 2, 1, false, 8> bb; bb.init(w1, S * F * 2, f0, 0, lds, w, 0, lane);
    Engine<G1_NS, 2, 8> e;
    gemm_run<8192, S / 32, 0>(e, a, bb, [] {}, [](int) {}, [](auto) {});
    auto& acc = e.acc;
    STAMP(1, 1);
#pragma unroll
    for (int jp = 0; jp < 4; ++jp)
#pragma unroll
        for (int i = 0; i < 2; ++i) {
            f32x4 u[2];
#pragma unroll
            for (int h = 0; h < 2; ++h) {
                const u32x2 bv = bias[2 * jp + h];
                const f32x4 v = acc[i][2 * jp + h] + f32x4{bf_lo(bv[0]), bf_hi(bv[0]), bf_lo(bv[1]), bf_hi(bv[1])};
                const f32x2 lo = gelu_tanh(f32x2{v[0], v[1]}), hi = gelu_tanh(f32x2{v[2], v[3]});
                u[h] = f32x4{lo[0], lo[1], hi[0], hi[1]};
            }
            store_wt(ws + WS_UF, F * D * 2, (u32)(((f0 / 32 + jp) * 4 + g) * D + d0 + w * 32 + i * 16 + c) * 16u, pack8(u[0], u[1]));
        }
    publish(ws, CNT_U + dt * 2 + (x >> 2));
}

constexpr int P1_NS = 8, P1_BASE = 8192, P2_NS = 6, P2_SLOT = 12288;

__device__ __forceinline__ void post_tile(int b, const char* w2, const char* b2, const char* wo, char* ws, char* out, lds_ptr lds, int w, int lane) {
    const int x = b & 7, t = b >> 3, h = t & 1;
    const int s0 = ((x >> 1) * 8 + (t & 7)) * 64, dt = (x & 1) * 4 + (t >> 3), d0 = dt * 128;
    const int g = lane >> 4, c = lane & 15;
    constexpr int N1 = F / 64, N2 = D / 32;
    static_assert(N1 % P1_NS == 0 && P1_BASE + (P1_NS - 1) * 8192 >= (P2_NS - 1) * P2_SLOT, "the last w2 slot lies in the last wo slot");
    float omg[4], bs[4];
#pragma unroll
    for (int j = 0; j < 4; ++j) {
        const int s = s0 + j * 16 + c;
        asm volatile("global_load_dword %0, %1, off" : "=v"(omg[j]) : "v"(ws + WS_G + s * 4) : "memory");
        asm volatile("global_load_ushort %0, %1, off" : "=v"(bs[j]) : "v"(b2 + s * 2) : "memory");
    }
    OpTr<128, D * 2, 1, false, 2> a2; a2.init(wo, D * D * 2, d0, 0, lds, w, w * 2, lane);
    OpFrag<64, S, 1> b2o; b2o.init(ws + WS_OF, S * D * 2, s0, 0, lds + 8192, w, 0, lane);
    Engine<P2_NS, 2, 4> e;
    auto& acc = e.acc;
    u32 seen = 0;
    {
        OpReg<2, D, 1, true> a1; a1.init(ws + WS_UF, F * D * 2, d0 + w * 32, h * N1, lane);
        OpTr<128, S * 2, 1, false, 8> b1; b1.init(w2, F * S * 2, s0 & ~127, h * N1, lds + P1_BASE, w, 4 * h, lane);
        Engine<P1_NS, 2, 8> e1;
        constexpr int TAIL = Rounds<P1_NS, N1>::TAIL;
        gemm_run<8192, N1, 2>(e1, a1, b1,
            [&] { if (w == 0) await(ws, CNT_U + dt * 2 + h, F / 256); __builtin_amdgcn_s_barrier(); STAMP(1, 3); },
            [&](int r) { if (r == PRIO_SWITCH) __builtin_amdgcn_s_setprio(1); if (r == Rounds<P1_NS, N1>::N - 1) sample(seen, ws, CNT_O + (s0 >> 7)); },
            [&](auto p) { if constexpr (p.v == TAIL - 2) { confirm(seen, ws, CNT_O + (s0 >> 7), 16); STAMP(1, 6); gemm_fill<P2_SLOT>(a2, b2o, 0, P2_NS - 1); } });
        STAMP(1, 4);
#pragma unroll
        for (int j = 0; j < 4; ++j) {
            PIN(omg[j]); PIN(bs[j]);
            omg[j] = bf_round(1.f - omg[j]); bs[j] = __uint_as_float(__float_as_uint(bs[j]) << 16);
#pragma unroll
            for (int i = 0; i < 2; ++i) {
                store_wt(ws + WS_X, 256u << 15, (u32)(((b * 4 + w) * 8 + i * 4 + j) * 64 + lane) * 16u, __builtin_bit_cast(u32x4, e1.acc[i][4 + j]));
                acc[i][j] = (e1.acc[i][j] + bs[j]) * omg[j];
            }
        }
    }
    constexpr int PPW = 3, XW = 8;
    wait_bar<(P2_NS - 2) * PPW + XW>();
    gemm_fill<P2_SLOT>(a2, b2o, P2_NS - 1, P2_NS);
    gemm_first(e, a2, b2o);
    gemm_steps<0, P2_NS, MANY, P2_SLOT, 2, XW>(e, a2, b2o, P2_NS, [&](auto p) { if constexpr (p.v == P2_NS - 2) signal(ws, CNT_X + b); });
    for (int r = 1; r < 3; ++r) gemm_steps<0, P2_NS, MANY, P2_SLOT, 2, 0>(e, a2, b2o, (r + 1) * P2_NS, [](auto) {});
    seen = 0;
    u32x4 got[8];
    const u32x4 xsrd = make_srd(ws + WS_X, 256u << 15);
    gemm_steps<0, N2 - 3 * P2_NS, N2 - 3 * P2_NS, P2_SLOT, 2, 0>(e, a2, b2o, 4 * P2_NS, [&](auto p) {
        if constexpr (p.v == 0) sample(seen, ws, CNT_X + (b ^ 8));
        if constexpr (p.v == P2_NS - 2) {
            confirm(seen, ws, CNT_X + (b ^ 8), 1);
#pragma unroll
            for (int i = 0; i < 8; ++i) {
                got[i] = u32x4{};
                load_coh(got[i], lane * 16, xsrd, (u32)(((b ^ 8) * 4 + w) * 8 + i) * 1024u);
            }
        }
    });
    asm volatile("s_waitcnt vmcnt(0)" ::: "memory");
#pragma unroll
    for (int i = 0; i < 8; ++i) { PIN(got[i]); acc[i >> 2][i & 3] += __builtin_bit_cast(f32x4, got[i]) * omg[i & 3]; }
    STAMP(1, 5);
#pragma unroll
    for (int j = 0; j < 4; ++j) {
        const auto r0 = __builtin_amdgcn_permlane16_swap(cvtpk(acc[0][j][0], acc[0][j][1]), cvtpk(acc[1][j][0], acc[1][j][1]), false, false);
        const auto r1 = __builtin_amdgcn_permlane16_swap(cvtpk(acc[0][j][2], acc[0][j][3]), cvtpk(acc[1][j][2], acc[1][j][3]), false, false);
        const int s = s0 + j * 16 + c, d = d0 + w * 32 + (g & 1) * 16 + (g >> 1) * 8;
        *(u32x4*)(out + ((size_t)s * D + d) * 2) = u32x4{r0[0], r1[0], r0[1], r1[1]};
    }
}

constexpr int MAIN_LDS = Q_NS * Q_SLOT;
static_assert(MAIN_LDS >= P2_NS * P2_SLOT && MAIN_LDS >= P1_BASE + P1_NS * 8192 && MAIN_LDS >= 4 * KV_STAGE && 2 * MAIN_LDS <= 160 * 1024, "two workgroups per CU");

__global__ void __launch_bounds__(256, 2) k_main(const char* __restrict__ x, const char* __restrict__ wq, const char* __restrict__ w1, const char* __restrict__ b1, const char* __restrict__ w2, const char* __restrict__ b2,
                                                 const char* __restrict__ wo, char* __restrict__ ws, char* __restrict__ out) {
    extern __shared__ __attribute__((aligned(16))) char smem[];
    const int lane = threadIdx.x & 63, w = __builtin_amdgcn_readfirstlane(threadIdx.x >> 6), b = blockIdx.x;
    STAMP(1, 0);
    if (b < MID_ATT) {
        attention(b, x, wq, ws, (lds_ptr)smem, w, lane);
    } else {
        const int gb = b - MID_ATT;
        __builtin_amdgcn_s_setprio(G1_PRIO);
        if (gb < MID_G1) gemm1_tile(gb, w1, b1, ws, (lds_ptr)smem, w, lane);
        else {
            const u32 scratch = (u32)(uintptr_t)(lds_ptr)smem + w * 256;
            __builtin_amdgcn_s_sleep(127);
            __builtin_amdgcn_s_sleep(PF_SLEEP);
            for (int i = 0; i < 4; ++i) {
                touch<F * S * 2, MID_PF * 4, 256>(w2, (gb - MID_G1) * 4 + i, scratch);
                asm volatile("s_waitcnt vmcnt(0)" ::: "memory");
            }
            touch<D * D * 2, MID_PF, 256>(wo, gb - MID_G1, scratch);
            wait_bar<0>();
        }
        STAMP(1, 2);
        __builtin_amdgcn_s_setprio(POST_PRIO);
        post_tile(gb, w2, b2, wo, ws, out, (lds_ptr)smem, w, lane);
    }
    STAMP(1, 7);
}

extern "C" void kernel_launch(void* const* d_in, const int* in_sizes, int n_in, void* d_out, int out_size, void* d_ws, size_t ws_size,
                              hipStream_t stream) {
    const char* x = (const char*)d_in[0];
    const char *wq = (const char*)d_in[1], *wk = (const char*)d_in[2], *wv = (const char*)d_in[3], *wo = (const char*)d_in[4];
    const char *w1 = (const char*)d_in[6], *b1 = (const char*)d_in[7], *w2 = (const char*)d_in[8], *b2 = (const char*)d_in[9];
    const char* gw = (const char*)d_in[10];
    char* ws = (char*)d_ws;
    (void)hipFuncSetAttribute((const void*)k_pre, hipFuncAttributeMaxDynamicSharedMemorySize, PRE_LDS);
    (void)hipFuncSetAttribute((const void*)k_main, hipFuncAttributeMaxDynamicSharedMemorySize, MAIN_LDS);
    k_pre<<<PRE_KV + PRE_LN + PRE_PF, 512, PRE_LDS, stream>>>(x, wq, wk, wv, gw, w1, ws);
    k_main<<<MID_ATT + MID_G1 + MID_PF, 256, MAIN_LDS, stream>>>(x, wq, w1, b1, w2, b2, wo, ws, (char*)d_out);
}
```

```cpp
#define STAMPS
#define PROBE 0
#define PROBE_KB -1
#include <hip/hip_runtime.h>
#include <stdint.h>

typedef unsigned u32;
typedef u32 u32x4 __attribute__((ext_vector_type(4)));
typedef u32 u32x2 __attribute__((ext_vector_type(2)));
typedef float f32x2 __attribute__((ext_vector_type(2)));
typedef float f32x4 __attribute__((ext_vector_type(4)));
typedef float f32x16 __attribute__((ext_vector_type(16)));
typedef __bf16 bf16x2 __attribute__((ext_vector_type(2)));
typedef __bf16 bf16x8 __attribute__((ext_vector_type(8)));
typedef short s16x4 __attribute__((ext_vector_type(4)));
#define LDS __attribute__((address_space(3)))
typedef LDS char* lds_ptr;

constexpr int S = 2048, D = 1024, F = 3584;
constexpr u32 WS_HF = 0, WS_OF = 8u << 20, WS_UF = 12u << 20, WS_KV = 20u << 20;
constexpr u32 WS_G = WS_KV + (256u << 10), WS_BND = WS_G + (8u << 10), WS_CNT = WS_BND + (4u << 10);
constexpr u32 WS_X = 24u << 20;
constexpr int CNT_U = 0, CNT_O = 16, CNT_X = 32, CNT_N = 288, CNT_STRIDE = 256;
constexpr float QSCALE = 0.17677669529663687f * 1.4426950408889634f;
constexpr float SAFE_LOG2 = 60.f;
constexpr float SAFE_V = 1.0e12f;

#define SB() __builtin_amdgcn_sched_barrier(0)
#define PIN(x) asm volatile("" : "+v"(x))
#define M16(a, b, c) __builtin_amdgcn_mfma_f32_16x16x32_bf16(__builtin_bit_cast(bf16x8, a), __builtin_bit_cast(bf16x8, b), c, 0, 0, 0)
#define M32(a, b, c) __builtin_amdgcn_mfma_f32_32x32x16_bf16(__builtin_bit_cast(bf16x8, a), __builtin_bit_cast(bf16x8, b), c, 0, 0, 0)
__device__ __forceinline__ void mma16(f32x4& c, const u32x4& a, const u32x4& b) { asm volatile("v_mfma_f32_16x16x32_bf16 %0, %1, %2, %0" : "+v"(c) : "v"(a), "v"(b)); }
__device__ __forceinline__ void mfma_settle() { asm volatile("s_nop 7\n\ts_nop 1" ::: "memory"); SB(); }
#define EX2(x) __builtin_amdgcn_exp2f(x)

__device__ __forceinline__ u32 cvtpk(float lo, float hi) { f32x2 v = {lo, hi}; return __builtin_bit_cast(u32, __builtin_convertvector(v, bf16x2)); }
__device__ __forceinline__ float bf_lo(u32 v) { return __uint_as_float(v << 16); }
__device__ __forceinline__ float bf_hi(u32 v) { return __uint_as_float(v & 0xffff0000u); }
__device__ __forceinline__ float bf_round(float x) { return bf_lo(cvtpk(x, x)); }
__device__ __forceinline__ u32x4 pack8(const f32x4& a, const f32x4& b) { return u32x4{cvtpk(a[0], a[1]), cvtpk(a[2], a[3]), cvtpk(b[0], b[1]), cvtpk(b[2], b[3])}; }

__device__ __forceinline__ u32x4 make_srd(const void* p, u32 bytes) {
    const uint64_t a = (uint64_t)p;
    return u32x4{(u32)a, (u32)(a >> 32) & 0xffffu, bytes, 0x00020000u};
}
__device__ __forceinline__ void dma16(u32 lds, u32 voff, const u32x4& srd, u32 soff) {
    asm volatile("s_mov_b32 m0, %0\n\ts_nop 0\n\tbuffer_load_dwordx4 %1, %2, %3 offen lds" ::"s"(lds), "v"(voff), "s"(srd), "s"(soff) : "memory");
}
__device__ __forceinline__ void dma16_coh(u32 lds, u32 voff, const u32x4& srd, u32 soff) {
    asm volatile("s_mov_b32 m0, %0\n\ts_nop 0\n\tbuffer_load_dwordx4 %1, %2, %3 offen sc1 lds" ::"s"(lds), "v"(voff), "s"(srd), "s"(soff) : "memory");
}
template <int N> __device__ __forceinline__ void wait_bar() {
    __builtin_amdgcn_s_waitcnt(0xc07f);
    asm volatile("s_waitcnt vmcnt(%0)\n\ts_barrier" ::"n"(N) : "memory");
}
__device__ __forceinline__ u32x4 lds_rd128(lds_ptr p) { return *(LDS const u32x4*)p; }
__device__ __forceinline__ u32x2 lds_rdtr(lds_ptr p) { return __builtin_bit_cast(u32x2, __builtin_amdgcn_ds_read_tr16_b64_v4i16((LDS s16x4*)p)); }

typedef __attribute__((address_space(1))) u32 gu32;
__device__ __forceinline__ void store_wt(char* base, u32 bytes, u32 off, const u32x4& v) {
    __builtin_amdgcn_raw_buffer_store_b128(v, __builtin_amdgcn_make_buffer_rsrc(base, 0, bytes, 0x00020000), off, 0, 16);
}
__device__ __forceinline__ void load_coh(u32x4& f, u32 voff, const u32x4& srd, u32 soff) {
    asm volatile("buffer_load_dwordx4 %0, %1, %2, %3 offen sc1" : "+v"(f) : "v"(voff), "s"(srd), "s"(soff) : "memory");
}
__device__ __forceinline__ void signal(char* ws, int cnt) {
    if (threadIdx.x == 0) __hip_atomic_fetch_add((gu32*)(ws + WS_CNT + cnt * CNT_STRIDE), 1u, __ATOMIC_RELAXED, __HIP_MEMORY_SCOPE_AGENT);
}
__device__ __forceinline__ void publish(char* ws, int cnt) {
    asm volatile("s_waitcnt vmcnt(0)\n\ts_barrier" ::: "memory");
    signal(ws, cnt);
}
__device__ __forceinline__ void await(char* ws, int cnt, u32 target) {
    gu32* p = (gu32*)(ws + WS_CNT + cnt * CNT_STRIDE);
    for (int spins = 0; spins < (1 << 20); ++spins) {
        if (__builtin_amdgcn_readfirstlane(__hip_atomic_load(p, __ATOMIC_RELAXED, __HIP_MEMORY_SCOPE_AGENT)) >= target) break;
        __builtin_amdgcn_s_sleep(1);
    }
    asm volatile("" ::: "memory");
}

__device__ __forceinline__ void sample(u32& seen, char* ws, int cnt) {
    asm volatile("global_load_dword %0, %1, off sc1" : "+v"(seen) : "v"(ws + WS_CNT + cnt * CNT_STRIDE) : "memory");
}
__device__ __forceinline__ void confirm(u32& seen, char* ws, int cnt, u32 target) {
    PIN(seen);
    if (__builtin_amdgcn_readfirstlane(seen) < target) await(ws, cnt, target);
}

#define STAMP(k, i) do { if (threadIdx.x == 0) { unsigned long long* t_ = (unsigned long long*)(ws + (64u << 20)) + (((k) * 512 + blockIdx.x) * 8 + (i)) * 2; \
    t_[0] = __builtin_amdgcn_s_memrealtime(); t_[1] = __builtin_amdgcn_s_memtime(); } } while (0)

template <int R, int NR, int KSTR> struct OpFrag {
    static constexpr int BYTES = 4 * R * 16, PPW = BYTES / 4096, RPF = 1;
    static constexpr bool REG = false;
    u32x4 srd; u32 voff, ldst; lds_ptr rd;
    __device__ __forceinline__ void init(const void* base, u32 bytes, int row0, int k0, lds_ptr img, int w, int wrow, int lane) {
        srd = make_srd(base, bytes);
        voff = (u32)(row0 + lane + (w + 4 * k0) * NR) * 16u;
        ldst = (u32)(uintptr_t)img + w * (R * 16);
        rd = img + ((lane >> 4) * R + wrow + (lane & 15)) * 16;
    }
    __device__ __forceinline__ void issue(int slot, u32 pos, int i) const { dma16_coh(ldst + pos + i * 1024, voff, srd, (u32)slot * (KSTR * 4u * NR * 16u) + i * 1024u); }
    __device__ __forceinline__ void read(u32x4& f, int pos, int i, int) const { f = lds_rd128(rd + pos + i * 256); }
};

template <int C, int RS, int KS, bool NAT, int NF> struct OpTr {
    static constexpr int BYTES = KS * 32 * C * 2, PPW = BYTES / 4096, RPF = 2;
    static constexpr bool REG = false;
    u32x4 srd; u32 voff, ldst; lds_ptr rd[NF];
    __device__ __forceinline__ void init(const void* base, u32 bytes, int col0, int k0, lds_ptr img, int w, int wch, int lane) {
        srd = make_srd(base, bytes);
        ldst = (u32)(uintptr_t)img + w * 1024;
        const int g = lane >> 4, q = (lane >> 2) & 3, p = lane & 3;
        if constexpr (C == 128) {
            const int f = NAT ? (g | (((w >> 1) & 1) << 2)) : (4 * (w & 1) + g);
            voff = (u32)(32 * k0 + 4 * w + g) * RS + col0 * 2 + ((((lane & 15) >> 1) ^ f) * 32) + (lane & 1) * 16;
            const int row = NAT ? 8 * g + q : 4 * g + q, fr = NAT ? (q | ((g & 1) << 2)) : (row & 7);
#pragma unroll
            for (int i = 0; i < NF; ++i) rd[i] = img + row * 256 + (((wch ^ i) ^ fr) * 32) + p * 8;
        } else {
            static_assert(C == 128 || C == 64, "unsupported transposed operand");
            const int f = NAT ? ((g & 1) | ((w & 1) << 1)) : g;
            voff = (u32)(32 * k0 + 8 * w + (lane >> 3)) * RS + col0 * 2 + ((((lane & 7) >> 1) ^ f) * 32) + (lane & 1) * 16;
            const int row = NAT ? 8 * g + q : 4 * g + q, fr = NAT ? ((q >> 1) | ((g & 1) << 1)) : ((row >> 1) & 3);
#pragma unroll
            for (int i = 0; i < NF; ++i) rd[i] = img + row * 128 + (((wch ^ i) ^ fr) * 32) + p * 8;
        }
    }
    __device__ __forceinline__ void issue(int slot, u32 pos, int i) const { dma16(ldst + pos + i * 4096, voff, srd, (u32)(slot * KS * 32 + i * (2048 / C)) * RS); }
    __device__ __forceinline__ void read(u32x4& f, int pos, int i, int t) const {
        const u32x2 v = lds_rdtr(rd[i % NF] + pos + i / NF * (32 * C * 2) + t * ((NAT ? 4 : 16) * C * 2));
        f[2 * t] = v[0]; f[2 * t + 1] = v[1];
    }
};

struct OpRow {
    static constexpr int BYTES = 128 * 128, PPW = 4, RPF = 1;
    static constexpr bool REG = false;
    u32x4 srd; u32 voff, ldst; lds_ptr rd[2];
    __device__ __forceinline__ void init(const void* base, u32 bytes, int row0, lds_ptr img, int w, int wrow, int lane) {
        srd = make_srd(base, bytes);
        ldst = (u32)(uintptr_t)img + w * 1024;
        const int g = lane >> 4, c = lane & 15;
        voff = (u32)(row0 + 8 * w + (lane >> 3)) * (D * 2) + (((lane & 7) ^ (4 * (w & 1) + g)) * 16);
#pragma unroll
        for (int ks = 0; ks < 2; ++ks) rd[ks] = img + (wrow + c) * 128 + (((4 * ks + g) ^ (c >> 1)) * 16);
    }
    __device__ __forceinline__ void issue(int slot, u32 pos, int i) const { dma16(ldst + pos + i * 4096, voff, srd, (u32)slot * 128u + i * (32u * D * 2u)); }
    __device__ __forceinline__ void read(u32x4& f, int pos, int i, int) const { f = lds_rd128(rd[i >> 2] + pos + (i & 3) * 2048); }
};

template <int NF, int NR, int KSTR, bool COH> struct OpReg {
    static constexpr int PPW = NF, RPF = 0;
    static constexpr bool REG = true;
    u32x4 srd; u32 voff;
    __device__ __forceinline__ void init(const void* base, u32 bytes, int row0, int k0, int lane) {
        srd = make_srd(base, bytes);
        voff = (u32)(row0 + (lane & 15) + ((lane >> 4) + 4 * k0) * NR) * 16u;
    }
    __device__ __forceinline__ void load(u32x4& f, int slot, int i) const {
        const u32 soff = (u32)slot * (KSTR * 4u * NR * 16u);
        if (COH) asm volatile("buffer_load_dwordx4 %0, %1, %2, %3 offen offset:%4 sc1" : "+v"(f) : "v"(voff), "s"(srd), "s"(soff), "n"(i * 256) : "memory");
        else asm volatile("buffer_load_dwordx4 %0, %1, %2, %3 offen offset:%4" : "+v"(f) : "v"(voff), "s"(srd), "s"(soff), "n"(i * 256) : "memory");
    }
};

template <int NA, int NB> struct Frags { u32x4 a[NA], b[NB]; };
template <int NS, int NA, int NB, int KS = 1> struct Engine { f32x4 acc[NA][NB]; Frags<NA * KS, NB * KS> f[2]; u32x4 ring[NS][NA]; };

#define STAG 1
#define RDX 2
template <int KS, int SWAP, bool READ, bool ISSUE, bool RISSUE, int NA, int NB, class A, class B>
__device__ __forceinline__ void gemm_step(f32x4 (&acc)[NA][NB], const Frags<NA * KS, NB * KS>& cur, Frags<NA * KS, NB * KS>& nxt, u32x4 (&ring)[NA],
                                          u32x4 (&prev)[NA], const A& a, const B& b, int rpos, int islot, u32 ipos) {
    constexpr int NM = NA * NB * KS, NRA = NA * A::RPF, NRK = NRA + NB * B::RPF, NRD = NRK * KS, RPM = (RDX * NRD + NM - 1) / NM;
    constexpr int ND = A::PPW + B::PPW, DSTEP = NM / ND, DOFF = STAG % DSTEP;
    if constexpr (A::REG) {
#pragma unroll
        for (int i = 0; i < NA; ++i) PIN(ring[i]);
    }
#pragma unroll
    for (int m = 0; m < NM; ++m) {
        const int i = m % NA, j = m / NA % NB, ks = m / (NA * NB);
        const u32x4 av = A::REG ? ring[i] : cur.a[ks * NA + i];
        if (i >= SWAP) mma16(acc[i][j], cur.b[ks * NB + j], av);
        else mma16(acc[i][j], av, cur.b[ks * NB + j]);
        if constexpr (READ) {
#pragma unroll
            for (int rr = m * RPM; rr < (m + 1) * RPM && rr < NRD; ++rr) {
                const int k = rr / NRK, r = rr % NRK;
                if (r < B::RPF) b.read(nxt.b[k * NB], rpos, k * NB, r);
                else if (r >= B::RPF + NRA) b.read(nxt.b[k * NB + (r - NRA) / B::RPF], rpos, k * NB + (r - NRA) / B::RPF, (r - NRA) % B::RPF);
                else if constexpr (!A::REG) a.read(nxt.a[k * NA + (r - B::RPF) / A::RPF], rpos, k * NA + (r - B::RPF) / A::RPF, (r - B::RPF) % A::RPF);
            }
        }
        if (m % DSTEP == DOFF && m / DSTEP < ND) {
            const int d = m / DSTEP;
            if (d < B::PPW) { if constexpr (ISSUE) b.issue(islot, ipos, d); }
            else if constexpr (A::REG) { if constexpr (RISSUE) a.load(prev[d - B::PPW], islot - 1, d - B::PPW); }
            else if constexpr (ISSUE) a.issue(islot, ipos, d - B::PPW);
        }
        SB();
    }
}

template <int V> struct IC { static constexpr int v = V; };
template <int NS, int PA, int PB, bool REG> constexpr int younger(int rem) {
    if (rem == 2) return 0;
    int n = 0;
    for (int k = 1; k <= NS - 2; ++k) n += (rem + k > NS ? PB + (REG ? 0 : PA) : 0) + (REG && rem + k > NS - 1 ? PA : 0);
    return n;
}
template <int P, int COUNT, int REM, int SLOTB, int SWAP, int XW, int NS, int NA, int NB, int KS, class A, class B, class Hook>
__device__ __forceinline__ void gemm_steps(Engine<NS, NA, NB, KS>& e, const A& a, const B& b, int islot, const Hook& hook) {
    constexpr bool READ = REM > 1;
    if constexpr (READ) wait_bar<younger<NS, A::PPW, B::PPW, A::REG>(REM) + (P < NS - 2 ? XW : 0)>();
    hook(IC<P>{});
    gemm_step<KS, SWAP, READ, (REM > NS), (A::REG && REM > NS - 1)>(e.acc, e.f[P & 1], e.f[(P & 1) ^ 1], e.ring[P % NS], e.ring[(P + NS - 1) % NS], a, b,
                                                                   (P + 1) % NS * SLOTB, islot + P, P % NS * SLOTB);
    if constexpr (COUNT > 1) gemm_steps<P + 1, COUNT - 1, REM - 1, SLOTB, SWAP, XW>(e, a, b, islot, hook);
    else if constexpr (REM == 1) mfma_settle();
}

template <int SLOTB, class A, class B> __device__ __forceinline__ void gemm_fill(const A& a, const B& b, int p0, int p1) {
#pragma unroll
    for (int p = p0; p < p1; ++p) {
#pragma unroll
        for (int i = 0; i < B::PPW; ++i) b.issue(p, p * SLOTB, i);
        if constexpr (!A::REG) {
#pragma unroll
            for (int i = 0; i < A::PPW; ++i) a.issue(p, p * SLOTB, i);
        }
    }
}
template <int NS, int NA, int NB, int KS, class A, class B> __device__ __forceinline__ void gemm_first(Engine<NS, NA, NB, KS>& e, const A& a, const B& b) {
    if constexpr (!A::REG) {
#pragma unroll
        for (int i = 0; i < KS * NA * A::RPF; ++i) a.read(e.f[0].a[i / A::RPF], 0, i / A::RPF, i % A::RPF);
    }
#pragma unroll
    for (int i = 0; i < KS * NB * B::RPF; ++i) b.read(e.f[0].b[i / B::RPF], 0, i / B::RPF, i % B::RPF);
    SB();
}

constexpr int MANY = 1 << 20;
template <int NS, int NSLOTS> struct Rounds { static constexpr int RL = NS % 2 ? 2 * NS : NS, N = (NSLOTS - NS) / RL, TAIL = NSLOTS - N * RL; };
template <int SLOTB, int NSLOTS, int SWAP, int NS, int NA, int NB, int KS, class A, class B, class Gate, class Round, class Tail>
__device__ __forceinline__ void gemm_run(Engine<NS, NA, NB, KS>& e, const A& a, const B& b, const Gate& gate, const Round& round, const Tail& tail) {
    using R = Rounds<NS, NSLOTS>;
#pragma unroll
    for (int i = 0; i < NA; ++i)
#pragma unroll
        for (int j = 0; j < NB; ++j) e.acc[i][j] = f32x4{};
    gemm_fill<SLOTB>(a, b, 0, NS);
    if constexpr (A::REG) {
        gate();
#pragma unroll
        for (int p = 0; p < NS; ++p)
#pragma unroll
            for (int i = 0; i < NA; ++i) { e.ring[p][i] = u32x4{}; if (p < NS - 1) a.load(e.ring[p][i], p, i); }
        wait_bar<NA>();
    } else {
        wait_bar<(NS - 1) * (A::PPW + B::PPW)>();
    }
    gemm_first(e, a, b);
    for (int r = 0; r < R::N; ++r) { round(r); gemm_steps<0, R::RL, MANY, SLOTB, SWAP, 0>(e, a, b, r * R::RL + NS, [](auto) {}); }
    gemm_steps<0, R::TAIL, R::TAIL, SLOTB, SWAP, 0>(e, a, b, R::N * R::RL + NS, tail);
}

constexpr int PRE_KV = 64, PRE_LN = 128, PRE_PF = 64, PRE_LDS = 8 * 16384 + 2048;

template <int CTRL> __device__ __forceinline__ float dpp_add(float v) {
    return v + __builtin_bit_cast(float, __builtin_amdgcn_update_dpp(0, __builtin_bit_cast(int, v), CTRL, 0xf, 0xf, true));
}
__device__ __forceinline__ float group_sum(float v) {
    const auto a = __builtin_amdgcn_permlane16_swap(__float_as_uint(v), __float_as_uint(v), false, false);
    v = __uint_as_float(a[0]) + __uint_as_float(a[1]);
    const auto b = __builtin_amdgcn_permlane32_swap(__float_as_uint(v), __float_as_uint(v), false, false);
    return __uint_as_float(b[0]) + __uint_as_float(b[1]);
}

__device__ __forceinline__ float row_sum(float v) { return dpp_add<0x140>(dpp_add<0x141>(dpp_add<0x4e>(dpp_add<0xb1>(v)))); }
__device__ __forceinline__ float fold32(float a, float b) {
    const auto r = __builtin_amdgcn_permlane32_swap(__float_as_uint(a), __float_as_uint(b), false, false);
    return __uint_as_float(r[0]) + __uint_as_float(r[1]);
}
__device__ __forceinline__ float fold16(float a, float b) {
    const auto r = __builtin_amdgcn_permlane16_swap(__float_as_uint(a), __float_as_uint(b), false, false);
    return __uint_as_float(r[0]) + __uint_as_float(r[1]);
}
template <int CTRL> __device__ __forceinline__ float dpp_max(float v) {
    return fmaxf(v, __builtin_bit_cast(float, __builtin_amdgcn_update_dpp(0, __builtin_bit_cast(int, v), CTRL, 0xf, 0xf, true)));
}
__device__ __forceinline__ float wave_max(float v) {
    v = dpp_max<0xb1>(v); v = dpp_max<0x4e>(v); v = dpp_max<0x141>(v); v = dpp_max<0x140>(v);
    const auto a = __builtin_amdgcn_permlane16_swap(__float_as_uint(v), __float_as_uint(v), false, false);
    v = fmaxf(__uint_as_float(a[0]), __uint_as_float(a[1]));
    const auto b = __builtin_amdgcn_permlane32_swap(__float_as_uint(v), __float_as_uint(v), false, false);
    return fmaxf(__uint_as_float(b[0]), __uint_as_float(b[1]));
}

template <u32 BYTES, int NB, int NTHR> __device__ __forceinline__ void touch(const char* p, int b, u32 scratch) {
    constexpr u32 LINES = BYTES / 128, PER = (LINES + NB - 1) / NB, N = (PER + NTHR - 1) / NTHR;
    const u32x4 srd = make_srd(p, BYTES);
#pragma unroll
    for (u32 i = 0; i < N; ++i) {
        const u32 l = i * NTHR + threadIdx.x, off = l < PER ? (b * PER + l) * 128u : 0x80000000u;
        asm volatile("s_mov_b32 m0, %0\n\ts_nop 0\n\tbuffer_load_dword %1, %2, 0 offen lds" ::"s"(scratch), "v"(off), "s"(srd) : "memory");
    }
}

__device__ __forceinline__ void warm_pre(const char* wq, const char* w1, int b, u32 scratch) {
    touch<D * D * 2, PRE_LN + PRE_PF, 512>(wq, b, scratch);
    touch<S * F * 2, PRE_LN + PRE_PF, 512>(w1, b, scratch);
}

__device__ __forceinline__ void kv_block(int t, const char* x, const char* wk, const char* wv, char* ws, lds_ptr lds, int w8, int lane) {
    const int g = lane >> 4, c = lane & 15, q = (lane >> 2) & 3, p = lane & 3;
    const lds_ptr img = lds + w8 * 16384;
    const u32x4 sk = make_srd(wk, D * 64), sv = make_srd(wv, D * 64);
    const u32 voff = (u32)(lane >> 2) * 64 + ((((lane >> 1) & 1) ^ (lane >> 5)) * 32) + (lane & 1) * 16;
    u32x4 xf[2][4];
#pragma unroll
    for (int ks = 0; ks < 4; ++ks)
#pragma unroll
        for (int j = 0; j < 2; ++j) xf[j][ks] = *(const u32x4*)(x + (size_t)(t * 32 + j * 16 + c) * (D * 2) + (w8 * 128 + ks * 32 + g * 8) * 2);
#pragma unroll
    for (int i = 0; i < 8; ++i) {
        dma16((u32)(uintptr_t)img + i * 1024, voff, sk, w8 * 8192 + i * 1024);
        dma16((u32)(uintptr_t)img + 8192 + i * 1024, voff, sv, w8 * 8192 + i * 1024);
    }
    asm volatile("s_waitcnt vmcnt(0)" ::: "memory");
    f32x4 acc[8];
#pragma unroll
    for (int i = 0; i < 8; ++i) acc[i] = f32x4{};
#pragma unroll
    for (int ks = 0; ks < 4; ++ks)
#pragma unroll
        for (int i = 0; i < 4; ++i) {
            const lds_ptr rd = img + (i >> 1) * 8192 + (ks * 32 + 8 * g + q) * 64 + (((i & 1) ^ (g & 1)) * 32) + p * 8;
            const u32x2 lo = lds_rdtr(rd), hi = lds_rdtr(rd + 256);
            const u32x4 wf = {lo[0], lo[1], hi[0], hi[1]};
#pragma unroll
            for (int j = 0; j < 2; ++j) acc[2 * i + j] = i < 2 ? M16(wf, xf[j][ks], acc[2 * i + j]) : M16(xf[j][ks], wf, acc[2 * i + j]);
        }
#pragma unroll
    for (int i = 0; i < 8; ++i) *(LDS f32x4*)(img + i * 1024 + lane * 16) = acc[i];
    wait_bar<0>();
    STAMP(0, 6);
    if (w8 >= 4) return;
    const int ta = w8 < 2 ? w8 : 2 * w8, tb = w8 < 2 ? w8 + 2 : 2 * w8 + 1;
    f32x4 a = {}, b = {};
#pragma unroll
    for (int v = 0; v < 8; ++v) { a += *(LDS const f32x4*)(lds + v * 16384 + ta * 1024 + lane * 16); b += *(LDS const f32x4*)(lds + v * 16384 + tb * 1024 + lane * 16); }
    *(u32x4*)(ws + WS_KV + t * 4096 + w8 * 1024 + lane * 16) = pack8(a, b);
    float bnd = 0.f;
    if (w8 < 2) {
#pragma unroll
        for (int r = 0; r < 4; ++r) bnd += a[r] * a[r] + b[r] * b[r];
        bnd = group_sum(bnd);
        bnd = bnd == bnd ? bnd : __builtin_inff();
    } else {
#pragma unroll
        for (int r = 0; r < 4; ++r)
            if (!(fabsf(a[r]) <= SAFE_V && fabsf(b[r]) <= SAFE_V)) bnd = __builtin_inff();
    }
    bnd = wave_max(bnd);
    if (lane == 0) ((float*)(ws + WS_BND))[t * 4 + w8] = bnd;
}

__device__ __forceinline__ void ln_rows(int r, const char* x, const char* gw, const char* wq, const char* w1, char* ws, lds_ptr lds, u32 scratch, int w8, int lane) {
    const int u = w8 >> 2, cq = w8 & 3, chunk = 2 * r + u, row0 = 32 * (chunk >> 2) + 4 * (chunk & 3);
    u32x2 xv[8];
    float sh[8];
#pragma unroll
    for (int e = 0; e < 8; ++e) {
        const char* row = x + (size_t)(row0 + (e & 3) + 16 * (e >> 2)) * (D * 2);
        xv[e] = *(const u32x2*)(row + cq * 512 + lane * 8);
        sh[e] = bf_lo(*(const u32*)row);
    }
    u32x2 gv = *(const u32x2*)(gw + cq * 512 + lane * 8);
    PIN(gv);
#pragma unroll
    for (int e = 0; e < 8; ++e) PIN(xv[e]);
    warm_pre(wq, w1, r, scratch);
    float part[3][8];
#pragma unroll
    for (int e = 0; e < 8; ++e) {
        const float a0 = bf_lo(xv[e][0]), a1 = bf_hi(xv[e][0]), a2 = bf_lo(xv[e][1]), a3 = bf_hi(xv[e][1]);
        const float d0 = a0 - sh[e], d1 = a1 - sh[e], d2 = a2 - sh[e], d3 = a3 - sh[e];
        part[0][e] = (d0 + d1) + (d2 + d3);
        part[1][e] = (d0 * d0 + d1 * d1) + (d2 * d2 + d3 * d3);
        part[2][e] = (a0 * bf_lo(gv[0]) + a1 * bf_hi(gv[0])) + (a2 * bf_lo(gv[1]) + a3 * bf_hi(gv[1]));
    }
    LDS float* red = (LDS float*)(lds + 65536);
#pragma unroll
    for (int k = 0; k < 3; ++k)
#pragma unroll
        for (int h = 0; h < 2; ++h) {
            const float v = row_sum(fold16(fold32(part[k][4 * h], part[k][4 * h + 2]), fold32(part[k][4 * h + 1], part[k][4 * h + 3])));
            if ((lane & 15) == 0) red[w8 * 24 + k * 8 + 4 * h + (lane >> 4)] = v;
        }
    wait_bar<63>();
    STAMP(0, 6);
    const int e8 = lane & 7;
    float st[3], mysh = 0.f;
#pragma unroll
    for (int k = 0; k < 3; ++k) st[k] = (red[(4 * u) * 24 + k * 8 + e8] + red[(4 * u + 1) * 24 + k * 8 + e8]) + (red[(4 * u + 2) * 24 + k * 8 + e8] + red[(4 * u + 3) * 24 + k * 8 + e8]);
#pragma unroll
    for (int e = 0; e < 8; ++e) mysh = e8 == e ? sh[e] : mysh;
    const float dm = st[0] * (1.f / D), var = fmaxf(st[1] * (1.f / D) - dm * dm, 0.f);
    const float mean = bf_round(mysh + dm), rs = 1.f / bf_round(sqrtf(bf_round(bf_round(var) + 1e-5f)));
    if (cq == 0 && lane < 8) ((float*)(ws + WS_G))[row0 + (lane & 3) + 16 * (lane >> 2)] = bf_round(1.f / (1.f + __expf(-bf_round(st[2]))));
    float h[4][8];
#pragma unroll
    for (int e = 0; e < 8; ++e) {
        const float m = __uint_as_float(__builtin_amdgcn_readlane(__float_as_uint(mean), e)), q = __uint_as_float(__builtin_amdgcn_readlane(__float_as_uint(rs), e));
        h[0][e] = (bf_lo(xv[e][0]) - m) * q; h[1][e] = (bf_hi(xv[e][0]) - m) * q; h[2][e] = (bf_lo(xv[e][1]) - m) * q; h[3][e] = (bf_hi(xv[e][1]) - m) * q;
    }
    const lds_ptr tb = lds + w8 * 4096;
#pragma unroll
    for (int m = 0; m < 4; ++m)
        *(LDS u32x4*)(tb + lane * 64 + ((m ^ ((lane >> 1) & 3)) * 16)) = u32x4{cvtpk(h[m][0], h[m][1]), cvtpk(h[m][2], h[m][3]), cvtpk(h[m][4], h[m][5]), cvtpk(h[m][6], h[m][7])};
    char* dst = ws + WS_HF + (size_t)chunk * (D * 16) + cq * 4096 + lane * 16;
#pragma unroll
    for (int i = 0; i < 4; ++i) *(u32x4*)(dst + i * 1024) = lds_rd128(tb + (i * 16 + (lane >> 2)) * 64 + (((lane & 3) ^ ((lane >> 3) & 3)) * 16));
}

__global__ void __launch_bounds__(512, 2) k_pre(const char* __restrict__ x, const char* __restrict__ wq, const char* __restrict__ wk, const char* __restrict__ wv,
                                                const char* __restrict__ gw, const char* __restrict__ w1, char* __restrict__ ws) {
    extern __shared__ __attribute__((aligned(16))) char smem[];
    const int lane = threadIdx.x & 63, w8 = __builtin_amdgcn_readfirstlane(threadIdx.x >> 6), b = blockIdx.x;
    STAMP(0, 0);
    if (b < PRE_KV) kv_block(b, x, wk, wv, ws, (lds_ptr)smem, w8, lane);
    else {
        const int r = b - PRE_KV;
        const u32 scratch = (u32)(uintptr_t)(lds_ptr)smem + 8 * 16384 + w8 * 256;
        if (r < PRE_LN) ln_rows(r, x, gw, wq, w1, ws, (lds_ptr)smem, scratch, w8, lane);
        else {
            warm_pre(wq, w1, r, scratch);
            if (r == PRE_LN && threadIdx.x < CNT_N) *(u32*)(ws + WS_CNT + threadIdx.x * CNT_STRIDE) = 0u;
        }
        asm volatile("s_waitcnt vmcnt(0)" ::: "memory");
    }
    STAMP(0, 7);
}

#define G1_PRIO 1
#define G1_NS_ 6
#define POST_PRIO 0
#define G_SLEEP 0
#define PF_SLEEP 64
#define Q_PRIO 2
#define PRIO_SWITCH 99
#define ATT_PRIO 0
constexpr int MID_ATT = 256, MID_G1 = 224, MID_PF = 32, G1_NS = G1_NS_;
constexpr int KV_STAGE = 16384, KV_NSTAGE = 16;

#define ATT_LOOP_ASM \
    "s_mov_b32 s23, 16\n" \
    "s_mov_b32 s24, 0xc000\n" \
    "s_mov_b32 s22, 0\n" \
    "s_add_u32 s20, %[ldst], 0x800\n" \
    "s_mov_b32 s21, 0x8800\n" \
    "ATT_LOOP_%=:\n" \
    "v_mfma_f32_16x16x32_bf16 v[8:11], v[80:83], v[68:71], 0\n" \
    "v_exp_f32 v0, v0\n" \
    "v_exp_f32 v1, v1\n" \
    "v_mfma_f32_16x16x32_bf16 v[12:15], v[84:87], v[68:71], 0\n" \
    "ds_read_b128 v[96:99], v124 offset:4096\n" \
    "v_exp_f32 v2, v2\n" \
    "v_exp_f32 v3, v3\n" \
    "v_mfma_f32_16x16x32_bf16 v[40:43], v[104:107], v[120:123], v[40:43]\n" \
    "v_cvt_pk_bf16_f32 v116, v0, v1\n" \
    "v_exp_f32 v4, v4\n" \
    "v_exp_f32 v5, v5\n" \
    "v_mfma_f32_16x16x32_bf16 v[44:47], v[108:111], v[120:123], v[44:47]\n" \
    "s_mov_b32 m0, s20\n" \
    "s_add_u32 s20, s20, 0x400\n" \
    "buffer_load_dwordx4 v126, %[srd], s21 offen lds\n" \
    "s_add_u32 s21, s21, 0x400\n" \
    "v_cvt_pk_bf16_f32 v117, v2, v3\n" \
    "v_exp_f32 v6, v6\n" \
    "v_exp_f32 v7, v7\n" \
    "v_mfma_f32_16x16x32_bf16 v[60:63], v[112:115], v[120:123], v[60:63]\n" \
    "v_cvt_pk_bf16_f32 v118, v4, v5\n" \
    "v_cvt_pk_bf16_f32 v119, v6, v7\n" \
    "s_waitcnt lgkmcnt(1)\n" \
    "v_mfma_f32_16x16x32_bf16 v[0:3], v[80:83], v[72:75], 0\n" \
    "v_exp_f32 v8, v8\n" \
    "v_exp_f32 v9, v9\n" \
    "v_mfma_f32_16x16x32_bf16 v[4:7], v[84:87], v[72:75], 0\n" \
    "ds_read_b128 v[100:103], v124 offset:5120\n" \
    "v_exp_f32 v10, v10\n" \
    "v_exp_f32 v11, v11\n" \
    "v_mfma_f32_16x16x32_bf16 v[16:19], v[88:91], v[116:119], v[16:19]\n" \
    "v_cvt_pk_bf16_f32 v120, v8, v9\n" \
    "v_exp_f32 v12, v12\n" \
    "v_exp_f32 v13, v13\n" \
    "v_mfma_f32_16x16x32_bf16 v[20:23], v[92:95], v[116:119], v[20:23]\n" \
    "v_cvt_pk_bf16_f32 v121, v10, v11\n" \
    "v_exp_f32 v14, v14\n" \
    "v_exp_f32 v15, v15\n" \
    "v_mfma_f32_16x16x32_bf16 v[48:51], v[112:115], v[116:119], v[48:51]\n" \
    "v_cvt_pk_bf16_f32 v122, v12, v13\n" \
    "v_cvt_pk_bf16_f32 v123, v14, v15\n" \
    "v_mfma_f32_16x16x32_bf16 v[8:11], v[80:83], v[76:79], 0\n" \
    "v_exp_f32 v0, v0\n" \
    "v_exp_f32 v1, v1\n" \
    "v_mfma_f32_16x16x32_bf16 v[12:15], v[84:87], v[76:79], 0\n" \
    "ds_read_b128 v[104:107], v124 offset:6144\n" \
    "v_exp_f32 v2, v2\n" \
    "v_exp_f32 v3, v3\n" \
    "v_mfma_f32_16x16x32_bf16 v[24:27], v[88:91], v[120:123], v[24:27]\n" \
    "v_cvt_pk_bf16_f32 v116, v0, v1\n" \
    "v_exp_f32 v4, v4\n" \
    "v_exp_f32 v5, v5\n" \
    "v_mfma_f32_16x16x32_bf16 v[28:31], v[92:95], v[120:123], v[28:31]\n" \
    "s_mov_b32 m0, s20\n" \
    "s_add_u32 s20, s20, 0x400\n" \
    "buffer_load_dwordx4 v126, %[srd], s21 offen lds\n" \
    "s_add_u32 s21, s21, 0x400\n" \
    "s_add_u32 s22, s22, 0x4000\n" \
    "s_and_b32 s22, s22, 0xffff\n" \
    "s_add_u32 s20, s22, %[ldst]\n" \
    "s_add_u32 s21, s21, 0x3000\n" \
    "v_cvt_pk_bf16_f32 v117, v2, v3\n" \
    "v_exp_f32 v6, v6\n" \
    "v_exp_f32 v7, v7\n" \
    "v_mfma_f32_16x16x32_bf16 v[52:55], v[112:115], v[120:123], v[52:55]\n" \
    "v_cvt_pk_bf16_f32 v118, v4, v5\n" \
    "v_cvt_pk_bf16_f32 v119, v6, v7\n" \
    "s_waitcnt lgkmcnt(1)\n" \
    "v_mfma_f32_16x16x32_bf16 v[0:3], v[96:99], v[64:67], 0\n" \
    "v_exp_f32 v8, v8\n" \
    "v_exp_f32 v9, v9\n" \
    "v_mfma_f32_16x16x32_bf16 v[4:7], v[100:103], v[64:67], 0\n" \
    "ds_read_b128 v[108:111], v124 offset:7168\n" \
    "v_exp_f32 v10, v10\n" \
    "v_exp_f32 v11, v11\n" \
    "v_mfma_f32_16x16x32_bf16 v[32:35], v[88:91], v[116:119], v[32:35]\n" \
    "v_cvt_pk_bf16_f32 v120, v8, v9\n" \
    "v_exp_f32 v12, v12\n" \
    "v_exp_f32 v13, v13\n" \
    "v_mfma_f32_16x16x32_bf16 v[36:39], v[92:95], v[116:119], v[36:39]\n" \
    "v_cvt_pk_bf16_f32 v121, v10, v11\n" \
    "v_exp_f32 v14, v14\n" \
    "v_exp_f32 v15, v15\n" \
    "v_mfma_f32_16x16x32_bf16 v[56:59], v[112:115], v[116:119], v[56:59]\n" \
    "v_cvt_pk_bf16_f32 v122, v12, v13\n" \
    "v_cvt_pk_bf16_f32 v123, v14, v15\n" \
    "v_mfma_f32_16x16x32_bf16 v[8:11], v[96:99], v[68:71], 0\n" \
    "v_exp_f32 v0, v0\n" \
    "v_exp_f32 v1, v1\n" \
    "v_mfma_f32_16x16x32_bf16 v[12:15], v[100:103], v[68:71], 0\n" \
    "ds_read_b128 v[80:83], v124 offset:8192\n" \
    "v_exp_f32 v2, v2\n" \
    "v_exp_f32 v3, v3\n" \
    "v_mfma_f32_16x16x32_bf16 v[40:43], v[88:91], v[120:123], v[40:43]\n" \
    "v_cvt_pk_bf16_f32 v116, v0, v1\n" \
    "v_exp_f32 v4, v4\n" \
    "v_exp_f32 v5, v5\n" \
    "v_mfma_f32_16x16x32_bf16 v[44:47], v[92:95], v[120:123], v[44:47]\n" \
    "v_cvt_pk_bf16_f32 v117, v2, v3\n" \
    "v_exp_f32 v6, v6\n" \
    "v_exp_f32 v7, v7\n" \
    "v_mfma_f32_16x16x32_bf16 v[60:63], v[112:115], v[120:123], v[60:63]\n" \
    "v_cvt_pk_bf16_f32 v118, v4, v5\n" \
    "v_cvt_pk_bf16_f32 v119, v6, v7\n" \
    "s_waitcnt lgkmcnt(1)\n" \
    "v_mfma_f32_16x16x32_bf16 v[0:3], v[96:99], v[72:75], 0\n" \
    "v_exp_f32 v8, v8\n" \
    "v_exp_f32 v9, v9\n" \
    "v_mfma_f32_16x16x32_bf16 v[4:7], v[100:103], v[72:75], 0\n" \
    "ds_read_b128 v[84:87], v124 offset:9216\n" \
    "v_exp_f32 v10, v10\n" \
    "v_exp_f32 v11, v11\n" \
    "v_mfma_f32_16x16x32_bf16 v[16:19], v[104:107], v[116:119], v[16:19]\n" \
    "v_cvt_pk_bf16_f32 v120, v8, v9\n" \
    "v_exp_f32 v12, v12\n" \
    "v_exp_f32 v13, v13\n" \
    "v_mfma_f32_16x16x32_bf16 v[20:23], v[108:111], v[116:119], v[20:23]\n" \
    "v_cvt_pk_bf16_f32 v121, v10, v11\n" \
    "v_exp_f32 v14, v14\n" \
    "v_exp_f32 v15, v15\n" \
    "v_mfma_f32_16x16x32_bf16 v[48:51], v[112:115], v[116:119], v[48:51]\n" \
    "v_cvt_pk_bf16_f32 v122, v12, v13\n" \
    "v_cvt_pk_bf16_f32 v123, v14, v15\n" \
    "v_mfma_f32_16x16x32_bf16 v[8:11], v[96:99], v[76:79], 0\n" \
    "v_exp_f32 v0, v0\n" \
    "v_exp_f32 v1, v1\n" \
    "v_mfma_f32_16x16x32_bf16 v[12:15], v[100:103], v[76:79], 0\n" \
    "ds_read_b128 v[88:91], v124 offset:10240\n" \
    "v_exp_f32 v2, v2\n" \
    "v_exp_f32 v3, v3\n" \
    "v_mfma_f32_16x16x32_bf16 v[24:27], v[104:107], v[120:123], v[24:27]\n" \
    "v_cvt_pk_bf16_f32 v116, v0, v1\n" \
    "v_exp_f32 v4, v4\n" \
    "v_exp_f32 v5, v5\n" \
    "v_mfma_f32_16x16x32_bf16 v[28:31], v[108:111], v[120:123], v[28:31]\n" \
    "v_cvt_pk_bf16_f32 v117, v2, v3\n" \
    "v_exp_f32 v6, v6\n" \
    "v_exp_f32 v7, v7\n" \
    "v_mfma_f32_16x16x32_bf16 v[52:55], v[112:115], v[120:123], v[52:55]\n" \
    "v_cvt_pk_bf16_f32 v118, v4, v5\n" \
    "v_cvt_pk_bf16_f32 v119, v6, v7\n" \
    "s_waitcnt lgkmcnt(1)\n" \
    "v_mfma_f32_16x16x32_bf16 v[0:3], v[80:83], v[64:67], 0\n" \
    "v_exp_f32 v8, v8\n" \
    "v_exp_f32 v9, v9\n" \
    "v_mfma_f32_16x16x32_bf16 v[4:7], v[84:87], v[64:67], 0\n" \
    "ds_read_b128 v[92:95], v124 offset:11264\n" \
    "v_exp_f32 v10, v10\n" \
    "v_exp_f32 v11, v11\n" \
    "v_mfma_f32_16x16x32_bf16 v[32:35], v[104:107], v[116:119], v[32:35]\n" \
    "v_cvt_pk_bf16_f32 v120, v8, v9\n" \
    "v_exp_f32 v12, v12\n" \
    "v_exp_f32 v13, v13\n" \
    "v_mfma_f32_16x16x32_bf16 v[36:39], v[108:111], v[116:119], v[36:39]\n" \
    "v_cvt_pk_bf16_f32 v121, v10, v11\n" \
    "v_exp_f32 v14, v14\n" \
    "v_exp_f32 v15, v15\n" \
    "v_mfma_f32_16x16x32_bf16 v[56:59], v[112:115], v[116:119], v[56:59]\n" \
    "v_cvt_pk_bf16_f32 v122, v12, v13\n" \
    "v_cvt_pk_bf16_f32 v123, v14, v15\n" \
    "v_mfma_f32_16x16x32_bf16 v[8:11], v[80:83], v[68:71], 0\n" \
    "v_exp_f32 v0, v0\n" \
    "v_exp_f32 v1, v1\n" \
    "v_mfma_f32_16x16x32_bf16 v[12:15], v[84:87], v[68:71], 0\n" \
    "ds_read_b128 v[96:99], v124 offset:12288\n" \
    "v_exp_f32 v2, v2\n" \
    "v_exp_f32 v3, v3\n" \
    "v_mfma_f32_16x16x32_bf16 v[40:43], v[104:107], v[120:123], v[40:43]\n" \
    "v_cvt_pk_bf16_f32 v116, v0, v1\n" \
    "v_exp_f32 v4, v4\n" \
    "v_exp_f32 v5, v5\n" \
    "v_mfma_f32_16x16x32_bf16 v[44:47], v[108:111], v[120:123], v[44:47]\n" \
    "v_cvt_pk_bf16_f32 v117, v2, v3\n" \
    "v_exp_f32 v6, v6\n" \
    "v_exp_f32 v7, v7\n" \
    "v_mfma_f32_16x16x32_bf16 v[60:63], v[112:115], v[120:123], v[60:63]\n" \
    "v_cvt_pk_bf16_f32 v118, v4, v5\n" \
    "v_cvt_pk_bf16_f32 v119, v6, v7\n" \
    "s_waitcnt lgkmcnt(1)\n" \
    "v_mfma_f32_16x16x32_bf16 v[0:3], v[80:83], v[72:75], 0\n" \
    "v_exp_f32 v8, v8\n" \
    "v_exp_f32 v9, v9\n" \
    "v_mfma_f32_16x16x32_bf16 v[4:7], v[84:87], v[72:75], 0\n" \
    "ds_read_b128 v[100:103], v124 offset:13312\n" \
    "v_exp_f32 v10, v10\n" \
    "v_exp_f32 v11, v11\n" \
    "v_mfma_f32_16x16x32_bf16 v[16:19], v[88:91], v[116:119], v[16:19]\n" \
    "v_cvt_pk_bf16_f32 v120, v8, v9\n" \
    "v_exp_f32 v12, v12\n" \
    "v_exp_f32 v13, v13\n" \
    "v_mfma_f32_16x16x32_bf16 v[20:23], v[92:95], v[116:119], v[20:23]\n" \
    "v_cvt_pk_bf16_f32 v121, v10, v11\n" \
    "v_exp_f32 v14, v14\n" \
    "v_exp_f32 v15, v15\n" \
    "v_mfma_f32_16x16x32_bf16 v[48:51], v[112:115], v[116:119], v[48:51]\n" \
    "v_cvt_pk_bf16_f32 v122, v12, v13\n" \
    "v_cvt_pk_bf16_f32 v123, v14, v15\n" \
    "v_mfma_f32_16x16x32_bf16 v[8:11], v[80:83], v[76:79], 0\n" \
    "v_exp_f32 v0, v0\n" \
    "v_exp_f32 v1, v1\n" \
    "v_mfma_f32_16x16x32_bf16 v[12:15], v[84:87], v[76:79], 0\n" \
    "ds_read_b128 v[104:107], v124 offset:14336\n" \
    "v_exp_f32 v2, v2\n" \
    "v_exp_f32 v3, v3\n" \
    "v_mfma_f32_16x16x32_bf16 v[24:27], v[88:91], v[120:123], v[24:27]\n" \
    "v_cvt_pk_bf16_f32 v116, v0, v1\n" \
    "v_exp_f32 v4, v4\n" \
    "v_exp_f32 v5, v5\n" \
    "v_mfma_f32_16x16x32_bf16 v[28:31], v[92:95], v[120:123], v[28:31]\n" \
    "v_cvt_pk_bf16_f32 v117, v2, v3\n" \
    "v_exp_f32 v6, v6\n" \
    "v_exp_f32 v7, v7\n" \
    "v_mfma_f32_16x16x32_bf16 v[52:55], v[112:115], v[120:123], v[52:55]\n" \
    "v_cvt_pk_bf16_f32 v118, v4, v5\n" \
    "v_cvt_pk_bf16_f32 v119, v6, v7\n" \
    "s_waitcnt lgkmcnt(1)\n" \
    "v_mfma_f32_16x16x32_bf16 v[0:3], v[96:99], v[64:67], 0\n" \
    "v_exp_f32 v8, v8\n" \
    "v_exp_f32 v9, v9\n" \
    "v_mfma_f32_16x16x32_bf16 v[4:7], v[100:103], v[64:67], 0\n" \
    "ds_read_b128 v[108:111], v124 offset:15360\n" \
    "v_exp_f32 v10, v10\n" \
    "v_exp_f32 v11, v11\n" \
    "v_mfma_f32_16x16x32_bf16 v[32:35], v[88:91], v[116:119], v[32:35]\n" \
    "v_cvt_pk_bf16_f32 v120, v8, v9\n" \
    "v_exp_f32 v12, v12\n" \
    "v_exp_f32 v13, v13\n" \
    "v_mfma_f32_16x16x32_bf16 v[36:39], v[92:95], v[116:119], v[36:39]\n" \
    "v_cvt_pk_bf16_f32 v121, v10, v11\n" \
    "v_exp_f32 v14, v14\n" \
    "v_exp_f32 v15, v15\n" \
    "v_mfma_f32_16x16x32_bf16 v[56:59], v[112:115], v[116:119], v[56:59]\n" \
    "v_cvt_pk_bf16_f32 v122, v12, v13\n" \
    "v_cvt_pk_bf16_f32 v123, v14, v15\n" \
    "s_waitcnt vmcnt(4)\n" \
    "s_barrier\n" \
    "v_mfma_f32_16x16x32_bf16 v[8:11], v[96:99], v[68:71], 0\n" \
    "v_exp_f32 v0, v0\n" \
    "v_exp_f32 v1, v1\n" \
    "v_mfma_f32_16x16x32_bf16 v[12:15], v[100:103], v[68:71], 0\n" \
    "ds_read_b128 v[80:83], v125 offset:0\n" \
    "v_exp_f32 v2, v2\n" \
    "v_exp_f32 v3, v3\n" \
    "v_mfma_f32_16x16x32_bf16 v[40:43], v[88:91], v[120:123], v[40:43]\n" \
    "v_cvt_pk_bf16_f32 v116, v0, v1\n" \
    "v_exp_f32 v4, v4\n" \
    "v_exp_f32 v5, v5\n" \
    "v_mfma_f32_16x16x32_bf16 v[44:47], v[92:95], v[120:123], v[44:47]\n" \
    "s_mov_b32 m0, s20\n" \
    "s_add_u32 s20, s20, 0x400\n" \
    "buffer_load_dwordx4 v126, %[srd], s21 offen lds\n" \
    "s_add_u32 s21, s21, 0x400\n" \
    "v_cvt_pk_bf16_f32 v117, v2, v3\n" \
    "v_exp_f32 v6, v6\n" \
    "v_exp_f32 v7, v7\n" \
    "v_mfma_f32_16x16x32_bf16 v[60:63], v[112:115], v[120:123], v[60:63]\n" \
    "v_cvt_pk_bf16_f32 v118, v4, v5\n" \
    "v_cvt_pk_bf16_f32 v119, v6, v7\n" \
    "s_waitcnt lgkmcnt(1)\n" \
    "v_mfma_f32_16x16x32_bf16 v[0:3], v[96:99], v[72:75], 0\n" \
    "v_exp_f32 v8, v8\n" \
    "v_exp_f32 v9, v9\n" \
    "v_mfma_f32_16x16x32_bf16 v[4:7], v[100:103], v[72:75], 0\n" \
    "ds_read_b128 v[84:87], v125 offset:1024\n" \
    "v_exp_f32 v10, v10\n" \
    "v_exp_f32 v11, v11\n" \
    "v_mfma_f32_16x16x32_bf16 v[16:19], v[104:107], v[116:119], v[16:19]\n" \
    "v_cvt_pk_bf16_f32 v120, v8, v9\n" \
    "v_exp_f32 v12, v12\n" \
    "v_exp_f32 v13, v13\n" \
    "v_mfma_f32_16x16x32_bf16 v[20:23], v[108:111], v[116:119], v[20:23]\n" \
    "v_cvt_pk_bf16_f32 v121, v10, v11\n" \
    "v_exp_f32 v14, v14\n" \
    "v_exp_f32 v15, v15\n" \
    "v_mfma_f32_16x16x32_bf16 v[48:51], v[112:115], v[116:119], v[48:51]\n" \
    "v_cvt_pk_bf16_f32 v122, v12, v13\n" \
    "v_cvt_pk_bf16_f32 v123, v14, v15\n" \
    "v_mfma_f32_16x16x32_bf16 v[8:11], v[96:99], v[76:79], 0\n" \
    "v_exp_f32 v0, v0\n" \
    "v_exp_f32 v1, v1\n" \
    "v_mfma_f32_16x16x32_bf16 v[12:15], v[100:103], v[76:79], 0\n" \
    "ds_read_b128 v[88:91], v125 offset:2048\n" \
    "v_exp_f32 v2, v2\n" \
    "v_exp_f32 v3, v3\n" \
    "v_mfma_f32_16x16x32_bf16 v[24:27], v[104:107], v[120:123], v[24:27]\n" \
    "v_cvt_pk_bf16_f32 v116, v0, v1\n" \
    "v_exp_f32 v4, v4\n" \
    "v_exp_f32 v5, v5\n" \
    "v_mfma_f32_16x16x32_bf16 v[28:31], v[108:111], v[120:123], v[28:31]\n" \
    "s_mov_b32 m0, s20\n" \
    "s_add_u32 s20, s20, 0x400\n" \
    "buffer_load_dwordx4 v126, %[srd], s21 offen lds\n" \
    "s_add_u32 s21, s21, 0x400\n" \
    "v_cvt_pk_bf16_f32 v117, v2, v3\n" \
    "v_exp_f32 v6, v6\n" \
    "v_exp_f32 v7, v7\n" \
    "v_mfma_f32_16x16x32_bf16 v[52:55], v[112:115], v[120:123], v[52:55]\n" \
    "v_cvt_pk_bf16_f32 v118, v4, v5\n" \
    "v_cvt_pk_bf16_f32 v119, v6, v7\n" \
    "s_waitcnt lgkmcnt(1)\n" \
    "v_mfma_f32_16x16x32_bf16 v[0:3], v[80:83], v[64:67], 0\n" \
    "v_exp_f32 v8, v8\n" \
    "v_exp_f32 v9, v9\n" \
    "v_mfma_f32_16x16x32_bf16 v[4:7], v[84:87], v[64:67], 0\n" \
    "ds_read_b128 v[92:95], v125 offset:3072\n" \
    "v_exp_f32 v10, v10\n" \
    "v_exp_f32 v11, v11\n" \
    "v_mfma_f32_16x16x32_bf16 v[32:35], v[104:107], v[116:119], v[32:35]\n" \
    "v_cvt_pk_bf16_f32 v120, v8, v9\n" \
    "v_exp_f32 v12, v12\n" \
    "v_exp_f32 v13, v13\n" \
    "v_mfma_f32_16x16x32_bf16 v[36:39], v[108:111], v[116:119], v[36:39]\n" \
    "v_cvt_pk_bf16_f32 v121, v10, v11\n" \
    "v_exp_f32 v14, v14\n" \
    "v_exp_f32 v15, v15\n" \
    "v_mfma_f32_16x16x32_bf16 v[56:59], v[112:115], v[116:119], v[56:59]\n" \
    "v_cvt_pk_bf16_f32 v122, v12, v13\n" \
    "v_cvt_pk_bf16_f32 v123, v14, v15\n" \
    "v_mov_b32 v124, v125\n" \
    "s_add_u32 s24, s24, 0x4000\n" \
    "s_and_b32 s24, s24, 0xffff\n" \
    "v_add_u32 v125, s24, v127\n" \
    "s_sub_u32 s23, s23, 1\n" \
    "s_cmp_lg_u32 s23, 0\n" \
    "s_cbranch_scc1 ATT_LOOP_%=\n" \
    "v_mfma_f32_16x16x32_bf16 v[40:43], v[104:107], v[120:123], v[40:43]\n" \
    "v_mfma_f32_16x16x32_bf16 v[44:47], v[108:111], v[120:123], v[44:47]\n" \
    "v_mfma_f32_16x16x32_bf16 v[60:63], v[112:115], v[120:123], v[60:63]\n" \
    "s_waitcnt vmcnt(0) lgkmcnt(0)\n" \
    "s_nop 15\n"

constexpr int Q_NS = 3, Q_SLOT = 24576;
__device__ __forceinline__ void attention(int b, const char* x, const char* wq, char* ws, lds_ptr lds, int w, int lane) {
    const int xc = b & 7, t = b >> 3, st128 = (xc >> 1) * 4 + (t & 3), hp = (xc & 1) * 8 + (t >> 2);
    const int head = 2 * hp + (w & 1), s0 = st128 * 128 + (w >> 1) * 64, g = lane >> 4, c = lane & 15;
    const u32x4 srd = make_srd(ws + WS_KV, S * 128);
    const u32 voff = w * 4096 + lane * 16, ldst = (u32)(uintptr_t)lds + w * 4096;
    auto dma = [&](int st, int p0, int p1) {
#pragma unroll
        for (int p = p0; p < p1; ++p) dma16(ldst + ((st + 2) & 3) * KV_STAGE + p * 1024, voff, srd, st * KV_STAGE + p * 1024);
    };
    __builtin_amdgcn_s_setprio(Q_PRIO);
    float gate[4];
#pragma unroll
    for (int i = 0; i < 4; ++i) gate[i] = ((const float*)(ws + WS_G))[s0 + i * 16 + c];
    const f32x4 kb2 = ((const f32x4*)(ws + WS_BND))[lane];
    u32x4 q[4];
    {
        OpTr<64, D * 2, 2, true, 2> a; a.init(wq, D * D * 2, hp * 64, 0, lds, w, (w & 1) * 2, lane);
        OpRow bb; bb.init(x, S * D * 2, st128 * 128, lds + a.BYTES, w, (w >> 1) * 64, lane);
        Engine<Q_NS, 2, 4, 2> e;
        static_assert((D / 64 - 1) % Q_NS == 0 && Q_SLOT <= 2 * KV_STAGE, "the last Q slot lies below the first two K/V stages");
        gemm_run<Q_SLOT, D / 64, 2>(e, a, bb, [] {}, [](int) {}, [&](auto p) { if constexpr (p.v == Rounds<Q_NS, D / 64>::TAIL - 2) { dma(0, 0, 4); dma(1, 0, 4); } });
        wait_bar<4>();
        dma(2, 0, 2);
#pragma unroll
        for (int i = 0; i < 4; ++i) q[i] = pack8(e.acc[0][i] * QSCALE, e.acc[1][i] * QSCALE);
    }
    const float k2 = wave_max(fmaxf(fmaxf(kb2[0], kb2[1]), fmaxf(kb2[2], kb2[3])));
    bool ok = true;
#pragma unroll
    for (int i = 0; i < 4; ++i) {
        float n = 0.f;
#pragma unroll
        for (int m = 0; m < 4; ++m) { const float a = bf_lo(q[i][m]), h = bf_hi(q[i][m]); n += a * a + h * h; }
        ok = ok && group_sum(n) * k2 * 1.02f <= SAFE_LOG2 * SAFE_LOG2;
        PIN(gate[i]);
    }
    const bool safe = __all(ok);
    __builtin_amdgcn_s_setprio(ATT_PRIO);
    f32x4 o[8];
#pragma unroll
    for (int i = 0; i < 8; ++i) o[i] = f32x4{};
    float lsum[4];
    const lds_ptr base = lds + lane * 16;
    const f32x4 z = {};
    STAMP(1, 1);
    if (safe) {
        const u32 one = c == 0 ? 0x3f803f80u : 0u;
        const u32x4 ones = {one, one, one, one};
        const lds_ptr first = base + 2 * KV_STAGE;
        u32x4 ka = lds_rd128(first), kb = lds_rd128(first + 1024), v0 = lds_rd128(first + 2048), v1 = lds_rd128(first + 3072);
        f32x4 sa = M16(ka, q[0], z), sb = M16(kb, q[0], z);
        f32x16 oa = {}, ob = {}, l = {};
        u32x4 vz0 = {}, vz1 = {}, pz = {};
        u32 cur = (u32)(uintptr_t)first, nxt = cur + KV_STAGE;
        asm volatile("s_nop 7\n" ATT_LOOP_ASM
                     : "+{v[0:3]}"(sa), "+{v[4:7]}"(sb), "+{v[16:31]}"(oa), "+{v[32:47]}"(ob), "+{v[48:63]}"(l), "+{v[80:83]}"(ka), "+{v[84:87]}"(kb),
                       "+{v[88:91]}"(v0), "+{v[92:95]}"(v1), "+{v[104:107]}"(vz0), "+{v[108:111]}"(vz1), "+{v[120:123]}"(pz), "+{v124}"(cur), "+{v125}"(nxt)
                     : "{v[64:67]}"(q[0]), "{v[68:71]}"(q[1]), "{v[72:75]}"(q[2]), "{v[76:79]}"(q[3]), "{v[112:115]}"(ones), "{v126}"(voff),
                       "{v127}"((u32)(uintptr_t)base), [srd] "s"(srd), [ldst] "s"(ldst)
                     : "v8", "v9", "v10", "v11", "v12", "v13", "v14", "v15", "v96", "v97", "v98", "v99", "v100", "v101", "v102", "v103",
                       "v116", "v117", "v118", "v119", "s20", "s21", "s22", "s23", "s24", "scc", "memory");
#pragma unroll
        for (int i = 0; i < 4; ++i) {
            lsum[i] = __shfl(l[4 * i], c);
            o[i] = f32x4{oa[4 * i], oa[4 * i + 1], oa[4 * i + 2], oa[4 * i + 3]};
            o[4 + i] = f32x4{ob[4 * i], ob[4 * i + 1], ob[4 * i + 2], ob[4 * i + 3]};
        }
    } else {
        float mx[4];
#pragma unroll
        for (int i = 0; i < 4; ++i) { mx[i] = -__builtin_inff(); lsum[i] = 0.f; }
        dma(2, 2, 4);
        for (int st = 0; st < KV_NSTAGE; ++st) {
            wait_bar<4>();
            dma(st + 3, 0, 4);
#pragma unroll 1
            for (int kb = 0; kb < 4; ++kb) {
                const lds_ptr p = base + ((st + 2) & 3) * KV_STAGE + kb * 4096;
                const u32x4 k0 = lds_rd128(p), k1 = lds_rd128(p + 1024), v0 = lds_rd128(p + 2048), v1 = lds_rd128(p + 3072);
#pragma unroll
                for (int i = 0; i < 4; ++i) {
                    f32x4 sa = M16(k0, q[i], z), sb = M16(k1, q[i], z);
                    float m = mx[i];
#pragma unroll
                    for (int r = 0; r < 4; ++r) m = fmaxf(m, fmaxf(sa[r], sb[r]));
                    m = fmaxf(m, __shfl_xor(m, 16)); m = fmaxf(m, __shfl_xor(m, 32));
                    const float alpha = EX2(mx[i] - m);
                    mx[i] = m;
                    float t = 0.f;
#pragma unroll
                    for (int r = 0; r < 4; ++r) { sa[r] = EX2(sa[r] - m); sb[r] = EX2(sb[r] - m); t += sa[r] + sb[r]; }
                    lsum[i] = lsum[i] * alpha + t;
                    const u32x4 pp = pack8(sa, sb);
                    o[2 * i] = M16(v0, pp, o[2 * i] * alpha);
                    o[2 * i + 1] = M16(v1, pp, o[2 * i + 1] * alpha);
                }
            }
        }
        asm volatile("s_waitcnt vmcnt(0)" ::: "memory");
#pragma unroll
        for (int i = 0; i < 4; ++i) lsum[i] = group_sum(lsum[i]);
    }
    STAMP(1, 6);
#pragma unroll
    for (int i = 0; i < 4; ++i) {
        const float r = gate[i] / lsum[i];
        store_wt(ws + WS_OF, S * D * 2, (u32)((head * 4 + g) * S + s0 + i * 16 + c) * 16u, pack8(o[2 * i] * r, o[2 * i + 1] * r));
    }
    publish(ws, CNT_O + st128);
}

__device__ __forceinline__ f32x2 gelu_tanh(f32x2 x) {
    constexpr float C0 = -2.f * 0.7978845608028654f * 1.4426950408889634f, C1 = C0 * 0.044715f;
    const f32x2 y = x * __builtin_elementwise_fma(x * x, f32x2{C1, C1}, f32x2{C0, C0});
    const f32x2 d = f32x2{EX2(y[0]), EX2(y[1])} + 1.f;
    return x * f32x2{__builtin_amdgcn_rcpf(d[0]), __builtin_amdgcn_rcpf(d[1])};
}

__device__ __forceinline__ void gemm1_tile(int b, const void* w1, const char* b1, char* ws, lds_ptr lds, int w, int lane) {
    const int x = b & 7, t = b >> 3;
    const int dt = (x & 1) * 4 + (t & 3), d0 = dt * 128, f0 = ((x >> 1) * 7 + (t >> 2)) * 128;
    const int g = lane >> 4, c = lane & 15;
    u32x2 bias[8];
#pragma unroll
    for (int j = 0; j < 8; ++j) bias[j] = *(const u32x2*)(b1 + (f0 + j * 16 + g * 4) * 2);
    OpReg<2, D, 1, false> a; a.init(ws + WS_HF, S * D * 2, d0 + w * 32, 0, lane);
    OpTr<128, F * 2, 1, false, 8> bb; bb.init(w1, S * F * 2, f0, 0, lds, w, 0, lane);
    Engine<G1_NS, 2, 8> e;
    gemm_run<8192, S / 32, 0>(e, a, bb, [] {}, [](int) {}, [](auto) {});
    auto& acc = e.acc;
    STAMP(1, 1);
#pragma unroll
    for (int jp = 0; jp < 4; ++jp)
#pragma unroll
        for (int i = 0; i < 2; ++i) {
            f32x4 u[2];
#pragma unroll
            for (int h = 0; h < 2; ++h) {
                const u32x2 bv = bias[2 * jp + h];
                const f32x4 v = acc[i][2 * jp + h] + f32x4{bf_lo(bv[0]), bf_hi(bv[0]), bf_lo(bv[1]), bf_hi(bv[1])};
                const f32x2 lo = gelu_tanh(f32x2{v[0], v[1]}), hi = gelu_tanh(f32x2{v[2], v[3]});
                u[h] = f32x4{lo[0], lo[1], hi[0], hi[1]};
            }
            store_wt(ws + WS_UF, F * D * 2, (u32)(((f0 / 32 + jp) * 4 + g) * D + d0 + w * 32 + i * 16 + c) * 16u, pack8(u[0], u[1]));
        }
    publish(ws, CNT_U + dt * 2 + (x >> 2));
}

constexpr int P1_NS = 8, P1_BASE = 8192, P2_NS = 6, P2_SLOT = 12288;

__device__ __forceinline__ void post_tile(int b, const char* w2, const char* b2, const char* wo, char* ws, char* out, lds_ptr lds, int w, int lane) {
    const int x = b & 7, t = b >> 3, h = t & 1;
    const int s0 = ((x >> 1) * 8 + (t & 7)) * 64, dt = (x & 1) * 4 + (t >> 3), d0 = dt * 128;
    const int g = lane >> 4, c = lane & 15;
    constexpr int N1 = F / 64, N2 = D / 32;
    static_assert(N1 % P1_NS == 0 && P1_BASE + (P1_NS - 1) * 8192 >= (P2_NS - 1) * P2_SLOT, "the last w2 slot lies in the last wo slot");
    float omg[4], bs[4];
#pragma unroll
    for (int j = 0; j < 4; ++j) {
        const int s = s0 + j * 16 + c;
        asm volatile("global_load_dword %0, %1, off" : "=v"(omg[j]) : "v"(ws + WS_G + s * 4) : "memory");
        asm volatile("global_load_ushort %0, %1, off" : "=v"(bs[j]) : "v"(b2 + s * 2) : "memory");
    }
    OpTr<128, D * 2, 1, false, 2> a2; a2.init(wo, D * D * 2, d0, 0, lds, w, w * 2, lane);
    OpFrag<64, S, 1> b2o; b2o.init(ws + WS_OF, S * D * 2, s0, 0, lds + 8192, w, 0, lane);
    Engine<P2_NS, 2, 4> e;
    auto& acc = e.acc;
    u32 seen = 0;
    {
        OpReg<2, D, 1, true> a1; a1.init(ws + WS_UF, F * D * 2, d0 + w * 32, h * N1, lane);
        OpTr<128, S * 2, 1, false, 8> b1; b1.init(w2, F * S * 2, s0 & ~127, h * N1, lds + P1_BASE, w, 4 * h, lane);
        Engine<P1_NS, 2, 8> e1;
        constexpr int TAIL = Rounds<P1_NS, N1>::TAIL;
        gemm_run<8192, N1, 2>(e1, a1, b1,
            [&] { if (w == 0) await(ws, CNT_U + dt * 2 + h, F / 256); __builtin_amdgcn_s_barrier(); STAMP(1, 3); },
            [&](int r) { if (r == PRIO_SWITCH) __builtin_amdgcn_s_setprio(1); if (r == Rounds<P1_NS, N1>::N - 1) sample(seen, ws, CNT_O + (s0 >> 7)); },
            [&](auto p) { if constexpr (p.v == TAIL - 2) { confirm(seen, ws, CNT_O + (s0 >> 7), 16); STAMP(1, 6); gemm_fill<P2_SLOT>(a2, b2o, 0, P2_NS - 1); } });
        STAMP(1, 4);
#pragma unroll
        for (int j = 0; j < 4; ++j) {
            PIN(omg[j]); PIN(bs[j]);
            omg[j] = bf_round(1.f - omg[j]); bs[j] = __uint_as_float(__float_as_uint(bs[j]) << 16);
#pragma unroll
            for (int i = 0; i < 2; ++i) {
                store_wt(ws + WS_X, 256u << 15, (u32)(((b * 4 + w) * 8 + i * 4 + j) * 64 + lane) * 16u, __builtin_bit_cast(u32x4, e1.acc[i][4 + j]));
                acc[i][j] = (e1.acc[i][j] + bs[j]) * omg[j];
            }
        }
    }
    constexpr int PPW = 3, XW = 8;
    wait_bar<(P2_NS - 2) * PPW + XW>();
    gemm_fill<P2_SLOT>(a2, b2o, P2_NS - 1, P2_NS);
    gemm_first(e, a2, b2o);
    gemm_steps<0, P2_NS, MANY, P2_SLOT, 2, XW>(e, a2, b2o, P2_NS, [&](auto p) { if constexpr (p.v == P2_NS - 2) signal(ws, CNT_X + b); });
    for (int r = 1; r < 3; ++r) gemm_steps<0, P2_NS, MANY, P2_SLOT, 2, 0>(e, a2, b2o, (r + 1) * P2_NS, [](auto) {});
    seen = 0;
    u32x4 got[8];
    const u32x4 xsrd = make_srd(ws + WS_X, 256u << 15);
    gemm_steps<0, N2 - 3 * P2_NS, N2 - 3 * P2_NS, P2_SLOT, 2, 0>(e, a2, b2o, 4 * P2_NS, [&](auto p) {
        if constexpr (p.v == 0) sample(seen, ws, CNT_X + (b ^ 8));
        if constexpr (p.v == P2_NS - 2) {
            confirm(seen, ws, CNT_X + (b ^ 8), 1);
#pragma unroll
            for (int i = 0; i < 8; ++i) {
                got[i] = u32x4{};
                load_coh(got[i], lane * 16, xsrd, (u32)(((b ^ 8) * 4 + w) * 8 + i) * 1024u);
            }
        }
    });
    asm volatile("s_waitcnt vmcnt(0)" ::: "memory");
#pragma unroll
    for (int i = 0; i < 8; ++i) { PIN(got[i]); acc[i >> 2][i & 3] += __builtin_bit_cast(f32x4, got[i]) * omg[i & 3]; }
    STAMP(1, 5);
#pragma unroll
    for (int j = 0; j < 4; ++j) {
        const auto r0 = __builtin_amdgcn_permlane16_swap(cvtpk(acc[0][j][0], acc[0][j][1]), cvtpk(acc[1][j][0], acc[1][j][1]), false, false);
        const auto r1 = __builtin_amdgcn_permlane16_swap(cvtpk(acc[0][j][2], acc[0][j][3]), cvtpk(acc[1][j][2], acc[1][j][3]), false, false);
        const int s = s0 + j * 16 + c, d = d0 + w * 32 + (g & 1) * 16 + (g >> 1) * 8;
        *(u32x4*)(out + ((size_t)s * D + d) * 2) = u32x4{r0[0], r1[0], r0[1], r1[1]};
    }
}

constexpr int MAIN_LDS = Q_NS * Q_SLOT;
static_assert(MAIN_LDS >= P2_NS * P2_SLOT && MAIN_LDS >= P1_BASE + P1_NS * 8192 && MAIN_LDS >= 4 * KV_STAGE && 2 * MAIN_LDS <= 160 * 1024, "two workgroups per CU");

__global__ void __launch_bounds__(256, 2) k_main(const char* __restrict__ x, const char* __restrict__ wq, const char* __restrict__ w1, const char* __restrict__ b1, const char* __restrict__ w2, const char* __restrict__ b2,
                                                 const char* __restrict__ wo, char* __restrict__ ws, char* __restrict__ out) {
    extern __shared__ __attribute__((aligned(16))) char smem[];
    const int lane = threadIdx.x & 63, w = __builtin_amdgcn_readfirstlane(threadIdx.x >> 6), b = blockIdx.x;
    STAMP(1, 0);
    if (b < MID_ATT) {
        attention(b, x, wq, ws, (lds_ptr)smem, w, lane);
    } else {
        const int gb = b - MID_ATT;
        __builtin_amdgcn_s_setprio(G1_PRIO);
        if (gb < MID_G1) gemm1_tile(gb, w1, b1, ws, (lds_ptr)smem, w, lane);
        else {
            const u32 scratch = (u32)(uintptr_t)(lds_ptr)smem + w * 256;
            __builtin_amdgcn_s_sleep(127);
            __builtin_amdgcn_s_sleep(PF_SLEEP);
            for (int i = 0; i < 4; ++i) {
                touch<F * S * 2, MID_PF * 4, 256>(w2, (gb - MID_G1) * 4 + i, scratch);
                asm volatile("s_waitcnt vmcnt(0)" ::: "memory");
            }
            touch<D * D * 2, MID_PF, 256>(wo, gb - MID_G1, scratch);
            wait_bar<0>();
        }
        STAMP(1, 2);
        __builtin_amdgcn_s_setprio(POST_PRIO);
        post_tile(gb, w2, b2, wo, ws, out, (lds_ptr)smem, w, lane);
    }
    STAMP(1, 7);
}

__global__ void k_probe(char* ws) {
    const unsigned long long* t = (const unsigned long long*)(ws + (64u << 20));
    const unsigned long long now = __builtin_amdgcn_s_memrealtime();
    const unsigned long long a = t[0], b = PROBE_KB < 0 ? now : t[PROBE_KB * 2];
    unsigned long long d = (b - a) * PROBE;
    if (d > 100000) d = 100000;
    while (__builtin_amdgcn_s_memrealtime() - now < d) {}
}

extern "C" void kernel_launch(void* const* d_in, const int* in_sizes, int n_in, void* d_out, int out_size, void* d_ws, size_t ws_size,
                              hipStream_t stream) {
    const char* x = (const char*)d_in[0];
    const char *wq = (const char*)d_in[1], *wk = (const char*)d_in[2], *wv = (const char*)d_in[3], *wo = (const char*)d_in[4];
    const char *w1 = (const char*)d_in[6], *b1 = (const char*)d_in[7], *w2 = (const char*)d_in[8], *b2 = (const char*)d_in[9];
    const char* gw = (const char*)d_in[10];
    char* ws = (char*)d_ws;
    (void)hipFuncSetAttribute((const void*)k_pre, hipFuncAttributeMaxDynamicSharedMemorySize, PRE_LDS);
    (void)hipFuncSetAttribute((const void*)k_main, hipFuncAttributeMaxDynamicSharedMemorySize, MAIN_LDS);
    k_pre<<<PRE_KV + PRE_LN + PRE_PF, 512, PRE_LDS, stream>>>(x, wq, wk, wv, gw, w1, ws);
    k_main<<<MID_ATT + MID_G1 + MID_PF, 256, MAIN_LDS, stream>>>(x, wq, w1, b1, w2, b2, wo, ws, (char*)d_out);
    k_probe<<<1, 64, 0, stream>>>(ws);
}
```

```cpp
#include <hip/hip_runtime.h>
#include <stdint.h>

typedef unsigned u32;
typedef u32 u32x4 __attribute__((ext_vector_type(4)));
typedef u32 u32x2 __attribute__((ext_vector_type(2)));
typedef float f32x2 __attribute__((ext_vector_type(2)));
typedef float f32x4 __attribute__((ext_vector_type(4)));
typedef float f32x16 __attribute__((ext_vector_type(16)));
typedef __bf16 bf16x2 __attribute__((ext_vector_type(2)));
typedef __bf16 bf16x8 __attribute__((ext_vector_type(8)));
typedef short s16x4 __attribute__((ext_vector_type(4)));
#define LDS __attribute__((address_space(3)))
typedef LDS char* lds_ptr;

constexpr int S = 2048, D = 1024, F = 3584;
constexpr u32 WS_HF = 0, WS_OF = 8u << 20, WS_UF = 12u << 20, WS_KV = 20u << 20;
constexpr u32 WS_G = WS_KV + (256u << 10), WS_BND = WS_G + (8u << 10), WS_CNT = WS_BND + (4u << 10);
constexpr u32 WS_X = 24u << 20;
constexpr int CNT_U = 0, CNT_O = 16, CNT_X = 32, CNT_N = 288, CNT_STRIDE = 256;
constexpr float QSCALE = 0.17677669529663687f * 1.4426950408889634f;
constexpr float SAFE_LOG2 = 60.f;
constexpr float SAFE_V = 1.0e12f;

#define PLAIN_U 0
#define PLAIN_O 0
#define CODE_PF 8
#define SB() __builtin_amdgcn_sched_barrier(0)
#define PIN(x) asm volatile("" : "+v"(x))
#define M16(a, b, c) __builtin_amdgcn_mfma_f32_16x16x32_bf16(__builtin_bit_cast(bf16x8, a), __builtin_bit_cast(bf16x8, b), c, 0, 0, 0)
#define M32(a, b, c) __builtin_amdgcn_mfma_f32_32x32x16_bf16(__builtin_bit_cast(bf16x8, a), __builtin_bit_cast(bf16x8, b), c, 0, 0, 0)
__device__ __forceinline__ void mma16(f32x4& c, const u32x4& a, const u32x4& b) { asm volatile("v_mfma_f32_16x16x32_bf16 %0, %1, %2, %0" : "+v"(c) : "v"(a), "v"(b)); }
__device__ __forceinline__ void mfma_settle() { asm volatile("s_nop 7\n\ts_nop 1" ::: "memory"); SB(); }
#define EX2(x) __builtin_amdgcn_exp2f(x)

__device__ __forceinline__ u32 cvtpk(float lo, float hi) { f32x2 v = {lo, hi}; return __builtin_bit_cast(u32, __builtin_convertvector(v, bf16x2)); }
__device__ __forceinline__ float bf_lo(u32 v) { return __uint_as_float(v << 16); }
__device__ __forceinline__ float bf_hi(u32 v) { return __uint_as_float(v & 0xffff0000u); }
__device__ __forceinline__ float bf_round(float x) { return bf_lo(cvtpk(x, x)); }
__device__ __forceinline__ u32x4 pack8(const f32x4& a, const f32x4& b) { return u32x4{cvtpk(a[0], a[1]), cvtpk(a[2], a[3]), cvtpk(b[0], b[1]), cvtpk(b[2], b[3])}; }

__device__ __forceinline__ u32x4 make_srd(const void* p, u32 bytes) {
    const uint64_t a = (uint64_t)p;
    return u32x4{(u32)a, (u32)(a >> 32) & 0xffffu, bytes, 0x00020000u};
}
__device__ __forceinline__ void dma16(u32 lds, u32 voff, const u32x4& srd, u32 soff) {
    asm volatile("s_mov_b32 m0, %0\n\ts_nop 0\n\tbuffer_load_dwordx4 %1, %2, %3 offen lds" ::"s"(lds), "v"(voff), "s"(srd), "s"(soff) : "memory");
}
__device__ __forceinline__ void dma16_coh(u32 lds, u32 voff, const u32x4& srd, u32 soff) {
    asm volatile("s_mov_b32 m0, %0\n\ts_nop 0\n\tbuffer_load_dwordx4 %1, %2, %3 offen sc1 lds" ::"s"(lds), "v"(voff), "s"(srd), "s"(soff) : "memory");
}
template <int N> __device__ __forceinline__ void wait_bar() {
    __builtin_amdgcn_s_waitcnt(0xc07f);
    asm volatile("s_waitcnt vmcnt(%0)\n\ts_barrier" ::"n"(N) : "memory");
}
__device__ __forceinline__ u32x4 lds_rd128(lds_ptr p) { return *(LDS const u32x4*)p; }
__device__ __forceinline__ u32x2 lds_rdtr(lds_ptr p) { return __builtin_bit_cast(u32x2, __builtin_amdgcn_ds_read_tr16_b64_v4i16((LDS s16x4*)p)); }

typedef __attribute__((address_space(1))) u32 gu32;
__device__ __forceinline__ void store_wt(char* base, u32 bytes, u32 off, const u32x4& v) {
    __builtin_amdgcn_raw_buffer_store_b128(v, __builtin_amdgcn_make_buffer_rsrc(base, 0, bytes, 0x00020000), off, 0, 16);
}
__device__ __forceinline__ void load_coh(u32x4& f, u32 voff, const u32x4& srd, u32 soff) {
    asm volatile("buffer_load_dwordx4 %0, %1, %2, %3 offen sc1" : "+v"(f) : "v"(voff), "s"(srd), "s"(soff) : "memory");
}
__device__ __forceinline__ void signal(char* ws, int cnt) {
    if (threadIdx.x == 0) __hip_atomic_fetch_add((gu32*)(ws + WS_CNT + cnt * CNT_STRIDE), 1u, __ATOMIC_RELAXED, __HIP_MEMORY_SCOPE_AGENT);
}
__device__ __forceinline__ void publish(char* ws, int cnt) {
    asm volatile("s_waitcnt vmcnt(0)\n\ts_barrier" ::: "memory");
    signal(ws, cnt);
}
__device__ __forceinline__ void await(char* ws, int cnt, u32 target) {
    gu32* p = (gu32*)(ws + WS_CNT + cnt * CNT_STRIDE);
    for (int spins = 0; spins < (1 << 20); ++spins) {
        if (__builtin_amdgcn_readfirstlane(__hip_atomic_load(p, __ATOMIC_RELAXED, __HIP_MEMORY_SCOPE_AGENT)) >= target) break;
        __builtin_amdgcn_s_sleep(1);
    }
    asm volatile("" ::: "memory");
}
__device__ __forceinline__ void acquire() { asm volatile("buffer_inv sc1" ::: "memory"); }

__device__ __forceinline__ void sample(u32& seen, char* ws, int cnt) {
    asm volatile("global_load_dword %0, %1, off sc1" : "+v"(seen) : "v"(ws + WS_CNT + cnt * CNT_STRIDE) : "memory");
}
__device__ __forceinline__ void confirm(u32& seen, char* ws, int cnt, u32 target) {
    PIN(seen);
    if (__builtin_amdgcn_readfirstlane(seen) < target) await(ws, cnt, target);
}

#define STAMP(k, i)

template <int R, int NR, int KSTR> struct OpFrag {
    static constexpr int BYTES = 4 * R * 16, PPW = BYTES / 4096, RPF = 1;
    static constexpr bool REG = false;
    u32x4 srd; u32 voff, ldst; lds_ptr rd;
    __device__ __forceinline__ void init(const void* base, u32 bytes, int row0, int k0, lds_ptr img, int w, int wrow, int lane) {
        srd = make_srd(base, bytes);
        voff = (u32)(row0 + lane + (w + 4 * k0) * NR) * 16u;
        ldst = (u32)(uintptr_t)img + w * (R * 16);
        rd = img + ((lane >> 4) * R + wrow + (lane & 15)) * 16;
    }
    __device__ __forceinline__ void issue(int slot, u32 pos, int i) const { (PLAIN_O ? dma16 : dma16_coh)(ldst + pos + i * 1024, voff, srd, (u32)slot * (KSTR * 4u * NR * 16u) + i * 1024u); }
    __device__ __forceinline__ void read(u32x4& f, int pos, int i, int) const { f = lds_rd128(rd + pos + i * 256); }
};

template <int C, int RS, int KS, bool NAT, int NF> struct OpTr {
    static constexpr int BYTES = KS * 32 * C * 2, PPW = BYTES / 4096, RPF = 2;
    static constexpr bool REG = false;
    u32x4 srd; u32 voff, ldst; lds_ptr rd[NF];
    __device__ __forceinline__ void init(const void* base, u32 bytes, int col0, int k0, lds_ptr img, int w, int wch, int lane) {
        srd = make_srd(base, bytes);
        ldst = (u32)(uintptr_t)img + w * 1024;
        const int g = lane >> 4, q = (lane >> 2) & 3, p = lane & 3;
        if constexpr (C == 128) {
            const int f = NAT ? (g | (((w >> 1) & 1) << 2)) : (4 * (w & 1) + g);
            voff = (u32)(32 * k0 + 4 * w + g) * RS + col0 * 2 + ((((lane & 15) >> 1) ^ f) * 32) + (lane & 1) * 16;
            const int row = NAT ? 8 * g + q : 4 * g + q, fr = NAT ? (q | ((g & 1) << 2)) : (row & 7);
#pragma unroll
            for (int i = 0; i < NF; ++i) rd[i] = img + row * 256 + (((wch ^ i) ^ fr) * 32) + p * 8;
        } else {
            static_assert(C == 128 || C == 64, "unsupported transposed operand");
            const int f = NAT ? ((g & 1) | ((w & 1) << 1)) : g;
            voff = (u32)(32 * k0 + 8 * w + (lane >> 3)) * RS + col0 * 2 + ((((lane & 7) >> 1) ^ f) * 32) + (lane & 1) * 16;
            const int row = NAT ? 8 * g + q : 4 * g + q, fr = NAT ? ((q >> 1) | ((g & 1) << 1)) : ((row >> 1) & 3);
#pragma unroll
            for (int i = 0; i < NF; ++i) rd[i] = img + row * 128 + (((wch ^ i) ^ fr) * 32) + p * 8;
        }
    }
    __device__ __forceinline__ void issue(int slot, u32 pos, int i) const { dma16(ldst + pos + i * 4096, voff, srd, (u32)(slot * KS * 32 + i * (2048 / C)) * RS); }
    __device__ __forceinline__ void read(u32x4& f, int pos, int i, int t) const {
        const u32x2 v = lds_rdtr(rd[i % NF] + pos + i / NF * (32 * C * 2) + t * ((NAT ? 4 : 16) * C * 2));
        f[2 * t] = v[0]; f[2 * t + 1] = v[1];
    }
};

struct OpRow {
    static constexpr int BYTES = 128 * 128, PPW = 4, RPF = 1;
    static constexpr bool REG = false;
    u32x4 srd; u32 voff, ldst; lds_ptr rd[2];
    __device__ __forceinline__ void init(const void* base, u32 bytes, int row0, lds_ptr img, int w, int wrow, int lane) {
        srd = make_srd(base, bytes);
        ldst = (u32)(uintptr_t)img + w * 1024;
        const int g = lane >> 4, c = lane & 15;
        voff = (u32)(row0 + 8 * w + (lane >> 3)) * (D * 2) + (((lane & 7) ^ (4 * (w & 1) + g)) * 16);
#pragma unroll
        for (int ks = 0; ks < 2; ++ks) rd[ks] = img + (wrow + c) * 128 + (((4 * ks + g) ^ (c >> 1)) * 16);
    }
    __device__ __forceinline__ void issue(int slot, u32 pos, int i) const { dma16(ldst + pos + i * 4096, voff, srd, (u32)slot * 128u + i * (32u * D * 2u)); }
    __device__ __forceinline__ void read(u32x4& f, int pos, int i, int) const { f = lds_rd128(rd[i >> 2] + pos + (i & 3) * 2048); }
};

template <int NF, int NR, int KSTR, bool COH> struct OpReg {
    static constexpr int PPW = NF, RPF = 0;
    static constexpr bool REG = true;
    u32x4 srd; u32 voff;
    __device__ __forceinline__ void init(const void* base, u32 bytes, int row0, int k0, int lane) {
        srd = make_srd(base, bytes);
        voff = (u32)(row0 + (lane & 15) + ((lane >> 4) + 4 * k0) * NR) * 16u;
    }
    __device__ __forceinline__ void load(u32x4& f, int slot, int i) const {
        const u32 soff = (u32)slot * (KSTR * 4u * NR * 16u);
        if (COH) asm volatile("buffer_load_dwordx4 %0, %1, %2, %3 offen offset:%4 sc1" : "+v"(f) : "v"(voff), "s"(srd), "s"(soff), "n"(i * 256) : "memory");
        else asm volatile("buffer_load_dwordx4 %0, %1, %2, %3 offen offset:%4" : "+v"(f) : "v"(voff), "s"(srd), "s"(soff), "n"(i * 256) : "memory");
    }
};

template <int NA, int NB> struct Frags { u32x4 a[NA], b[NB]; };
template <int NS, int NA, int NB, int KS = 1> struct Engine { f32x4 acc[NA][NB]; Frags<NA * KS, NB * KS> f[2]; u32x4 ring[NS][NA]; };

#define STAG 1
#define RDX 2
template <int KS, int SWAP, bool READ, bool ISSUE, bool RISSUE, int NA, int NB, class A, class B>
__device__ __forceinline__ void gemm_step(f32x4 (&acc)[NA][NB], const Frags<NA * KS, NB * KS>& cur, Frags<NA * KS, NB * KS>& nxt, u32x4 (&ring)[NA],
                                          u32x4 (&prev)[NA], const A& a, const B& b, int rpos, int islot, u32 ipos) {
    constexpr int NM = NA * NB * KS, NRA = NA * A::RPF, NRK = NRA + NB * B::RPF, NRD = NRK * KS, RPM = (RDX * NRD + NM - 1) / NM;
    constexpr int ND = A::PPW + B::PPW, DSTEP = NM / ND, DOFF = STAG % DSTEP;
    if constexpr (A::REG) {
#pragma unroll
        for (int i = 0; i < NA; ++i) PIN(ring[i]);
    }
#pragma unroll
    for (int m = 0; m < NM; ++m) {
        const int i = m % NA, j = m / NA % NB, ks = m / (NA * NB);
        const u32x4 av = A::REG ? ring[i] : cur.a[ks * NA + i];
        if (i >= SWAP) mma16(acc[i][j], cur.b[ks * NB + j], av);
        else mma16(acc[i][j], av, cur.b[ks * NB + j]);
        if constexpr (READ) {
#pragma unroll
            for (int rr = m * RPM; rr < (m + 1) * RPM && rr < NRD; ++rr) {
                const int k = rr / NRK, r = rr % NRK;
                if (r < B::RPF) b.read(nxt.b[k * NB], rpos, k * NB, r);
                else if (r >= B::RPF + NRA) b.read(nxt.b[k * NB + (r - NRA) / B::RPF], rpos, k * NB + (r - NRA) / B::RPF, (r - NRA) % B::RPF);
                else if constexpr (!A::REG) a.read(nxt.a[k * NA + (r - B::RPF) / A::RPF], rpos, k * NA + (r - B::RPF) / A::RPF, (r - B::RPF) % A::RPF);
            }
        }
        if (m % DSTEP == DOFF && m / DSTEP < ND) {
            const int d = m / DSTEP;
            if (d < B::PPW) { if constexpr (ISSUE) b.issue(islot, ipos, d); }
            else if constexpr (A::REG) { if constexpr (RISSUE) a.load(prev[d - B::PPW], islot - 1, d - B::PPW); }
            else if constexpr (ISSUE) a.issue(islot, ipos, d - B::PPW);
        }
        SB();
    }
}

template <int V> struct IC { static constexpr int v = V; };
template <int NS, int PA, int PB, bool REG> constexpr int younger(int rem) {
    if (rem == 2) return 0;
    int n = 0;
    for (int k = 1; k <= NS - 2; ++k) n += (rem + k > NS ? PB + (REG ? 0 : PA) : 0) + (REG && rem + k > NS - 1 ? PA : 0);
    return n;
}
template <int P, int COUNT, int REM, int SLOTB, int SWAP, int XW, int NS, int NA, int NB, int KS, class A, class B, class Hook>
__device__ __forceinline__ void gemm_steps(Engine<NS, NA, NB, KS>& e, const A& a, const B& b, int islot, const Hook& hook) {
    constexpr bool READ = REM > 1;
    if constexpr (READ) wait_bar<younger<NS, A::PPW, B::PPW, A::REG>(REM) + (P < NS - 2 ? XW : 0)>();
    hook(IC<P>{});
    gemm_step<KS, SWAP, READ, (REM > NS), (A::REG && REM > NS - 1)>(e.acc, e.f[P & 1], e.f[(P & 1) ^ 1], e.ring[P % NS], e.ring[(P + NS - 1) % NS], a, b,
                                                                   (P + 1) % NS * SLOTB, islot + P, P % NS * SLOTB);
    if constexpr (COUNT > 1) gemm_steps<P + 1, COUNT - 1, REM - 1, SLOTB, SWAP, XW>(e, a, b, islot, hook);
    else if constexpr (REM == 1) mfma_settle();
}

template <int SLOTB, class A, class B> __device__ __forceinline__ void gemm_fill(const A& a, const B& b, int p0, int p1) {
#pragma unroll
    for (int p = p0; p < p1; ++p) {
#pragma unroll
        for (int i = 0; i < B::PPW; ++i) b.issue(p, p * SLOTB, i);
        if constexpr (!A::REG) {
#pragma unroll
            for (int i = 0; i < A::PPW; ++i) a.issue(p, p * SLOTB, i);
        }
    }
}
template <int NS, int NA, int NB, int KS, class A, class B> __device__ __forceinline__ void gemm_first(Engine<NS, NA, NB, KS>& e, const A& a, const B& b) {
    if constexpr (!A::REG) {
#pragma unroll
        for (int i = 0; i < KS * NA * A::RPF; ++i) a.read(e.f[0].a[i / A::RPF], 0, i / A::RPF, i % A::RPF);
    }
#pragma unroll
    for (int i = 0; i < KS * NB * B::RPF; ++i) b.read(e.f[0].b[i / B::RPF], 0, i / B::RPF, i % B::RPF);
    SB();
}

constexpr int MANY = 1 << 20;
template <int NS, int NSLOTS> struct Rounds { static constexpr int RL = NS % 2 ? 2 * NS : NS, N = (NSLOTS - NS) / RL, TAIL = NSLOTS - N * RL; };
template <int SLOTB, int NSLOTS, int SWAP, int NS, int NA, int NB, int KS, class A, class B, class Gate, class Round, class Tail>
__device__ __forceinline__ void gemm_run(Engine<NS, NA, NB, KS>& e, const A& a, const B& b, const Gate& gate, const Round& round, const Tail& tail) {
    using R = Rounds<NS, NSLOTS>;
#pragma unroll
    for (int i = 0; i < NA; ++i)
#pragma unroll
        for (int j = 0; j < NB; ++j) e.acc[i][j] = f32x4{};
    gemm_fill<SLOTB>(a, b, 0, NS);
    if constexpr (A::REG) {
        gate();
#pragma unroll
        for (int p = 0; p < NS; ++p)
#pragma unroll
            for (int i = 0; i < NA; ++i) { e.ring[p][i] = u32x4{}; if (p < NS - 1) a.load(e.ring[p][i], p, i); }
        wait_bar<NA>();
    } else {
        wait_bar<(NS - 1) * (A::PPW + B::PPW)>();
    }
    gemm_first(e, a, b);
    for (int r = 0; r < R::N; ++r) { round(r); gemm_steps<0, R::RL, MANY, SLOTB, SWAP, 0>(e, a, b, r * R::RL + NS, [](auto) {}); }
    gemm_steps<0, R::TAIL, R::TAIL, SLOTB, SWAP, 0>(e, a, b, R::N * R::RL + NS, tail);
}

constexpr int PRE_KV = 64, PRE_LN = 128, PRE_PF = 64, PRE_LDS = 8 * 16384 + 2048;

template <int CTRL> __device__ __forceinline__ float dpp_add(float v) {
    return v + __builtin_bit_cast(float, __builtin_amdgcn_update_dpp(0, __builtin_bit_cast(int, v), CTRL, 0xf, 0xf, true));
}
__device__ __forceinline__ float group_sum(float v) {
    const auto a = __builtin_amdgcn_permlane16_swap(__float_as_uint(v), __float_as_uint(v), false, false);
    v = __uint_as_float(a[0]) + __uint_as_float(a[1]);
    const auto b = __builtin_amdgcn_permlane32_swap(__float_as_uint(v), __float_as_uint(v), false, false);
    return __uint_as_float(b[0]) + __uint_as_float(b[1]);
}

__device__ __forceinline__ float row_sum(float v) { return dpp_add<0x140>(dpp_add<0x141>(dpp_add<0x4e>(dpp_add<0xb1>(v)))); }
__device__ __forceinline__ float fold32(float a, float b) {
    const auto r = __builtin_amdgcn_permlane32_swap(__float_as_uint(a), __float_as_uint(b), false, false);
    return __uint_as_float(r[0]) + __uint_as_float(r[1]);
}
__device__ __forceinline__ float fold16(float a, float b) {
    const auto r = __builtin_amdgcn_permlane16_swap(__float_as_uint(a), __float_as_uint(b), false, false);
    return __uint_as_float(r[0]) + __uint_as_float(r[1]);
}
template <int CTRL> __device__ __forceinline__ float dpp_max(float v) {
    return fmaxf(v, __builtin_bit_cast(float, __builtin_amdgcn_update_dpp(0, __builtin_bit_cast(int, v), CTRL, 0xf, 0xf, true)));
}
__device__ __forceinline__ float wave_max(float v) {
    v = dpp_max<0xb1>(v); v = dpp_max<0x4e>(v); v = dpp_max<0x141>(v); v = dpp_max<0x140>(v);
    const auto a = __builtin_amdgcn_permlane16_swap(__float_as_uint(v), __float_as_uint(v), false, false);
    v = fmaxf(__uint_as_float(a[0]), __uint_as_float(a[1]));
    const auto b = __builtin_amdgcn_permlane32_swap(__float_as_uint(v), __float_as_uint(v), false, false);
    return fmaxf(__uint_as_float(b[0]), __uint_as_float(b[1]));
}

template <u32 BYTES, int NB, int NTHR> __device__ __forceinline__ void touch(const char* p, int b, u32 scratch) {
    constexpr u32 LINES = BYTES / 128, PER = (LINES + NB - 1) / NB, N = (PER + NTHR - 1) / NTHR;
    const u32x4 srd = make_srd(p, BYTES);
#pragma unroll
    for (u32 i = 0; i < N; ++i) {
        const u32 l = i * NTHR + threadIdx.x, off = l < PER ? (b * PER + l) * 128u : 0x80000000u;
        asm volatile("s_mov_b32 m0, %0\n\ts_nop 0\n\tbuffer_load_dword %1, %2, 0 offen lds" ::"s"(scratch), "v"(off), "s"(srd) : "memory");
    }
}

__device__ __forceinline__ void warm_pre(const char* wq, const char* w1, int b, u32 scratch) {
    touch<D * D * 2, PRE_LN + PRE_PF, 512>(wq, b, scratch);
    touch<S * F * 2, PRE_LN + PRE_PF, 512>(w1, b, scratch);
}

__device__ __forceinline__ void warm_code(u32 scratch) {
    uint64_t a, e;
    asm volatile("s_getpc_b64 s[40:41]\n\ts_add_u32 s40, s40, main_code_begin@rel32@lo+4\n\ts_addc_u32 s41, s41, main_code_begin@rel32@hi+12" : "={s[40:41]}"(a) :: "scc");
    asm volatile("s_getpc_b64 s[42:43]\n\ts_add_u32 s42, s42, main_code_end@rel32@lo+4\n\ts_addc_u32 s43, s43, main_code_end@rel32@hi+12" : "={s[42:43]}"(e) :: "scc");
    const u32x4 srd = make_srd((const void*)a, e > a ? (u32)(e - a) & ~3u : 0u);
    asm volatile("s_mov_b32 m0, %0\n\ts_nop 0\n\tbuffer_load_dword %1, %2, 0 offen lds" ::"s"(scratch), "v"(threadIdx.x * 128u), "s"(srd) : "memory");
}

__device__ __forceinline__ void kv_block(int t, const char* x, const char* wk, const char* wv, char* ws, lds_ptr lds, int w8, int lane) {
    const int g = lane >> 4, c = lane & 15, q = (lane >> 2) & 3, p = lane & 3;
    const lds_ptr img = lds + w8 * 16384;
    const u32x4 sk = make_srd(wk, D * 64), sv = make_srd(wv, D * 64);
    const u32 voff = (u32)(lane >> 2) * 64 + ((((lane >> 1) & 1) ^ (lane >> 5)) * 32) + (lane & 1) * 16;
    u32x4 xf[2][4];
#pragma unroll
    for (int ks = 0; ks < 4; ++ks)
#pragma unroll
        for (int j = 0; j < 2; ++j) xf[j][ks] = *(const u32x4*)(x + (size_t)(t * 32 + j * 16 + c) * (D * 2) + (w8 * 128 + ks * 32 + g * 8) * 2);
#pragma unroll
    for (int i = 0; i < 8; ++i) {
        dma16((u32)(uintptr_t)img + i * 1024, voff, sk, w8 * 8192 + i * 1024);
        dma16((u32)(uintptr_t)img + 8192 + i * 1024, voff, sv, w8 * 8192 + i * 1024);
    }
    asm volatile("s_waitcnt vmcnt(0)" ::: "memory");
    f32x4 acc[8];
#pragma unroll
    for (int i = 0; i < 8; ++i) acc[i] = f32x4{};
#pragma unroll
    for (int ks = 0; ks < 4; ++ks)
#pragma unroll
        for (int i = 0; i < 4; ++i) {
            const lds_ptr rd = img + (i >> 1) * 8192 + (ks * 32 + 8 * g + q) * 64 + (((i & 1) ^ (g & 1)) * 32) + p * 8;
            const u32x2 lo = lds_rdtr(rd), hi = lds_rdtr(rd + 256);
            const u32x4 wf = {lo[0], lo[1], hi[0], hi[1]};
#pragma unroll
            for (int j = 0; j < 2; ++j) acc[2 * i + j] = i < 2 ? M16(wf, xf[j][ks], acc[2 * i + j]) : M16(xf[j][ks], wf, acc[2 * i + j]);
        }
#pragma unroll
    for (int i = 0; i < 8; ++i) *(LDS f32x4*)(img + i * 1024 + lane * 16) = acc[i];
    wait_bar<0>();
    STAMP(0, 6);
    if (w8 >= 4) return;
    const int ta = w8 < 2 ? w8 : 2 * w8, tb = w8 < 2 ? w8 + 2 : 2 * w8 + 1;
    f32x4 a = {}, b = {};
#pragma unroll
    for (int v = 0; v < 8; ++v) { a += *(LDS const f32x4*)(lds + v * 16384 + ta * 1024 + lane * 16); b += *(LDS const f32x4*)(lds + v * 16384 + tb * 1024 + lane * 16); }
    *(u32x4*)(ws + WS_KV + t * 4096 + w8 * 1024 + lane * 16) = pack8(a, b);
    float bnd = 0.f;
    if (w8 < 2) {
#pragma unroll
        for (int r = 0; r < 4; ++r) bnd += a[r] * a[r] + b[r] * b[r];
        bnd = group_sum(bnd);
        bnd = bnd == bnd ? bnd : __builtin_inff();
    } else {
#pragma unroll
        for (int r = 0; r < 4; ++r)
            if (!(fabsf(a[r]) <= SAFE_V && fabsf(b[r]) <= SAFE_V)) bnd = __builtin_inff();
    }
    bnd = wave_max(bnd);
    if (lane == 0) ((float*)(ws + WS_BND))[t * 4 + w8] = bnd;
}

__device__ __forceinline__ void ln_rows(int r, const char* x, const char* gw, const char* wq, const char* w1, char* ws, lds_ptr lds, u32 scratch, int w8, int lane) {
    const int u = w8 >> 2, cq = w8 & 3, chunk = 2 * r + u, row0 = 32 * (chunk >> 2) + 4 * (chunk & 3);
    u32x2 xv[8];
    float sh[8];
#pragma unroll
    for (int e = 0; e < 8; ++e) {
        const char* row = x + (size_t)(row0 + (e & 3) + 16 * (e >> 2)) * (D * 2);
        xv[e] = *(const u32x2*)(row + cq * 512 + lane * 8);
        sh[e] = bf_lo(*(const u32*)row);
    }
    u32x2 gv = *(const u32x2*)(gw + cq * 512 + lane * 8);
    PIN(gv);
#pragma unroll
    for (int e = 0; e < 8; ++e) PIN(xv[e]);
    warm_pre(wq, w1, r, scratch);
    float part[3][8];
#pragma unroll
    for (int e = 0; e < 8; ++e) {
        const float a0 = bf_lo(xv[e][0]), a1 = bf_hi(xv[e][0]), a2 = bf_lo(xv[e][1]), a3 = bf_hi(xv[e][1]);
        const float d0 = a0 - sh[e], d1 = a1 - sh[e], d2 = a2 - sh[e], d3 = a3 - sh[e];
        part[0][e] = (d0 + d1) + (d2 + d3);
        part[1][e] = (d0 * d0 + d1 * d1) + (d2 * d2 + d3 * d3);
        part[2][e] = (a0 * bf_lo(gv[0]) + a1 * bf_hi(gv[0])) + (a2 * bf_lo(gv[1]) + a3 * bf_hi(gv[1]));
    }
    LDS float* red = (LDS float*)(lds + 65536);
#pragma unroll
    for (int k = 0; k < 3; ++k)
#pragma unroll
        for (int h = 0; h < 2; ++h) {
            const float v = row_sum(fold16(fold32(part[k][4 * h], part[k][4 * h + 2]), fold32(part[k][4 * h + 1], part[k][4 * h + 3])));
            if ((lane & 15) == 0) red[w8 * 24 + k * 8 + 4 * h + (lane >> 4)] = v;
        }
    wait_bar<63>();
    STAMP(0, 6);
    const int e8 = lane & 7;
    float st[3], mysh = 0.f;
#pragma unroll
    for (int k = 0; k < 3; ++k) st[k] = (red[(4 * u) * 24 + k * 8 + e8] + red[(4 * u + 1) * 24 + k * 8 + e8]) + (red[(4 * u + 2) * 24 + k * 8 + e8] + red[(4 * u + 3) * 24 + k * 8 + e8]);
#pragma unroll
    for (int e = 0; e < 8; ++e) mysh = e8 == e ? sh[e] : mysh;
    const float dm = st[0] * (1.f / D), var = fmaxf(st[1] * (1.f / D) - dm * dm, 0.f);
    const float mean = bf_round(mysh + dm), rs = 1.f / bf_round(sqrtf(bf_round(bf_round(var) + 1e-5f)));
    if (cq == 0 && lane < 8) ((float*)(ws + WS_G))[row0 + (lane & 3) + 16 * (lane >> 2)] = bf_round(1.f / (1.f + __expf(-bf_round(st[2]))));
    float h[4][8];
#pragma unroll
    for (int e = 0; e < 8; ++e) {
        const float m = __uint_as_float(__builtin_amdgcn_readlane(__float_as_uint(mean), e)), q = __uint_as_float(__builtin_amdgcn_readlane(__float_as_uint(rs), e));
        h[0][e] = (bf_lo(xv[e][0]) - m) * q; h[1][e] = (bf_hi(xv[e][0]) - m) * q; h[2][e] = (bf_lo(xv[e][1]) - m) * q; h[3][e] = (bf_hi(xv[e][1]) - m) * q;
    }
    const lds_ptr tb = lds + w8 * 4096;
#pragma unroll
    for (int m = 0; m < 4; ++m)
        *(LDS u32x4*)(tb + lane * 64 + ((m ^ ((lane >> 1) & 3)) * 16)) = u32x4{cvtpk(h[m][0], h[m][1]), cvtpk(h[m][2], h[m][3]), cvtpk(h[m][4], h[m][5]), cvtpk(h[m][6], h[m][7])};
    char* dst = ws + WS_HF + (size_t)chunk * (D * 16) + cq * 4096 + lane * 16;
#pragma unroll
    for (int i = 0; i < 4; ++i) *(u32x4*)(dst + i * 1024) = lds_rd128(tb + (i * 16 + (lane >> 2)) * 64 + (((lane & 3) ^ ((lane >> 3) & 3)) * 16));
}

__global__ void __launch_bounds__(512, 2) k_pre(const char* __restrict__ x, const char* __restrict__ wq, const char* __restrict__ wk, const char* __restrict__ wv,
                                                const char* __restrict__ gw, const char* __restrict__ w1, char* __restrict__ ws) {
    extern __shared__ __attribute__((aligned(16))) char smem[];
    const int lane = threadIdx.x & 63, w8 = __builtin_amdgcn_readfirstlane(threadIdx.x >> 6), b = blockIdx.x;
    STAMP(0, 0);
    if (b < PRE_KV) kv_block(b, x, wk, wv, ws, (lds_ptr)smem, w8, lane);
    else {
        const int r = b - PRE_KV;
        const u32 scratch = (u32)(uintptr_t)(lds_ptr)smem + 8 * 16384 + w8 * 256;
        if (r < PRE_LN) ln_rows(r, x, gw, wq, w1, ws, (lds_ptr)smem, scratch, w8, lane);
        else {
            if (r < PRE_LN + CODE_PF) warm_code(scratch);
            warm_pre(wq, w1, r, scratch);
            if (r == PRE_LN && threadIdx.x < CNT_N) *(u32*)(ws + WS_CNT + threadIdx.x * CNT_STRIDE) = 0u;
        }
        asm volatile("s_waitcnt vmcnt(0)" ::: "memory");
    }
    STAMP(0, 7);
}

#define G1_PRIO 1
#define G1_NS_ 6
#define POST_PRIO 0
#define G_SLEEP 0
#define PF_SLEEP 64
#define Q_PRIO 2
#define PRIO_SWITCH 99
#define ATT_PRIO 0
constexpr int MID_ATT = 256, MID_G1 = 224, MID_PF = 32, G1_NS = G1_NS_;
constexpr int KV_STAGE = 16384, KV_NSTAGE = 16;

#define ATT_LOOP_ASM \
    "s_mov_b32 s23, 16\n" \
    "s_mov_b32 s24, 0xc000\n" \
    "s_mov_b32 s22, 0\n" \
    "s_add_u32 s20, %[ldst], 0x800\n" \
    "s_mov_b32 s21, 0x8800\n" \
    "ATT_LOOP_%=:\n" \
    "v_mfma_f32_16x16x32_bf16 v[8:11], v[80:83], v[68:71], 0\n" \
    "v_exp_f32 v0, v0\n" \
    "v_exp_f32 v1, v1\n" \
    "v_mfma_f32_16x16x32_bf16 v[12:15], v[84:87], v[68:71], 0\n" \
    "ds_read_b128 v[96:99], v124 offset:4096\n" \
    "v_exp_f32 v2, v2\n" \
    "v_exp_f32 v3, v3\n" \
    "v_mfma_f32_16x16x32_bf16 v[40:43], v[104:107], v[120:123], v[40:43]\n" \
    "v_cvt_pk_bf16_f32 v116, v0, v1\n" \
    "v_exp_f32 v4, v4\n" \
    "v_exp_f32 v5, v5\n" \
    "v_mfma_f32_16x16x32_bf16 v[44:47], v[108:111], v[120:123], v[44:47]\n" \
    "s_mov_b32 m0, s20\n" \
    "s_add_u32 s20, s20, 0x400\n" \
    "buffer_load_dwordx4 v126, %[srd], s21 offen lds\n" \
    "s_add_u32 s21, s21, 0x400\n" \
    "v_cvt_pk_bf16_f32 v117, v2, v3\n" \
    "v_exp_f32 v6, v6\n" \
    "v_exp_f32 v7, v7\n" \
    "v_mfma_f32_16x16x32_bf16 v[60:63], v[112:115], v[120:123], v[60:63]\n" \
    "v_cvt_pk_bf16_f32 v118, v4, v5\n" \
    "v_cvt_pk_bf16_f32 v119, v6, v7\n" \
    "s_waitcnt lgkmcnt(1)\n" \
    "v_mfma_f32_16x16x32_bf16 v[0:3], v[80:83], v[72:75], 0\n" \
    "v_exp_f32 v8, v8\n" \
    "v_exp_f32 v9, v9\n" \
    "v_mfma_f32_16x16x32_bf16 v[4:7], v[84:87], v[72:75], 0\n" \
    "ds_read_b128 v[100:103], v124 offset:5120\n" \
    "v_exp_f32 v10, v10\n" \
    "v_exp_f32 v11, v11\n" \
    "v_mfma_f32_16x16x32_bf16 v[16:19], v[88:91], v[116:119], v[16:19]\n" \
    "v_cvt_pk_bf16_f32 v120, v8, v9\n" \
    "v_exp_f32 v12, v12\n" \
    "v_exp_f32 v13, v13\n" \
    "v_mfma_f32_16x16x32_bf16 v[20:23], v[92:95], v[116:119], v[20:23]\n" \
    "v_cvt_pk_bf16_f32 v121, v10, v11\n" \
    "v_exp_f32 v14, v14\n" \
    "v_exp_f32 v15, v15\n" \
    "v_mfma_f32_16x16x32_bf16 v[48:51], v[112:115], v[116:119], v[48:51]\n" \
    "v_cvt_pk_bf16_f32 v122, v12, v13\n" \
    "v_cvt_pk_bf16_f32 v123, v14, v15\n" \
    "v_mfma_f32_16x16x32_bf16 v[8:11], v[80:83], v[76:79], 0\n" \
    "v_exp_f32 v0, v0\n" \
    "v_exp_f32 v1, v1\n" \
    "v_mfma_f32_16x16x32_bf16 v[12:15], v[84:87], v[76:79], 0\n" \
    "ds_read_b128 v[104:107], v124 offset:6144\n" \
    "v_exp_f32 v2, v2\n" \
    "v_exp_f32 v3, v3\n" \
    "v_mfma_f32_16x16x32_bf16 v[24:27], v[88:91], v[120:123], v[24:27]\n" \
    "v_cvt_pk_bf16_f32 v116, v0, v1\n" \
    "v_exp_f32 v4, v4\n" \
    "v_exp_f32 v5, v5\n" \
    "v_mfma_f32_16x16x32_bf16 v[28:31], v[92:95], v[120:123], v[28:31]\n" \
    "s_mov_b32 m0, s20\n" \
    "s_add_u32 s20, s20, 0x400\n" \
    "buffer_load_dwordx4 v126, %[srd], s21 offen lds\n" \
    "s_add_u32 s21, s21, 0x400\n" \
    "s_add_u32 s22, s22, 0x4000\n" \
    "s_and_b32 s22, s22, 0xffff\n" \
    "s_add_u32 s20, s22, %[ldst]\n" \
    "s_add_u32 s21, s21, 0x3000\n" \
    "v_cvt_pk_bf16_f32 v117, v2, v3\n" \
    "v_exp_f32 v6, v6\n" \
    "v_exp_f32 v7, v7\n" \
    "v_mfma_f32_16x16x32_bf16 v[52:55], v[112:115], v[120:123], v[52:55]\n" \
    "v_cvt_pk_bf16_f32 v118, v4, v5\n" \
    "v_cvt_pk_bf16_f32 v119, v6, v7\n" \
    "s_waitcnt lgkmcnt(1)\n" \
    "v_mfma_f32_16x16x32_bf16 v[0:3], v[96:99], v[64:67], 0\n" \
    "v_exp_f32 v8, v8\n" \
    "v_exp_f32 v9, v9\n" \
    "v_mfma_f32_16x16x32_bf16 v[4:7], v[100:103], v[64:67], 0\n" \
    "ds_read_b128 v[108:111], v124 offset:7168\n" \
    "v_exp_f32 v10, v10\n" \
    "v_exp_f32 v11, v11\n" \
    "v_mfma_f32_16x16x32_bf16 v[32:35], v[88:91], v[116:119], v[32:35]\n" \
    "v_cvt_pk_bf16_f32 v120, v8, v9\n" \
    "v_exp_f32 v12, v12\n" \
    "v_exp_f32 v13, v13\n" \
    "v_mfma_f32_16x16x32_bf16 v[36:39], v[92:95], v[116:119], v[36:39]\n" \
    "v_cvt_pk_bf16_f32 v121, v10, v11\n" \
    "v_exp_f32 v14, v14\n" \
    "v_exp_f32 v15, v15\n" \
    "v_mfma_f32_16x16x32_bf16 v[56:59], v[112:115], v[116:119], v[56:59]\n" \
    "v_cvt_pk_bf16_f32 v122, v12, v13\n" \
    "v_cvt_pk_bf16_f32 v123, v14, v15\n" \
    "v_mfma_f32_16x16x32_bf16 v[8:11], v[96:99], v[68:71], 0\n" \
    "v_exp_f32 v0, v0\n" \
    "v_exp_f32 v1, v1\n" \
    "v_mfma_f32_16x16x32_bf16 v[12:15], v[100:103], v[68:71], 0\n" \
    "ds_read_b128 v[80:83], v124 offset:8192\n" \
    "v_exp_f32 v2, v2\n" \
    "v_exp_f32 v3, v3\n" \
    "v_mfma_f32_16x16x32_bf16 v[40:43], v[88:91], v[120:123], v[40:43]\n" \
    "v_cvt_pk_bf16_f32 v116, v0, v1\n" \
    "v_exp_f32 v4, v4\n" \
    "v_exp_f32 v5, v5\n" \
    "v_mfma_f32_16x16x32_bf16 v[44:47], v[92:95], v[120:123], v[44:47]\n" \
    "v_cvt_pk_bf16_f32 v117, v2, v3\n" \
    "v_exp_f32 v6, v6\n" \
    "v_exp_f32 v7, v7\n" \
    "v_mfma_f32_16x16x32_bf16 v[60:63], v[112:115], v[120:123], v[60:63]\n" \
    "v_cvt_pk_bf16_f32 v118, v4, v5\n" \
    "v_cvt_pk_bf16_f32 v119, v6, v7\n" \
    "s_waitcnt lgkmcnt(1)\n" \
    "v_mfma_f32_16x16x32_bf16 v[0:3], v[96:99], v[72:75], 0\n" \
    "v_exp_f32 v8, v8\n" \
    "v_exp_f32 v9, v9\n" \
    "v_mfma_f32_16x16x32_bf16 v[4:7], v[100:103], v[72:75], 0\n" \
    "ds_read_b128 v[84:87], v124 offset:9216\n" \
    "v_exp_f32 v10, v10\n" \
    "v_exp_f32 v11, v11\n" \
    "v_mfma_f32_16x16x32_bf16 v[16:19], v[104:107], v[116:119], v[16:19]\n" \
    "v_cvt_pk_bf16_f32 v120, v8, v9\n" \
    "v_exp_f32 v12, v12\n" \
    "v_exp_f32 v13, v13\n" \
    "v_mfma_f32_16x16x32_bf16 v[20:23], v[108:111], v[116:119], v[20:23]\n" \
    "v_cvt_pk_bf16_f32 v121, v10, v11\n" \
    "v_exp_f32 v14, v14\n" \
    "v_exp_f32 v15, v15\n" \
    "v_mfma_f32_16x16x32_bf16 v[48:51], v[112:115], v[116:119], v[48:51]\n" \
    "v_cvt_pk_bf16_f32 v122, v12, v13\n" \
    "v_cvt_pk_bf16_f32 v123, v14, v15\n" \
    "v_mfma_f32_16x16x32_bf16 v[8:11], v[96:99], v[76:79], 0\n" \
    "v_exp_f32 v0, v0\n" \
    "v_exp_f32 v1, v1\n" \
    "v_mfma_f32_16x16x32_bf16 v[12:15], v[100:103], v[76:79], 0\n" \
    "ds_read_b128 v[88:91], v124 offset:10240\n" \
    "v_exp_f32 v2, v2\n" \
    "v_exp_f32 v3, v3\n" \
    "v_mfma_f32_16x16x32_bf16 v[24:27], v[104:107], v[120:123], v[24:27]\n" \
    "v_cvt_pk_bf16_f32 v116, v0, v1\n" \
    "v_exp_f32 v4, v4\n" \
    "v_exp_f32 v5, v5\n" \
    "v_mfma_f32_16x16x32_bf16 v[28:31], v[108:111], v[120:123], v[28:31]\n" \
    "v_cvt_pk_bf16_f32 v117, v2, v3\n" \
    "v_exp_f32 v6, v6\n" \
    "v_exp_f32 v7, v7\n" \
    "v_mfma_f32_16x16x32_bf16 v[52:55], v[112:115], v[120:123], v[52:55]\n" \
    "v_cvt_pk_bf16_f32 v118, v4, v5\n" \
    "v_cvt_pk_bf16_f32 v119, v6, v7\n" \
    "s_waitcnt lgkmcnt(1)\n" \
    "v_mfma_f32_16x16x32_bf16 v[0:3], v[80:83], v[64:67], 0\n" \
    "v_exp_f32 v8, v8\n" \
    "v_exp_f32 v9, v9\n" \
    "v_mfma_f32_16x16x32_bf16 v[4:7], v[84:87], v[64:67], 0\n" \
    "ds_read_b128 v[92:95], v124 offset:11264\n" \
    "v_exp_f32 v10, v10\n" \
    "v_exp_f32 v11, v11\n" \
    "v_mfma_f32_16x16x32_bf16 v[32:35], v[104:107], v[116:119], v[32:35]\n" \
    "v_cvt_pk_bf16_f32 v120, v8, v9\n" \
    "v_exp_f32 v12, v12\n" \
    "v_exp_f32 v13, v13\n" \
    "v_mfma_f32_16x16x32_bf16 v[36:39], v[108:111], v[116:119], v[36:39]\n" \
    "v_cvt_pk_bf16_f32 v121, v10, v11\n" \
    "v_exp_f32 v14, v14\n" \
    "v_exp_f32 v15, v15\n" \
    "v_mfma_f32_16x16x32_bf16 v[56:59], v[112:115], v[116:119], v[56:59]\n" \
    "v_cvt_pk_bf16_f32 v122, v12, v13\n" \
    "v_cvt_pk_bf16_f32 v123, v14, v15\n" \
    "v_mfma_f32_16x16x32_bf16 v[8:11], v[80:83], v[68:71], 0\n" \
    "v_exp_f32 v0, v0\n" \
    "v_exp_f32 v1, v1\n" \
    "v_mfma_f32_16x16x32_bf16 v[12:15], v[84:87], v[68:71], 0\n" \
    "ds_read_b128 v[96:99], v124 offset:12288\n" \
    "v_exp_f32 v2, v2\n" \
    "v_exp_f32 v3, v3\n" \
    "v_mfma_f32_16x16x32_bf16 v[40:43], v[104:107], v[120:123], v[40:43]\n" \
    "v_cvt_pk_bf16_f32 v116, v0, v1\n" \
    "v_exp_f32 v4, v4\n" \
    "v_exp_f32 v5, v5\n" \
    "v_mfma_f32_16x16x32_bf16 v[44:47], v[108:111], v[120:123], v[44:47]\n" \
    "v_cvt_pk_bf16_f32 v117, v2, v3\n" \
    "v_exp_f32 v6, v6\n" \
    "v_exp_f32 v7, v7\n" \
    "v_mfma_f32_16x16x32_bf16 v[60:63], v[112:115], v[120:123], v[60:63]\n" \
    "v_cvt_pk_bf16_f32 v118, v4, v5\n" \
    "v_cvt_pk_bf16_f32 v119, v6, v7\n" \
    "s_waitcnt lgkmcnt(1)\n" \
    "v_mfma_f32_16x16x32_bf16 v[0:3], v[80:83], v[72:75], 0\n" \
    "v_exp_f32 v8, v8\n" \
    "v_exp_f32 v9, v9\n" \
    "v_mfma_f32_16x16x32_bf16 v[4:7], v[84:87], v[72:75], 0\n" \
    "ds_read_b128 v[100:103], v124 offset:13312\n" \
    "v_exp_f32 v10, v10\n" \
    "v_exp_f32 v11, v11\n" \
    "v_mfma_f32_16x16x32_bf16 v[16:19], v[88:91], v[116:119], v[16:19]\n" \
    "v_cvt_pk_bf16_f32 v120, v8, v9\n" \
    "v_exp_f32 v12, v12\n" \
    "v_exp_f32 v13, v13\n" \
    "v_mfma_f32_16x16x32_bf16 v[20:23], v[92:95], v[116:119], v[20:23]\n" \
    "v_cvt_pk_bf16_f32 v121, v10, v11\n" \
    "v_exp_f32 v14, v14\n" \
    "v_exp_f32 v15, v15\n" \
    "v_mfma_f32_16x16x32_bf16 v[48:51], v[112:115], v[116:119], v[48:51]\n" \
    "v_cvt_pk_bf16_f32 v122, v12, v13\n" \
    "v_cvt_pk_bf16_f32 v123, v14, v15\n" \
    "v_mfma_f32_16x16x32_bf16 v[8:11], v[80:83], v[76:79], 0\n" \
    "v_exp_f32 v0, v0\n" \
    "v_exp_f32 v1, v1\n" \
    "v_mfma_f32_16x16x32_bf16 v[12:15], v[84:87], v[76:79], 0\n" \
    "ds_read_b128 v[104:107], v124 offset:14336\n" \
    "v_exp_f32 v2, v2\n" \
    "v_exp_f32 v3, v3\n" \
    "v_mfma_f32_16x16x32_bf16 v[24:27], v[88:91], v[120:123], v[24:27]\n" \
    "v_cvt_pk_bf16_f32 v116, v0, v1\n" \
    "v_exp_f32 v4, v4\n" \
    "v_exp_f32 v5, v5\n" \
    "v_mfma_f32_16x16x32_bf16 v[28:31], v[92:95], v[120:123], v[28:31]\n" \
    "v_cvt_pk_bf16_f32 v117, v2, v3\n" \
    "v_exp_f32 v6, v6\n" \
    "v_exp_f32 v7, v7\n" \
    "v_mfma_f32_16x16x32_bf16 v[52:55], v[112:115], v[120:123], v[52:55]\n" \
    "v_cvt_pk_bf16_f32 v118, v4, v5\n" \
    "v_cvt_pk_bf16_f32 v119, v6, v7\n" \
    "s_waitcnt lgkmcnt(1)\n" \
    "v_mfma_f32_16x16x32_bf16 v[0:3], v[96:99], v[64:67], 0\n" \
    "v_exp_f32 v8, v8\n" \
    "v_exp_f32 v9, v9\n" \
    "v_mfma_f32_16x16x32_bf16 v[4:7], v[100:103], v[64:67], 0\n" \
    "ds_read_b128 v[108:111], v124 offset:15360\n" \
    "v_exp_f32 v10, v10\n" \
    "v_exp_f32 v11, v11\n" \
    "v_mfma_f32_16x16x32_bf16 v[32:35], v[88:91], v[116:119], v[32:35]\n" \
    "v_cvt_pk_bf16_f32 v120, v8, v9\n" \
    "v_exp_f32 v12, v12\n" \
    "v_exp_f32 v13, v13\n" \
    "v_mfma_f32_16x16x32_bf16 v[36:39], v[92:95], v[116:119], v[36:39]\n" \
    "v_cvt_pk_bf16_f32 v121, v10, v11\n" \
    "v_exp_f32 v14, v14\n" \
    "v_exp_f32 v15, v15\n" \
    "v_mfma_f32_16x16x32_bf16 v[56:59], v[112:115], v[116:119], v[56:59]\n" \
    "v_cvt_pk_bf16_f32 v122, v12, v13\n" \
    "v_cvt_pk_bf16_f32 v123, v14, v15\n" \
    "s_waitcnt vmcnt(4)\n" \
    "s_barrier\n" \
    "v_mfma_f32_16x16x32_bf16 v[8:11], v[96:99], v[68:71], 0\n" \
    "v_exp_f32 v0, v0\n" \
    "v_exp_f32 v1, v1\n" \
    "v_mfma_f32_16x16x32_bf16 v[12:15], v[100:103], v[68:71], 0\n" \
    "ds_read_b128 v[80:83], v125 offset:0\n" \
    "v_exp_f32 v2, v2\n" \
    "v_exp_f32 v3, v3\n" \
    "v_mfma_f32_16x16x32_bf16 v[40:43], v[88:91], v[120:123], v[40:43]\n" \
    "v_cvt_pk_bf16_f32 v116, v0, v1\n" \
    "v_exp_f32 v4, v4\n" \
    "v_exp_f32 v5, v5\n" \
    "v_mfma_f32_16x16x32_bf16 v[44:47], v[92:95], v[120:123], v[44:47]\n" \
    "s_mov_b32 m0, s20\n" \
    "s_add_u32 s20, s20, 0x400\n" \
    "buffer_load_dwordx4 v126, %[srd], s21 offen lds\n" \
    "s_add_u32 s21, s21, 0x400\n" \
    "v_cvt_pk_bf16_f32 v117, v2, v3\n" \
    "v_exp_f32 v6, v6\n" \
    "v_exp_f32 v7, v7\n" \
    "v_mfma_f32_16x16x32_bf16 v[60:63], v[112:115], v[120:123], v[60:63]\n" \
    "v_cvt_pk_bf16_f32 v118, v4, v5\n" \
    "v_cvt_pk_bf16_f32 v119, v6, v7\n" \
    "s_waitcnt lgkmcnt(1)\n" \
    "v_mfma_f32_16x16x32_bf16 v[0:3], v[96:99], v[72:75], 0\n" \
    "v_exp_f32 v8, v8\n" \
    "v_exp_f32 v9, v9\n" \
    "v_mfma_f32_16x16x32_bf16 v[4:7], v[100:103], v[72:75], 0\n" \
    "ds_read_b128 v[84:87], v125 offset:1024\n" \
    "v_exp_f32 v10, v10\n" \
    "v_exp_f32 v11, v11\n" \
    "v_mfma_f32_16x16x32_bf16 v[16:19], v[104:107], v[116:119], v[16:19]\n" \
    "v_cvt_pk_bf16_f32 v120, v8, v9\n" \
    "v_exp_f32 v12, v12\n" \
    "v_exp_f32 v13, v13\n" \
    "v_mfma_f32_16x16x32_bf16 v[20:23], v[108:111], v[116:119], v[20:23]\n" \
    "v_cvt_pk_bf16_f32 v121, v10, v11\n" \
    "v_exp_f32 v14, v14\n" \
    "v_exp_f32 v15, v15\n" \
    "v_mfma_f32_16x16x32_bf16 v[48:51], v[112:115], v[116:119], v[48:51]\n" \
    "v_cvt_pk_bf16_f32 v122, v12, v13\n" \
    "v_cvt_pk_bf16_f32 v123, v14, v15\n" \
    "v_mfma_f32_16x16x32_bf16 v[8:11], v[96:99], v[76:79], 0\n" \
    "v_exp_f32 v0, v0\n" \
    "v_exp_f32 v1, v1\n" \
    "v_mfma_f32_16x16x32_bf16 v[12:15], v[100:103], v[76:79], 0\n" \
    "ds_read_b128 v[88:91], v125 offset:2048\n" \
    "v_exp_f32 v2, v2\n" \
    "v_exp_f32 v3, v3\n" \
    "v_mfma_f32_16x16x32_bf16 v[24:27], v[104:107], v[120:123], v[24:27]\n" \
    "v_cvt_pk_bf16_f32 v116, v0, v1\n" \
    "v_exp_f32 v4, v4\n" \
    "v_exp_f32 v5, v5\n" \
    "v_mfma_f32_16x16x32_bf16 v[28:31], v[108:111], v[120:123], v[28:31]\n" \
    "s_mov_b32 m0, s20\n" \
    "s_add_u32 s20, s20, 0x400\n" \
    "buffer_load_dwordx4 v126, %[srd], s21 offen lds\n" \
    "s_add_u32 s21, s21, 0x400\n" \
    "v_cvt_pk_bf16_f32 v117, v2, v3\n" \
    "v_exp_f32 v6, v6\n" \
    "v_exp_f32 v7, v7\n" \
    "v_mfma_f32_16x16x32_bf16 v[52:55], v[112:115], v[120:123], v[52:55]\n" \
    "v_cvt_pk_bf16_f32 v118, v4, v5\n" \
    "v_cvt_pk_bf16_f32 v119, v6, v7\n" \
    "s_waitcnt lgkmcnt(1)\n" \
    "v_mfma_f32_16x16x32_bf16 v[0:3], v[80:83], v[64:67], 0\n" \
    "v_exp_f32 v8, v8\n" \
    "v_exp_f32 v9, v9\n" \
    "v_mfma_f32_16x16x32_bf16 v[4:7], v[84:87], v[64:67], 0\n" \
    "ds_read_b128 v[92:95], v125 offset:3072\n" \
    "v_exp_f32 v10, v10\n" \
    "v_exp_f32 v11, v11\n" \
    "v_mfma_f32_16x16x32_bf16 v[32:35], v[104:107], v[116:119], v[32:35]\n" \
    "v_cvt_pk_bf16_f32 v120, v8, v9\n" \
    "v_exp_f32 v12, v12\n" \
    "v_exp_f32 v13, v13\n" \
    "v_mfma_f32_16x16x32_bf16 v[36:39], v[108:111], v[116:119], v[36:39]\n" \
    "v_cvt_pk_bf16_f32 v121, v10, v11\n" \
    "v_exp_f32 v14, v14\n" \
    "v_exp_f32 v15, v15\n" \
    "v_mfma_f32_16x16x32_bf16 v[56:59], v[112:115], v[116:119], v[56:59]\n" \
    "v_cvt_pk_bf16_f32 v122, v12, v13\n" \
    "v_cvt_pk_bf16_f32 v123, v14, v15\n" \
    "v_mov_b32 v124, v125\n" \
    "s_add_u32 s24, s24, 0x4000\n" \
    "s_and_b32 s24, s24, 0xffff\n" \
    "v_add_u32 v125, s24, v127\n" \
    "s_sub_u32 s23, s23, 1\n" \
    "s_cmp_lg_u32 s23, 0\n" \
    "s_cbranch_scc1 ATT_LOOP_%=\n" \
    "v_mfma_f32_16x16x32_bf16 v[40:43], v[104:107], v[120:123], v[40:43]\n" \
    "v_mfma_f32_16x16x32_bf16 v[44:47], v[108:111], v[120:123], v[44:47]\n" \
    "v_mfma_f32_16x16x32_bf16 v[60:63], v[112:115], v[120:123], v[60:63]\n" \
    "s_waitcnt vmcnt(0) lgkmcnt(0)\n" \
    "s_nop 15\n"

constexpr int Q_NS = 3, Q_SLOT = 24576;
__device__ __forceinline__ void attention(int b, const char* x, const char* wq, char* ws, lds_ptr lds, int w, int lane) {
    const int xc = b & 7, t = b >> 3, st128 = (xc >> 1) * 4 + (t & 3), hp = (xc & 1) * 8 + (t >> 2);
    const int head = 2 * hp + (w & 1), s0 = st128 * 128 + (w >> 1) * 64, g = lane >> 4, c = lane & 15;
    const u32x4 srd = make_srd(ws + WS_KV, S * 128);
    const u32 voff = w * 4096 + lane * 16, ldst = (u32)(uintptr_t)lds + w * 4096;
    auto dma = [&](int st, int p0, int p1) {
#pragma unroll
        for (int p = p0; p < p1; ++p) dma16(ldst + ((st + 2) & 3) * KV_STAGE + p * 1024, voff, srd, st * KV_STAGE + p * 1024);
    };
    __builtin_amdgcn_s_setprio(Q_PRIO);
    float gate[4];
#pragma unroll
    for (int i = 0; i < 4; ++i) asm volatile("global_load_dword %0, %1, off" : "=v"(gate[i]) : "v"(ws + WS_G + (s0 + i * 16 + c) * 4) : "memory");
    f32x4 kb2;
    asm volatile("global_load_dwordx4 %0, %1, off" : "=v"(kb2) : "v"(ws + WS_BND + lane * 16) : "memory");
    u32x4 q[4];
    {
        OpTr<64, D * 2, 2, true, 2> a; a.init(wq, D * D * 2, hp * 64, 0, lds, w, (w & 1) * 2, lane);
        OpRow bb; bb.init(x, S * D * 2, st128 * 128, lds + a.BYTES, w, (w >> 1) * 64, lane);
        Engine<Q_NS, 2, 4, 2> e;
        static_assert((D / 64 - 1) % Q_NS == 0 && Q_SLOT <= 2 * KV_STAGE, "the last Q slot lies below the first two K/V stages");
        gemm_run<Q_SLOT, D / 64, 2>(e, a, bb, [] {}, [](int) {}, [&](auto p) { if constexpr (p.v == Rounds<Q_NS, D / 64>::TAIL - 2) { dma(0, 0, 4); dma(1, 0, 4); } });
        wait_bar<4>();
        dma(2, 0, 2);
#pragma unroll
        for (int i = 0; i < 4; ++i) q[i] = pack8(e.acc[0][i] * QSCALE, e.acc[1][i] * QSCALE);
    }
    PIN(kb2);
    const float k2 = wave_max(fmaxf(fmaxf(kb2[0], kb2[1]), fmaxf(kb2[2], kb2[3])));
    bool ok = true;
#pragma unroll
    for (int i = 0; i < 4; ++i) {
        float n = 0.f;
#pragma unroll
        for (int m = 0; m < 4; ++m) { const float a = bf_lo(q[i][m]), h = bf_hi(q[i][m]); n += a * a + h * h; }
        ok = ok && group_sum(n) * k2 * 1.02f <= SAFE_LOG2 * SAFE_LOG2;
        PIN(gate[i]);
    }
    const bool safe = __all(ok);
    __builtin_amdgcn_s_setprio(ATT_PRIO);
    f32x4 o[8];
#pragma unroll
    for (int i = 0; i < 8; ++i) o[i] = f32x4{};
    float lsum[4];
    const lds_ptr base = lds + lane * 16;
    const f32x4 z = {};
    STAMP(1, 1);
    if (safe) {
        const u32 one = c == 0 ? 0x3f803f80u : 0u;
        const u32x4 ones = {one, one, one, one};
        const lds_ptr first = base + 2 * KV_STAGE;
        u32x4 ka = lds_rd128(first), kb = lds_rd128(first + 1024), v0 = lds_rd128(first + 2048), v1 = lds_rd128(first + 3072);
        f32x4 sa = M16(ka, q[0], z), sb = M16(kb, q[0], z);
        f32x16 oa = {}, ob = {}, l = {};
        u32x4 vz0 = {}, vz1 = {}, pz = {};
        u32 cur = (u32)(uintptr_t)first, nxt = cur + KV_STAGE;
        asm volatile("s_nop 7\n" ATT_LOOP_ASM
                     : "+{v[0:3]}"(sa), "+{v[4:7]}"(sb), "+{v[16:31]}"(oa), "+{v[32:47]}"(ob), "+{v[48:63]}"(l), "+{v[80:83]}"(ka), "+{v[84:87]}"(kb),
                       "+{v[88:91]}"(v0), "+{v[92:95]}"(v1), "+{v[104:107]}"(vz0), "+{v[108:111]}"(vz1), "+{v[120:123]}"(pz), "+{v124}"(cur), "+{v125}"(nxt)
                     : "{v[64:67]}"(q[0]), "{v[68:71]}"(q[1]), "{v[72:75]}"(q[2]), "{v[76:79]}"(q[3]), "{v[112:115]}"(ones), "{v126}"(voff),
                       "{v127}"((u32)(uintptr_t)base), [srd] "s"(srd), [ldst] "s"(ldst)
                     : "v8", "v9", "v10", "v11", "v12", "v13", "v14", "v15", "v96", "v97", "v98", "v99", "v100", "v101", "v102", "v103",
                       "v116", "v117", "v118", "v119", "s20", "s21", "s22", "s23", "s24", "scc", "memory");
#pragma unroll
        for (int i = 0; i < 4; ++i) {
            lsum[i] = __shfl(l[4 * i], c);
            o[i] = f32x4{oa[4 * i], oa[4 * i + 1], oa[4 * i + 2], oa[4 * i + 3]};
            o[4 + i] = f32x4{ob[4 * i], ob[4 * i + 1], ob[4 * i + 2], ob[4 * i + 3]};
        }
    } else {
        float mx[4];
#pragma unroll
        for (int i = 0; i < 4; ++i) { mx[i] = -__builtin_inff(); lsum[i] = 0.f; }
        dma(2, 2, 4);
        for (int st = 0; st < KV_NSTAGE; ++st) {
            wait_bar<4>();
            dma(st + 3, 0, 4);
#pragma unroll 1
            for (int kb = 0; kb < 4; ++kb) {
                const lds_ptr p = base + ((st + 2) & 3) * KV_STAGE + kb * 4096;
                const u32x4 k0 = lds_rd128(p), k1 = lds_rd128(p + 1024), v0 = lds_rd128(p + 2048), v1 = lds_rd128(p + 3072);
#pragma unroll
                for (int i = 0; i < 4; ++i) {
                    f32x4 sa = M16(k0, q[i], z), sb = M16(k1, q[i], z);
                    float m = mx[i];
#pragma unroll
                    for (int r = 0; r < 4; ++r) m = fmaxf(m, fmaxf(sa[r], sb[r]));
                    m = fmaxf(m, __shfl_xor(m, 16)); m = fmaxf(m, __shfl_xor(m, 32));
                    const float alpha = EX2(mx[i] - m);
                    mx[i] = m;
                    float t = 0.f;
#pragma unroll
                    for (int r = 0; r < 4; ++r) { sa[r] = EX2(sa[r] - m); sb[r] = EX2(sb[r] - m); t += sa[r] + sb[r]; }
                    lsum[i] = lsum[i] * alpha + t;
                    const u32x4 pp = pack8(sa, sb);
                    o[2 * i] = M16(v0, pp, o[2 * i] * alpha);
                    o[2 * i + 1] = M16(v1, pp, o[2 * i + 1] * alpha);
                }
            }
        }
        asm volatile("s_waitcnt vmcnt(0)" ::: "memory");
#pragma unroll
        for (int i = 0; i < 4; ++i) lsum[i] = group_sum(lsum[i]);
    }
    STAMP(1, 6);
#pragma unroll
    for (int i = 0; i < 4; ++i) {
        const float r = gate[i] / lsum[i];
        store_wt(ws + WS_OF, S * D * 2, (u32)((head * 4 + g) * S + s0 + i * 16 + c) * 16u, pack8(o[2 * i] * r, o[2 * i + 1] * r));
    }
    publish(ws, CNT_O + st128);
}

__device__ __forceinline__ f32x2 gelu_tanh(f32x2 x) {
    constexpr float C0 = -2.f * 0.7978845608028654f * 1.4426950408889634f, C1 = C0 * 0.044715f;
    const f32x2 y = x * __builtin_elementwise_fma(x * x, f32x2{C1, C1}, f32x2{C0, C0});
    const f32x2 d = f32x2{EX2(y[0]), EX2(y[1])} + 1.f;
    return x * f32x2{__builtin_amdgcn_rcpf(d[0]), __builtin_amdgcn_rcpf(d[1])};
}

__device__ __forceinline__ void gemm1_tile(int b, const void* w1, const char* b1, char* ws, lds_ptr lds, int w, int lane) {
    const int x = b & 7, t = b >> 3;
    const int dt = (x & 1) * 4 + (t & 3), d0 = dt * 128, f0 = ((x >> 1) * 7 + (t >> 2)) * 128;
    const int g = lane >> 4, c = lane & 15;
    u32x2 bias[8];
#pragma unroll
    for (int j = 0; j < 8; ++j) bias[j] = *(const u32x2*)(b1 + (f0 + j * 16 + g * 4) * 2);
    OpReg<2, D, 1, false> a; a.init(ws + WS_HF, S * D * 2, d0 + w * 32, 0, lane);
    OpTr<128, F * 2, 1, false, 8> bb; bb.init(w1, S * F * 2, f0, 0, lds, w, 0, lane);
    Engine<G1_NS, 2, 8> e;
    gemm_run<8192, S / 32, 0>(e, a, bb, [] {}, [](int) {}, [](auto) {});
    auto& acc = e.acc;
    STAMP(1, 1);
#pragma unroll
    for (int jp = 0; jp < 4; ++jp)
#pragma unroll
        for (int i = 0; i < 2; ++i) {
            f32x4 u[2];
#pragma unroll
            for (int h = 0; h < 2; ++h) {
                const u32x2 bv = bias[2 * jp + h];
                const f32x4 v = acc[i][2 * jp + h] + f32x4{bf_lo(bv[0]), bf_hi(bv[0]), bf_lo(bv[1]), bf_hi(bv[1])};
                const f32x2 lo = gelu_tanh(f32x2{v[0], v[1]}), hi = gelu_tanh(f32x2{v[2], v[3]});
                u[h] = f32x4{lo[0], lo[1], hi[0], hi[1]};
            }
            store_wt(ws + WS_UF, F * D * 2, (u32)(((f0 / 32 + jp) * 4 + g) * D + d0 + w * 32 + i * 16 + c) * 16u, pack8(u[0], u[1]));
        }
    publish(ws, CNT_U + dt * 2 + (x >> 2));
}

constexpr int P1_NS = 8, P1_BASE = 8192, P2_NS = 6, P2_SLOT = 12288;

__device__ __forceinline__ void post_tile(int b, const char* w2, const char* b2, const char* wo, char* ws, char* out, lds_ptr lds, int w, int lane) {
    const int x = b & 7, t = b >> 3, h = t & 1;
    const int s0 = ((x >> 1) * 8 + (t & 7)) * 64, dt = (x & 1) * 4 + (t >> 3), d0 = dt * 128;
    const int g = lane >> 4, c = lane & 15;
    constexpr int N1 = F / 64, N2 = D / 32;
    static_assert(N1 % P1_NS == 0 && P1_BASE + (P1_NS - 1) * 8192 >= (P2_NS - 1) * P2_SLOT, "the last w2 slot lies in the last wo slot");
    float omg[4], bs[4];
#pragma unroll
    for (int j = 0; j < 4; ++j) {
        const int s = s0 + j * 16 + c;
        asm volatile("global_load_dword %0, %1, off" : "=v"(omg[j]) : "v"(ws + WS_G + s * 4) : "memory");
        asm volatile("global_load_ushort %0, %1, off" : "=v"(bs[j]) : "v"(b2 + s * 2) : "memory");
    }
    OpTr<128, D * 2, 1, false, 2> a2; a2.init(wo, D * D * 2, d0, 0, lds, w, w * 2, lane);
    OpFrag<64, S, 1> b2o; b2o.init(ws + WS_OF, S * D * 2, s0, 0, lds + 8192, w, 0, lane);
    Engine<P2_NS, 2, 4> e;
    auto& acc = e.acc;
    u32 seen = 0;
    {
        OpReg<2, D, 1, !PLAIN_U> a1; a1.init(ws + WS_UF, F * D * 2, d0 + w * 32, h * N1, lane);
        OpTr<128, S * 2, 1, false, 8> b1; b1.init(w2, F * S * 2, s0 & ~127, h * N1, lds + P1_BASE, w, 4 * h, lane);
        Engine<P1_NS, 2, 8> e1;
        constexpr int TAIL = Rounds<P1_NS, N1>::TAIL;
        gemm_run<8192, N1, 2>(e1, a1, b1,
            [&] { if (w == 0) { await(ws, CNT_U + dt * 2 + h, F / 256); if (PLAIN_U) { acquire(); asm volatile("s_waitcnt vmcnt(0)" ::: "memory"); } } __builtin_amdgcn_s_barrier(); STAMP(1, 3); },
            [&](int r) { if (r == PRIO_SWITCH) __builtin_amdgcn_s_setprio(1); if (r == Rounds<P1_NS, N1>::N - 1) sample(seen, ws, CNT_O + (s0 >> 7)); },
            [&](auto p) { if constexpr (p.v == TAIL - 2) { confirm(seen, ws, CNT_O + (s0 >> 7), 16); if (PLAIN_O) acquire(); STAMP(1, 6); gemm_fill<P2_SLOT>(a2, b2o, 0, P2_NS - 1); } });
        STAMP(1, 4);
#pragma unroll
        for (int j = 0; j < 4; ++j) {
            PIN(omg[j]); PIN(bs[j]);
            omg[j] = bf_round(1.f - omg[j]); bs[j] = __uint_as_float(__float_as_uint(bs[j]) << 16);
#pragma unroll
            for (int i = 0; i < 2; ++i) {
                store_wt(ws + WS_X, 256u << 15, (u32)(((b * 4 + w) * 8 + i * 4 + j) * 64 + lane) * 16u, __builtin_bit_cast(u32x4, e1.acc[i][4 + j]));
                acc[i][j] = (e1.acc[i][j] + bs[j]) * omg[j];
            }
        }
    }
    constexpr int PPW = 3, XW = 8;
    wait_bar<(P2_NS - 2) * PPW + XW>();
    gemm_fill<P2_SLOT>(a2, b2o, P2_NS - 1, P2_NS);
    gemm_first(e, a2, b2o);
    gemm_steps<0, P2_NS, MANY, P2_SLOT, 2, XW>(e, a2, b2o, P2_NS, [&](auto p) { if constexpr (p.v == P2_NS - 2) signal(ws, CNT_X + b); });
    for (int r = 1; r < 3; ++r) gemm_steps<0, P2_NS, MANY, P2_SLOT, 2, 0>(e, a2, b2o, (r + 1) * P2_NS, [](auto) {});
    seen = 0;
    u32x4 got[8];
    const u32x4 xsrd = make_srd(ws + WS_X, 256u << 15);
    gemm_steps<0, N2 - 3 * P2_NS, N2 - 3 * P2_NS, P2_SLOT, 2, 0>(e, a2, b2o, 4 * P2_NS, [&](auto p) {
        if constexpr (p.v == 0) sample(seen, ws, CNT_X + (b ^ 8));
        if constexpr (p.v == P2_NS - 2) {
            confirm(seen, ws, CNT_X + (b ^ 8), 1);
#pragma unroll
            for (int i = 0; i < 8; ++i) {
                got[i] = u32x4{};
                load_coh(got[i], lane * 16, xsrd, (u32)(((b ^ 8) * 4 + w) * 8 + i) * 1024u);
            }
        }
    });
    asm volatile("s_waitcnt vmcnt(0)" ::: "memory");
#pragma unroll
    for (int i = 0; i < 8; ++i) { PIN(got[i]); acc[i >> 2][i & 3] += __builtin_bit_cast(f32x4, got[i]) * omg[i & 3]; }
    STAMP(1, 5);
#pragma unroll
    for (int j = 0; j < 4; ++j) {
        const auto r0 = __builtin_amdgcn_permlane16_swap(cvtpk(acc[0][j][0], acc[0][j][1]), cvtpk(acc[1][j][0], acc[1][j][1]), false, false);
        const auto r1 = __builtin_amdgcn_permlane16_swap(cvtpk(acc[0][j][2], acc[0][j][3]), cvtpk(acc[1][j][2], acc[1][j][3]), false, false);
        const int s = s0 + j * 16 + c, d = d0 + w * 32 + (g & 1) * 16 + (g >> 1) * 8;
        *(u32x4*)(out + ((size_t)s * D + d) * 2) = u32x4{r0[0], r1[0], r0[1], r1[1]};
    }
}

constexpr int MAIN_LDS = Q_NS * Q_SLOT;
static_assert(MAIN_LDS >= P2_NS * P2_SLOT && MAIN_LDS >= P1_BASE + P1_NS * 8192 && MAIN_LDS >= 4 * KV_STAGE && 2 * MAIN_LDS <= 160 * 1024, "two workgroups per CU");

__global__ void __launch_bounds__(256, 2) k_main(const char* __restrict__ x, const char* __restrict__ wq, const char* __restrict__ w1, const char* __restrict__ b1, const char* __restrict__ w2, const char* __restrict__ b2,
                                                 const char* __restrict__ wo, char* __restrict__ ws, char* __restrict__ out) {
    extern __shared__ __attribute__((aligned(16))) char smem[];
    const int lane = threadIdx.x & 63, w = __builtin_amdgcn_readfirstlane(threadIdx.x >> 6), b = blockIdx.x;
    asm volatile("main_code_begin:");
    STAMP(1, 0);
    if (b < MID_ATT) {
        attention(b, x, wq, ws, (lds_ptr)smem, w, lane);
    } else {
        const int gb = b - MID_ATT;
        __builtin_amdgcn_s_setprio(G1_PRIO);
        if (gb < MID_G1) gemm1_tile(gb, w1, b1, ws, (lds_ptr)smem, w, lane);
        else {
            const u32 scratch = (u32)(uintptr_t)(lds_ptr)smem + w * 256;
            __builtin_amdgcn_s_sleep(127);
            __builtin_amdgcn_s_sleep(PF_SLEEP);
            for (int i = 0; i < 4; ++i) {
                touch<F * S * 2, MID_PF * 4, 256>(w2, (gb - MID_G1) * 4 + i, scratch);
                asm volatile("s_waitcnt vmcnt(0)" ::: "memory");
            }
            touch<D * D * 2, MID_PF, 256>(wo, gb - MID_G1, scratch);
            wait_bar<0>();
        }
        STAMP(1, 2);
        __builtin_amdgcn_s_setprio(POST_PRIO);
        post_tile(gb, w2, b2, wo, ws, out, (lds_ptr)smem, w, lane);
    }
    STAMP(1, 7);
    asm volatile("main_code_end:");
}

extern "C" void kernel_launch(void* const* d_in, const int* in_sizes, int n_in, void* d_out, int out_size, void* d_ws, size_t ws_size,
                              hipStream_t stream) {
    const char* x = (const char*)d_in[0];
    const char *wq = (const char*)d_in[1], *wk = (const char*)d_in[2], *wv = (const char*)d_in[3], *wo = (const char*)d_in[4];
    const char *w1 = (const char*)d_in[6], *b1 = (const char*)d_in[7], *w2 = (const char*)d_in[8], *b2 = (const char*)d_in[9];
    const char* gw = (const char*)d_in[10];
    char* ws = (char*)d_ws;
    (void)hipFuncSetAttribute((const void*)k_pre, hipFuncAttributeMaxDynamicSharedMemorySize, PRE_LDS);
    (void)hipFuncSetAttribute((const void*)k_main, hipFuncAttributeMaxDynamicSharedMemorySize, MAIN_LDS);
    k_pre<<<PRE_KV + PRE_LN + PRE_PF, 512, PRE_LDS, stream>>>(x, wq, wk, wv, gw, w1, ws);
    k_main<<<MID_ATT + MID_G1 + MID_PF, 256, MAIN_LDS, stream>>>(x, wq, w1, b1, w2, b2, wo, ws, (char*)d_out);
}
```

```cpp
#include <hip/hip_runtime.h>
#include <stdint.h>

typedef unsigned u32;
typedef u32 u32x4 __attribute__((ext_vector_type(4)));
typedef u32 u32x2 __attribute__((ext_vector_type(2)));
typedef float f32x2 __attribute__((ext_vector_type(2)));
typedef float f32x4 __attribute__((ext_vector_type(4)));
typedef float f32x16 __attribute__((ext_vector_type(16)));
typedef __bf16 bf16x2 __attribute__((ext_vector_type(2)));
typedef __bf16 bf16x8 __attribute__((ext_vector_type(8)));
typedef short s16x4 __attribute__((ext_vector_type(4)));
#define LDS __attribute__((address_space(3)))
typedef LDS char* lds_ptr;

constexpr int S = 2048, D = 1024, F = 3584;
constexpr u32 WS_HF = 0, WS_OF = 8u << 20, WS_UF = 12u << 20, WS_KV = 20u << 20;
constexpr u32 WS_G = WS_KV + (256u << 10), WS_BND = WS_G + (8u << 10), WS_CNT = WS_BND + (4u << 10);
constexpr u32 WS_X = 24u << 20;
constexpr int CNT_U = 0, CNT_O = 16, CNT_X = 32, CNT_N = 288, CNT_STRIDE = 256;
constexpr float QSCALE = 0.17677669529663687f * 1.4426950408889634f;
constexpr float SAFE_LOG2 = 60.f;
constexpr float SAFE_V = 1.0e12f;

#define PLAIN_U 0
#define PLAIN_O 0
#define CODE_PF 8
#define W1_ROWS 1024
#define W1_ROUND 1
#define SB() __builtin_amdgcn_sched_barrier(0)
#define PIN(x) asm volatile("" : "+v"(x))
#define M16(a, b, c) __builtin_amdgcn_mfma_f32_16x16x32_bf16(__builtin_bit_cast(bf16x8, a), __builtin_bit_cast(bf16x8, b), c, 0, 0, 0)
#define M32(a, b, c) __builtin_amdgcn_mfma_f32_32x32x16_bf16(__builtin_bit_cast(bf16x8, a), __builtin_bit_cast(bf16x8, b), c, 0, 0, 0)
__device__ __forceinline__ void mma16(f32x4& c, const u32x4& a, const u32x4& b) { asm volatile("v_mfma_f32_16x16x32_bf16 %0, %1, %2, %0" : "+v"(c) : "v"(a), "v"(b)); }
__device__ __forceinline__ void mfma_settle() { asm volatile("s_nop 7\n\ts_nop 1" ::: "memory"); SB(); }
#define EX2(x) __builtin_amdgcn_exp2f(x)

__device__ __forceinline__ u32 cvtpk(float lo, float hi) { f32x2 v = {lo, hi}; return __builtin_bit_cast(u32, __builtin_convertvector(v, bf16x2)); }
__device__ __forceinline__ float bf_lo(u32 v) { return __uint_as_float(v << 16); }
__device__ __forceinline__ float bf_hi(u32 v) { return __uint_as_float(v & 0xffff0000u); }
__device__ __forceinline__ float bf_round(float x) { return bf_lo(cvtpk(x, x)); }
__device__ __forceinline__ u32x4 pack8(const f32x4& a, const f32x4& b) { return u32x4{cvtpk(a[0], a[1]), cvtpk(a[2], a[3]), cvtpk(b[0], b[1]), cvtpk(b[2], b[3])}; }

__device__ __forceinline__ u32x4 make_srd(const void* p, u32 bytes) {
    const uint64_t a = (uint64_t)p;
    return u32x4{(u32)a, (u32)(a >> 32) & 0xffffu, bytes, 0x00020000u};
}
__device__ __forceinline__ void dma16(u32 lds, u32 voff, const u32x4& srd, u32 soff) {
    asm volatile("s_mov_b32 m0, %0\n\ts_nop 0\n\tbuffer_load_dwordx4 %1, %2, %3 offen lds" ::"s"(lds), "v"(voff), "s"(srd), "s"(soff) : "memory");
}
__device__ __forceinline__ void dma16_coh(u32 lds, u32 voff, const u32x4& srd, u32 soff) {
    asm volatile("s_mov_b32 m0, %0\n\ts_nop 0\n\tbuffer_load_dwordx4 %1, %2, %3 offen sc1 lds" ::"s"(lds), "v"(voff), "s"(srd), "s"(soff) : "memory");
}
template <int N> __device__ __forceinline__ void wait_bar() {
    __builtin_amdgcn_s_waitcnt(0xc07f);
    asm volatile("s_waitcnt vmcnt(%0)\n\ts_barrier" ::"n"(N) : "memory");
}
__device__ __forceinline__ u32x4 lds_rd128(lds_ptr p) { return *(LDS const u32x4*)p; }
__device__ __forceinline__ u32x2 lds_rdtr(lds_ptr p) { return __builtin_bit_cast(u32x2, __builtin_amdgcn_ds_read_tr16_b64_v4i16((LDS s16x4*)p)); }

typedef __attribute__((address_space(1))) u32 gu32;
__device__ __forceinline__ void store_wt(char* base, u32 bytes, u32 off, const u32x4& v) {
    __builtin_amdgcn_raw_buffer_store_b128(v, __builtin_amdgcn_make_buffer_rsrc(base, 0, bytes, 0x00020000), off, 0, 16);
}
__device__ __forceinline__ void load_coh(u32x4& f, u32 voff, const u32x4& srd, u32 soff) {
    asm volatile("buffer_load_dwordx4 %0, %1, %2, %3 offen sc1" : "+v"(f) : "v"(voff), "s"(srd), "s"(soff) : "memory");
}
__device__ __forceinline__ void signal(char* ws, int cnt) {
    if (threadIdx.x == 0) __hip_atomic_fetch_add((gu32*)(ws + WS_CNT + cnt * CNT_STRIDE), 1u, __ATOMIC_RELAXED, __HIP_MEMORY_SCOPE_AGENT);
}
__device__ __forceinline__ void publish(char* ws, int cnt) {
    asm volatile("s_waitcnt vmcnt(0)\n\ts_barrier" ::: "memory");
    signal(ws, cnt);
}
__device__ __forceinline__ void await(char* ws, int cnt, u32 target) {
    gu32* p = (gu32*)(ws + WS_CNT + cnt * CNT_STRIDE);
    for (int spins = 0; spins < (1 << 20); ++spins) {
        if (__builtin_amdgcn_readfirstlane(__hip_atomic_load(p, __ATOMIC_RELAXED, __HIP_MEMORY_SCOPE_AGENT)) >= target) break;
        __builtin_amdgcn_s_sleep(1);
    }
    asm volatile("" ::: "memory");
}
__device__ __forceinline__ void acquire() { asm volatile("buffer_inv sc1" ::: "memory"); }

__device__ __forceinline__ void sample(u32& seen, char* ws, int cnt) {
    asm volatile("global_load_dword %0, %1, off sc1" : "+v"(seen) : "v"(ws + WS_CNT + cnt * CNT_STRIDE) : "memory");
}
__device__ __forceinline__ void confirm(u32& seen, char* ws, int cnt, u32 target) {
    PIN(seen);
    if (__builtin_amdgcn_readfirstlane(seen) < target) await(ws, cnt, target);
}

#define STAMP(k, i)

template <int R, int NR, int KSTR> struct OpFrag {
    static constexpr int BYTES = 4 * R * 16, PPW = BYTES / 4096, RPF = 1;
    static constexpr bool REG = false;
    u32x4 srd; u32 voff, ldst; lds_ptr rd;
    __device__ __forceinline__ void init(const void* base, u32 bytes, int row0, int k0, lds_ptr img, int w, int wrow, int lane) {
        srd = make_srd(base, bytes);
        voff = (u32)(row0 + lane + (w + 4 * k0) * NR) * 16u;
        ldst = (u32)(uintptr_t)img + w * (R * 16);
        rd = img + ((lane >> 4) * R + wrow + (lane & 15)) * 16;
    }
    __device__ __forceinline__ void issue(int slot, u32 pos, int i) const { (PLAIN_O ? dma16 : dma16_coh)(ldst + pos + i * 1024, voff, srd, (u32)slot * (KSTR * 4u * NR * 16u) + i * 1024u); }
    __device__ __forceinline__ void read(u32x4& f, int pos, int i, int) const { f = lds_rd128(rd + pos + i * 256); }
};

template <int C, int RS, int KS, bool NAT, int NF> struct OpTr {
    static constexpr int BYTES = KS * 32 * C * 2, PPW = BYTES / 4096, RPF = 2;
    static constexpr bool REG = false;
    u32x4 srd; u32 voff, ldst; lds_ptr rd[NF];
    __device__ __forceinline__ void init(const void* base, u32 bytes, int col0, int k0, lds_ptr img, int w, int wch, int lane) {
        srd = make_srd(base, bytes);
        ldst = (u32)(uintptr_t)img + w * 1024;
        const int g = lane >> 4, q = (lane >> 2) & 3, p = lane & 3;
        if constexpr (C == 128) {
            const int f = NAT ? (g | (((w >> 1) & 1) << 2)) : (4 * (w & 1) + g);
            voff = (u32)(32 * k0 + 4 * w + g) * RS + col0 * 2 + ((((lane & 15) >> 1) ^ f) * 32) + (lane & 1) * 16;
            const int row = NAT ? 8 * g + q : 4 * g + q, fr = NAT ? (q | ((g & 1) << 2)) : (row & 7);
#pragma unroll
            for (int i = 0; i < NF; ++i) rd[i] = img + row * 256 + (((wch ^ i) ^ fr) * 32) + p * 8;
        } else {
            static_assert(C == 128 || C == 64, "unsupported transposed operand");
            const int f = NAT ? ((g & 1) | ((w & 1) << 1)) : g;
            voff = (u32)(32 * k0 + 8 * w + (lane >> 3)) * RS + col0 * 2 + ((((lane & 7) >> 1) ^ f) * 32) + (lane & 1) * 16;
            const int row = NAT ? 8 * g + q : 4 * g + q, fr = NAT ? ((q >> 1) | ((g & 1) << 1)) : ((row >> 1) & 3);
#pragma unroll
            for (int i = 0; i < NF; ++i) rd[i] = img + row * 128 + (((wch ^ i) ^ fr) * 32) + p * 8;
        }
    }
    __device__ __forceinline__ void issue(int slot, u32 pos, int i) const { dma16(ldst + pos + i * 4096, voff, srd, (u32)(slot * KS * 32 + i * (2048 / C)) * RS); }
    __device__ __forceinline__ void read(u32x4& f, int pos, int i, int t) const {
        const u32x2 v = lds_rdtr(rd[i % NF] + pos + i / NF * (32 * C * 2) + t * ((NAT ? 4 : 16) * C * 2));
        f[2 * t] = v[0]; f[2 * t + 1] = v[1];
    }
};

struct OpRow {
    static constexpr int BYTES = 128 * 128, PPW = 4, RPF = 1;
    static constexpr bool REG = false;
    u32x4 srd; u32 voff, ldst; lds_ptr rd[2];
    __device__ __forceinline__ void init(const void* base, u32 bytes, int row0, lds_ptr img, int w, int wrow, int lane) {
        srd = make_srd(base, bytes);
        ldst = (u32)(uintptr_t)img + w * 1024;
        const int g = lane >> 4, c = lane & 15;
        voff = (u32)(row0 + 8 * w + (lane >> 3)) * (D * 2) + (((lane & 7) ^ (4 * (w & 1) + g)) * 16);
#pragma unroll
        for (int ks = 0; ks < 2; ++ks) rd[ks] = img + (wrow + c) * 128 + (((4 * ks + g) ^ (c >> 1)) * 16);
    }
    __device__ __forceinline__ void issue(int slot, u32 pos, int i) const { dma16(ldst + pos + i * 4096, voff, srd, (u32)slot * 128u + i * (32u * D * 2u)); }
    __device__ __forceinline__ void read(u32x4& f, int pos, int i, int) const { f = lds_rd128(rd[i >> 2] + pos + (i & 3) * 2048); }
};

template <int NF, int NR, int KSTR, bool COH> struct OpReg {
    static constexpr int PPW = NF, RPF = 0;
    static constexpr bool REG = true;
    u32x4 srd; u32 voff;
    __device__ __forceinline__ void init(const void* base, u32 bytes, int row0, int k0, int lane) {
        srd = make_srd(base, bytes);
        voff = (u32)(row0 + (lane & 15) + ((lane >> 4) + 4 * k0) * NR) * 16u;
    }
    __device__ __forceinline__ void load(u32x4& f, int slot, int i) const {
        const u32 soff = (u32)slot * (KSTR * 4u * NR * 16u);
        if (COH) asm volatile("buffer_load_dwordx4 %0, %1, %2, %3 offen offset:%4 sc1" : "+v"(f) : "v"(voff), "s"(srd), "s"(soff), "n"(i * 256) : "memory");
        else asm volatile("buffer_load_dwordx4 %0, %1, %2, %3 offen offset:%4" : "+v"(f) : "v"(voff), "s"(srd), "s"(soff), "n"(i * 256) : "memory");
    }
};

template <int NA, int NB> struct Frags { u32x4 a[NA], b[NB]; };
template <int NS, int NA, int NB, int KS = 1> struct Engine { f32x4 acc[NA][NB]; Frags<NA * KS, NB * KS> f[2]; u32x4 ring[NS][NA]; };

#define STAG 1
#define RDX 2
template <int KS, int SWAP, bool READ, bool ISSUE, bool RISSUE, int NA, int NB, class A, class B>
__device__ __forceinline__ void gemm_step(f32x4 (&acc)[NA][NB], const Frags<NA * KS, NB * KS>& cur, Frags<NA * KS, NB * KS>& nxt, u32x4 (&ring)[NA],
                                          u32x4 (&prev)[NA], const A& a, const B& b, int rpos, int islot, u32 ipos) {
    constexpr int NM = NA * NB * KS, NRA = NA * A::RPF, NRK = NRA + NB * B::RPF, NRD = NRK * KS, RPM = (RDX * NRD + NM - 1) / NM;
    constexpr int ND = A::PPW + B::PPW, DSTEP = NM / ND, DOFF = STAG % DSTEP;
    if constexpr (A::REG) {
#pragma unroll
        for (int i = 0; i < NA; ++i) PIN(ring[i]);
    }
#pragma unroll
    for (int m = 0; m < NM; ++m) {
        const int i = m % NA, j = m / NA % NB, ks = m / (NA * NB);
        const u32x4 av = A::REG ? ring[i] : cur.a[ks * NA + i];
        if (i >= SWAP) mma16(acc[i][j], cur.b[ks * NB + j], av);
        else mma16(acc[i][j], av, cur.b[ks * NB + j]);
        if constexpr (READ) {
#pragma unroll
            for (int rr = m * RPM; rr < (m + 1) * RPM && rr < NRD; ++rr) {
                const int k = rr / NRK, r = rr % NRK;
                if (r < B::RPF) b.read(nxt.b[k * NB], rpos, k * NB, r);
                else if (r >= B::RPF + NRA) b.read(nxt.b[k * NB + (r - NRA) / B::RPF], rpos, k * NB + (r - NRA) / B::RPF, (r - NRA) % B::RPF);
                else if constexpr (!A::REG) a.read(nxt.a[k * NA + (r - B::RPF) / A::RPF], rpos, k * NA + (r - B::RPF) / A::RPF, (r - B::RPF) % A::RPF);
            }
        }
        if (m % DSTEP == DOFF && m / DSTEP < ND) {
            const int d = m / DSTEP;
            if (d < B::PPW) { if constexpr (ISSUE) b.issue(islot, ipos, d); }
            else if constexpr (A::REG) { if constexpr (RISSUE) a.load(prev[d - B::PPW], islot - 1, d - B::PPW); }
            else if constexpr (ISSUE) a.issue(islot, ipos, d - B::PPW);
        }
        SB();
    }
}

template <int V> struct IC { static constexpr int v = V; };
template <int NS, int PA, int PB, bool REG> constexpr int younger(int rem) {
    if (rem == 2) return 0;
    int n = 0;
    for (int k = 1; k <= NS - 2; ++k) n += (rem + k > NS ? PB + (REG ? 0 : PA) : 0) + (REG && rem + k > NS - 1 ? PA : 0);
    return n;
}
template <int P, int COUNT, int REM, int SLOTB, int SWAP, int XW, int NS, int NA, int NB, int KS, class A, class B, class Hook>
__device__ __forceinline__ void gemm_steps(Engine<NS, NA, NB, KS>& e, const A& a, const B& b, int islot, const Hook& hook) {
    constexpr bool READ = REM > 1;
    if constexpr (READ) wait_bar<younger<NS, A::PPW, B::PPW, A::REG>(REM) + (P < NS - 2 ? XW : 0)>();
    hook(IC<P>{});
    gemm_step<KS, SWAP, READ, (REM > NS), (A::REG && REM > NS - 1)>(e.acc, e.f[P & 1], e.f[(P & 1) ^ 1], e.ring[P % NS], e.ring[(P + NS - 1) % NS], a, b,
                                                                   (P + 1) % NS * SLOTB, islot + P, P % NS * SLOTB);
    if constexpr (COUNT > 1) gemm_steps<P + 1, COUNT - 1, REM - 1, SLOTB, SWAP, XW>(e, a, b, islot, hook);
    else if constexpr (REM == 1) mfma_settle();
}

template <int SLOTB, class A, class B> __device__ __forceinline__ void gemm_fill(const A& a, const B& b, int p0, int p1) {
#pragma unroll
    for (int p = p0; p < p1; ++p) {
#pragma unroll
        for (int i = 0; i < B::PPW; ++i) b.issue(p, p * SLOTB, i);
        if constexpr (!A::REG) {
#pragma unroll
            for (int i = 0; i < A::PPW; ++i) a.issue(p, p * SLOTB, i);
        }
    }
}
template <int NS, int NA, int NB, int KS, class A, class B> __device__ __forceinline__ void gemm_first(Engine<NS, NA, NB, KS>& e, const A& a, const B& b) {
    if constexpr (!A::REG) {
#pragma unroll
        for (int i = 0; i < KS * NA * A::RPF; ++i) a.read(e.f[0].a[i / A::RPF], 0, i / A::RPF, i % A::RPF);
    }
#pragma unroll
    for (int i = 0; i < KS * NB * B::RPF; ++i) b.read(e.f[0].b[i / B::RPF], 0, i / B::RPF, i % B::RPF);
    SB();
}

constexpr int MANY = 1 << 20;
template <int NS, int NSLOTS> struct Rounds { static constexpr int RL = NS % 2 ? 2 * NS : NS, N = (NSLOTS - NS) / RL, TAIL = NSLOTS - N * RL; };
template <int SLOTB, int NSLOTS, int SWAP, int NS, int NA, int NB, int KS, class A, class B, class Gate, class Round, class Tail>
__device__ __forceinline__ void gemm_run(Engine<NS, NA, NB, KS>& e, const A& a, const B& b, const Gate& gate, const Round& round, const Tail& tail) {
    using R = Rounds<NS, NSLOTS>;
#pragma unroll
    for (int i = 0; i < NA; ++i)
#pragma unroll
        for (int j = 0; j < NB; ++j) e.acc[i][j] = f32x4{};
    gemm_fill<SLOTB>(a, b, 0, NS);
    if constexpr (A::REG) {
        gate();
#pragma unroll
        for (int p = 0; p < NS; ++p)
#pragma unroll
            for (int i = 0; i < NA; ++i) { e.ring[p][i] = u32x4{}; if (p < NS - 1) a.load(e.ring[p][i], p, i); }
        wait_bar<NA>();
    } else {
        wait_bar<(NS - 1) * (A::PPW + B::PPW)>();
    }
    gemm_first(e, a, b);
    for (int r = 0; r < R::N; ++r) { round(r); gemm_steps<0, R::RL, MANY, SLOTB, SWAP, 0>(e, a, b, r * R::RL + NS, [](auto) {}); }
    gemm_steps<0, R::TAIL, R::TAIL, SLOTB, SWAP, 0>(e, a, b, R::N * R::RL + NS, tail);
}

constexpr int PRE_KV = 128, PRE_LN = 128, PRE_LDS = 65536 + 1024 + 2048, W1_PRE = W1_ROWS * F * 2;

template <int CTRL> __device__ __forceinline__ float dpp_add(float v) {
    return v + __builtin_bit_cast(float, __builtin_amdgcn_update_dpp(0, __builtin_bit_cast(int, v), CTRL, 0xf, 0xf, true));
}
__device__ __forceinline__ float group_sum(float v) {
    const auto a = __builtin_amdgcn_permlane16_swap(__float_as_uint(v), __float_as_uint(v), false, false);
    v = __uint_as_float(a[0]) + __uint_as_float(a[1]);
    const auto b = __builtin_amdgcn_permlane32_swap(__float_as_uint(v), __float_as_uint(v), false, false);
    return __uint_as_float(b[0]) + __uint_as_float(b[1]);
}

__device__ __forceinline__ float row_sum(float v) { return dpp_add<0x140>(dpp_add<0x141>(dpp_add<0x4e>(dpp_add<0xb1>(v)))); }
__device__ __forceinline__ float fold32(float a, float b) {
    const auto r = __builtin_amdgcn_permlane32_swap(__float_as_uint(a), __float_as_uint(b), false, false);
    return __uint_as_float(r[0]) + __uint_as_float(r[1]);
}
__device__ __forceinline__ float fold16(float a, float b) {
    const auto r = __builtin_amdgcn_permlane16_swap(__float_as_uint(a), __float_as_uint(b), false, false);
    return __uint_as_float(r[0]) + __uint_as_float(r[1]);
}
template <int CTRL> __device__ __forceinline__ float dpp_max(float v) {
    return fmaxf(v, __builtin_bit_cast(float, __builtin_amdgcn_update_dpp(0, __builtin_bit_cast(int, v), CTRL, 0xf, 0xf, true)));
}
__device__ __forceinline__ float wave_max(float v) {
    v = dpp_max<0xb1>(v); v = dpp_max<0x4e>(v); v = dpp_max<0x141>(v); v = dpp_max<0x140>(v);
    const auto a = __builtin_amdgcn_permlane16_swap(__float_as_uint(v), __float_as_uint(v), false, false);
    v = fmaxf(__uint_as_float(a[0]), __uint_as_float(a[1]));
    const auto b = __builtin_amdgcn_permlane32_swap(__float_as_uint(v), __float_as_uint(v), false, false);
    return fmaxf(__uint_as_float(b[0]), __uint_as_float(b[1]));
}

template <u32 BYTES, int NB, int NTHR> __device__ __forceinline__ void touch(const char* p, int b, u32 scratch) {
    constexpr u32 LINES = BYTES / 128, PER = (LINES + NB - 1) / NB, N = (PER + NTHR - 1) / NTHR;
    const u32x4 srd = make_srd(p, BYTES);
#pragma unroll
    for (u32 i = 0; i < N; ++i) {
        const u32 l = i * NTHR + threadIdx.x, off = l < PER ? (b * PER + l) * 128u : 0x80000000u;
        asm volatile("s_mov_b32 m0, %0\n\ts_nop 0\n\tbuffer_load_dword %1, %2, 0 offen lds" ::"s"(scratch), "v"(off), "s"(srd) : "memory");
    }
}

__device__ __forceinline__ void warm_code(u32 scratch) {
    uint64_t a, e;
    asm volatile("s_getpc_b64 s[40:41]\n\ts_add_u32 s40, s40, main_code_begin@rel32@lo+4\n\ts_addc_u32 s41, s41, main_code_begin@rel32@hi+12" : "={s[40:41]}"(a) :: "scc");
    asm volatile("s_getpc_b64 s[42:43]\n\ts_add_u32 s42, s42, main_code_end@rel32@lo+4\n\ts_addc_u32 s43, s43, main_code_end@rel32@hi+12" : "={s[42:43]}"(e) :: "scc");
    const u32x4 srd = make_srd((const void*)a, e > a ? (u32)(e - a) & ~3u : 0u);
    asm volatile("s_mov_b32 m0, %0\n\ts_nop 0\n\tbuffer_load_dword %1, %2, 0 offen lds" ::"s"(scratch), "v"(threadIdx.x * 128u), "s"(srd) : "memory");
}

template <bool V> __device__ __forceinline__ void kv_block(int t, const char* x, const char* wm, char* ws, lds_ptr lds, int w8, int lane) {
    const int g = lane >> 4, c = lane & 15, q = (lane >> 2) & 3, p = lane & 3;
    const lds_ptr img = lds + w8 * 8192;
    const u32x4 srd = make_srd(wm, D * 64);
    const u32 voff = (u32)(lane >> 2) * 64 + ((((lane >> 1) & 1) ^ (lane >> 5)) * 32) + (lane & 1) * 16;
    u32x4 xf[2][4];
#pragma unroll
    for (int ks = 0; ks < 4; ++ks)
#pragma unroll
        for (int j = 0; j < 2; ++j) xf[j][ks] = *(const u32x4*)(x + (size_t)(t * 32 + j * 16 + c) * (D * 2) + (w8 * 128 + ks * 32 + g * 8) * 2);
#pragma unroll
    for (int i = 0; i < 8; ++i) dma16((u32)(uintptr_t)img + i * 1024, voff, srd, w8 * 8192 + i * 1024);
    asm volatile("s_waitcnt vmcnt(0)" ::: "memory");
    f32x4 acc[4];
#pragma unroll
    for (int i = 0; i < 4; ++i) acc[i] = f32x4{};
#pragma unroll
    for (int ks = 0; ks < 4; ++ks)
#pragma unroll
        for (int i = 0; i < 2; ++i) {
            const lds_ptr rd = img + (ks * 32 + 8 * g + q) * 64 + ((i ^ (g & 1)) * 32) + p * 8;
            const u32x2 lo = lds_rdtr(rd), hi = lds_rdtr(rd + 256);
            const u32x4 wf = {lo[0], lo[1], hi[0], hi[1]};
#pragma unroll
            for (int j = 0; j < 2; ++j) acc[2 * i + j] = V ? M16(xf[j][ks], wf, acc[2 * i + j]) : M16(wf, xf[j][ks], acc[2 * i + j]);
        }
#pragma unroll
    for (int i = 0; i < 4; ++i) *(LDS f32x4*)(img + i * 1024 + lane * 16) = acc[i];
    wait_bar<0>();
    STAMP(0, 6);
    if (w8 >= 2) return;
    const int ta = V ? 2 * w8 : w8, tb = V ? 2 * w8 + 1 : w8 + 2, fr = t * 4 + 2 * V + w8;
    f32x4 a = {}, b = {};
#pragma unroll
    for (int v = 0; v < 8; ++v) { a += *(LDS const f32x4*)(lds + v * 8192 + ta * 1024 + lane * 16); b += *(LDS const f32x4*)(lds + v * 8192 + tb * 1024 + lane * 16); }
    *(u32x4*)(ws + WS_KV + fr * 1024 + lane * 16) = pack8(a, b);
    float bnd = 0.f;
    if (!V) {
#pragma unroll
        for (int r = 0; r < 4; ++r) bnd += a[r] * a[r] + b[r] * b[r];
        bnd = group_sum(bnd);
        bnd = bnd == bnd ? bnd : __builtin_inff();
    } else {
#pragma unroll
        for (int r = 0; r < 4; ++r)
            if (!(fabsf(a[r]) <= SAFE_V && fabsf(b[r]) <= SAFE_V)) bnd = __builtin_inff();
    }
    bnd = wave_max(bnd);
    if (lane == 0) ((float*)(ws + WS_BND))[fr] = bnd;
}

__device__ __forceinline__ void ln_rows(int r, const char* x, const char* gw, const char* wq, const char* w1, char* ws, lds_ptr lds, u32 scratch, int w8, int lane) {
    const int u = w8 >> 2, cq = w8 & 3, chunk = 2 * r + u, row0 = 32 * (chunk >> 2) + 4 * (chunk & 3);
    u32x2 xv[8];
    float sh[8];
#pragma unroll
    for (int e = 0; e < 8; ++e) {
        const char* row = x + (size_t)(row0 + (e & 3) + 16 * (e >> 2)) * (D * 2);
        xv[e] = *(const u32x2*)(row + cq * 512 + lane * 8);
        sh[e] = bf_lo(*(const u32*)row);
    }
    u32x2 gv = *(const u32x2*)(gw + cq * 512 + lane * 8);
    PIN(gv);
#pragma unroll
    for (int e = 0; e < 8; ++e) PIN(xv[e]);
    if (r < 8) warm_code(scratch);
    touch<D * D * 2, PRE_LN, 512>(wq, r, scratch);
    touch<W1_PRE, PRE_LN, 512>(w1, r, scratch);
    float part[3][8];
#pragma unroll
    for (int e = 0; e < 8; ++e) {
        const float a0 = bf_lo(xv[e][0]), a1 = bf_hi(xv[e][0]), a2 = bf_lo(xv[e][1]), a3 = bf_hi(xv[e][1]);
        const float d0 = a0 - sh[e], d1 = a1 - sh[e], d2 = a2 - sh[e], d3 = a3 - sh[e];
        part[0][e] = (d0 + d1) + (d2 + d3);
        part[1][e] = (d0 * d0 + d1 * d1) + (d2 * d2 + d3 * d3);
        part[2][e] = (a0 * bf_lo(gv[0]) + a1 * bf_hi(gv[0])) + (a2 * bf_lo(gv[1]) + a3 * bf_hi(gv[1]));
    }
    LDS float* red = (LDS float*)(lds + 65536);
#pragma unroll
    for (int k = 0; k < 3; ++k)
#pragma unroll
        for (int h = 0; h < 2; ++h) {
            const float v = row_sum(fold16(fold32(part[k][4 * h], part[k][4 * h + 2]), fold32(part[k][4 * h + 1], part[k][4 * h + 3])));
            if ((lane & 15) == 0) red[w8 * 24 + k * 8 + 4 * h + (lane >> 4)] = v;
        }
    wait_bar<63>();
    STAMP(0, 6);
    const int e8 = lane & 7;
    float st[3], mysh = 0.f;
#pragma unroll
    for (int k = 0; k < 3; ++k) st[k] = (red[(4 * u) * 24 + k * 8 + e8] + red[(4 * u + 1) * 24 + k * 8 + e8]) + (red[(4 * u + 2) * 24 + k * 8 + e8] + red[(4 * u + 3) * 24 + k * 8 + e8]);
#pragma unroll
    for (int e = 0; e < 8; ++e) mysh = e8 == e ? sh[e] : mysh;
    const float dm = st[0] * (1.f / D), var = fmaxf(st[1] * (1.f / D) - dm * dm, 0.f);
    const float mean = bf_round(mysh + dm), rs = 1.f / bf_round(sqrtf(bf_round(bf_round(var) + 1e-5f)));
    if (cq == 0 && lane < 8) ((float*)(ws + WS_G))[row0 + (lane & 3) + 16 * (lane >> 2)] = bf_round(1.f / (1.f + __expf(-bf_round(st[2]))));
    float h[4][8];
#pragma unroll
    for (int e = 0; e < 8; ++e) {
        const float m = __uint_as_float(__builtin_amdgcn_readlane(__float_as_uint(mean), e)), q = __uint_as_float(__builtin_amdgcn_readlane(__float_as_uint(rs), e));
        h[0][e] = (bf_lo(xv[e][0]) - m) * q; h[1][e] = (bf_hi(xv[e][0]) - m) * q; h[2][e] = (bf_lo(xv[e][1]) - m) * q; h[3][e] = (bf_hi(xv[e][1]) - m) * q;
    }
    const lds_ptr tb = lds + w8 * 4096;
#pragma unroll
    for (int m = 0; m < 4; ++m)
        *(LDS u32x4*)(tb + lane * 64 + ((m ^ ((lane >> 1) & 3)) * 16)) = u32x4{cvtpk(h[m][0], h[m][1]), cvtpk(h[m][2], h[m][3]), cvtpk(h[m][4], h[m][5]), cvtpk(h[m][6], h[m][7])};
    char* dst = ws + WS_HF + (size_t)chunk * (D * 16) + cq * 4096 + lane * 16;
#pragma unroll
    for (int i = 0; i < 4; ++i) *(u32x4*)(dst + i * 1024) = lds_rd128(tb + (i * 16 + (lane >> 2)) * 64 + (((lane & 3) ^ ((lane >> 3) & 3)) * 16));
}

__global__ void __launch_bounds__(512, 2) k_pre(const char* __restrict__ x, const char* __restrict__ wq, const char* __restrict__ wk, const char* __restrict__ wv,
                                                const char* __restrict__ gw, const char* __restrict__ w1, char* __restrict__ ws) {
    extern __shared__ __attribute__((aligned(16))) char smem[];
    const int lane = threadIdx.x & 63, w8 = __builtin_amdgcn_readfirstlane(threadIdx.x >> 6), b = blockIdx.x;
    STAMP(0, 0);
    if (b >= PRE_KV) {
        ln_rows(b - PRE_KV, x, gw, wq, w1, ws, (lds_ptr)smem, (u32)(uintptr_t)(lds_ptr)smem + 65536 + 1024 + w8 * 256, w8, lane);
        if (b == PRE_KV && threadIdx.x < CNT_N) *(u32*)(ws + WS_CNT + threadIdx.x * CNT_STRIDE) = 0u;
        asm volatile("s_waitcnt vmcnt(0)" ::: "memory");
    } else if (b & 1) kv_block<true>(b >> 1, x, wv, ws, (lds_ptr)smem, w8, lane);
    else kv_block<false>(b >> 1, x, wk, ws, (lds_ptr)smem, w8, lane);
    STAMP(0, 7);
}

#define G1_PRIO 1
#define G1_NS_ 6
#define POST_PRIO 0
#define G_SLEEP 0
#define PF_SLEEP 5
#define Q_PRIO 2
#define PRIO_SWITCH 99
#define ATT_PRIO 0
constexpr int MID_ATT = 256, MID_G1 = 224, MID_PF = 32, G1_NS = G1_NS_;
constexpr int KV_STAGE = 16384, KV_NSTAGE = 16;

#define ATT_LOOP_ASM \
    "s_mov_b32 s23, 16\n" \
    "s_mov_b32 s24, 0xc000\n" \
    "s_mov_b32 s22, 0\n" \
    "s_add_u32 s20, %[ldst], 0x800\n" \
    "s_mov_b32 s21, 0x8800\n" \
    "ATT_LOOP_%=:\n" \
    "v_mfma_f32_16x16x32_bf16 v[8:11], v[80:83], v[68:71], 0\n" \
    "v_exp_f32 v0, v0\n" \
    "v_exp_f32 v1, v1\n" \
    "v_mfma_f32_16x16x32_bf16 v[12:15], v[84:87], v[68:71], 0\n" \
    "ds_read_b128 v[96:99], v124 offset:4096\n" \
    "v_exp_f32 v2, v2\n" \
    "v_exp_f32 v3, v3\n" \
    "v_mfma_f32_16x16x32_bf16 v[40:43], v[104:107], v[120:123], v[40:43]\n" \
    "v_cvt_pk_bf16_f32 v116, v0, v1\n" \
    "v_exp_f32 v4, v4\n" \
    "v_exp_f32 v5, v5\n" \
    "v_mfma_f32_16x16x32_bf16 v[44:47], v[108:111], v[120:123], v[44:47]\n" \
    "s_mov_b32 m0, s20\n" \
    "s_add_u32 s20, s20, 0x400\n" \
    "buffer_load_dwordx4 v126, %[srd], s21 offen lds\n" \
    "s_add_u32 s21, s21, 0x400\n" \
    "v_cvt_pk_bf16_f32 v117, v2, v3\n" \
    "v_exp_f32 v6, v6\n" \
    "v_exp_f32 v7, v7\n" \
    "v_mfma_f32_16x16x32_bf16 v[60:63], v[112:115], v[120:123], v[60:63]\n" \
    "v_cvt_pk_bf16_f32 v118, v4, v5\n" \
    "v_cvt_pk_bf16_f32 v119, v6, v7\n" \
    "s_waitcnt lgkmcnt(1)\n" \
    "v_mfma_f32_16x16x32_bf16 v[0:3], v[80:83], v[72:75], 0\n" \
    "v_exp_f32 v8, v8\n" \
    "v_exp_f32 v9, v9\n" \
    "v_mfma_f32_16x16x32_bf16 v[4:7], v[84:87], v[72:75], 0\n" \
    "ds_read_b128 v[100:103], v124 offset:5120\n" \
    "v_exp_f32 v10, v10\n" \
    "v_exp_f32 v11, v11\n" \
    "v_mfma_f32_16x16x32_bf16 v[16:19], v[88:91], v[116:119], v[16:19]\n" \
    "v_cvt_pk_bf16_f32 v120, v8, v9\n" \
    "v_exp_f32 v12, v12\n" \
    "v_exp_f32 v13, v13\n" \
    "v_mfma_f32_16x16x32_bf16 v[20:23], v[92:95], v[116:119], v[20:23]\n" \
    "v_cvt_pk_bf16_f32 v121, v10, v11\n" \
    "v_exp_f32 v14, v14\n" \
    "v_exp_f32 v15, v15\n" \
    "v_mfma_f32_16x16x32_bf16 v[48:51], v[112:115], v[116:119], v[48:51]\n" \
    "v_cvt_pk_bf16_f32 v122, v12, v13\n" \
    "v_cvt_pk_bf16_f32 v123, v14, v15\n" \
    "v_mfma_f32_16x16x32_bf16 v[8:11], v[80:83], v[76:79], 0\n" \
    "v_exp_f32 v0, v0\n" \
    "v_exp_f32 v1, v1\n" \
    "v_mfma_f32_16x16x32_bf16 v[12:15], v[84:87], v[76:79], 0\n" \
    "ds_read_b128 v[104:107], v124 offset:6144\n" \
    "v_exp_f32 v2, v2\n" \
    "v_exp_f32 v3, v3\n" \
    "v_mfma_f32_16x16x32_bf16 v[24:27], v[88:91], v[120:123], v[24:27]\n" \
    "v_cvt_pk_bf16_f32 v116, v0, v1\n" \
    "v_exp_f32 v4, v4\n" \
    "v_exp_f32 v5, v5\n" \
    "v_mfma_f32_16x16x32_bf16 v[28:31], v[92:95], v[120:123], v[28:31]\n" \
    "s_mov_b32 m0, s20\n" \
    "s_add_u32 s20, s20, 0x400\n" \
    "buffer_load_dwordx4 v126, %[srd], s21 offen lds\n" \
    "s_add_u32 s21, s21, 0x400\n" \
    "s_add_u32 s22, s22, 0x4000\n" \
    "s_and_b32 s22, s22, 0xffff\n" \
    "s_add_u32 s20, s22, %[ldst]\n" \
    "s_add_u32 s21, s21, 0x3000\n" \
    "v_cvt_pk_bf16_f32 v117, v2, v3\n" \
    "v_exp_f32 v6, v6\n" \
    "v_exp_f32 v7, v7\n" \
    "v_mfma_f32_16x16x32_bf16 v[52:55], v[112:115], v[120:123], v[52:55]\n" \
    "v_cvt_pk_bf16_f32 v118, v4, v5\n" \
    "v_cvt_pk_bf16_f32 v119, v6, v7\n" \
    "s_waitcnt lgkmcnt(1)\n" \
    "v_mfma_f32_16x16x32_bf16 v[0:3], v[96:99], v[64:67], 0\n" \
    "v_exp_f32 v8, v8\n" \
    "v_exp_f32 v9, v9\n" \
    "v_mfma_f32_16x16x32_bf16 v[4:7], v[100:103], v[64:67], 0\n" \
    "ds_read_b128 v[108:111], v124 offset:7168\n" \
    "v_exp_f32 v10, v10\n" \
    "v_exp_f32 v11, v11\n" \
    "v_mfma_f32_16x16x32_bf16 v[32:35], v[88:91], v[116:119], v[32:35]\n" \
    "v_cvt_pk_bf16_f32 v120, v8, v9\n" \
    "v_exp_f32 v12, v12\n" \
    "v_exp_f32 v13, v13\n" \
    "v_mfma_f32_16x16x32_bf16 v[36:39], v[92:95], v[116:119], v[36:39]\n" \
    "v_cvt_pk_bf16_f32 v121, v10, v11\n" \
    "v_exp_f32 v14, v14\n" \
    "v_exp_f32 v15, v15\n" \
    "v_mfma_f32_16x16x32_bf16 v[56:59], v[112:115], v[116:119], v[56:59]\n" \
    "v_cvt_pk_bf16_f32 v122, v12, v13\n" \
    "v_cvt_pk_bf16_f32 v123, v14, v15\n" \
    "v_mfma_f32_16x16x32_bf16 v[8:11], v[96:99], v[68:71], 0\n" \
    "v_exp_f32 v0, v0\n" \
    "v_exp_f32 v1, v1\n" \
    "v_mfma_f32_16x16x32_bf16 v[12:15], v[100:103], v[68:71], 0\n" \
    "ds_read_b128 v[80:83], v124 offset:8192\n" \
    "v_exp_f32 v2, v2\n" \
    "v_exp_f32 v3, v3\n" \
    "v_mfma_f32_16x16x32_bf16 v[40:43], v[88:91], v[120:123], v[40:43]\n" \
    "v_cvt_pk_bf16_f32 v116, v0, v1\n" \
    "v_exp_f32 v4, v4\n" \
    "v_exp_f32 v5, v5\n" \
    "v_mfma_f32_16x16x32_bf16 v[44:47], v[92:95], v[120:123], v[44:47]\n" \
    "v_cvt_pk_bf16_f32 v117, v2, v3\n" \
    "v_exp_f32 v6, v6\n" \
    "v_exp_f32 v7, v7\n" \
    "v_mfma_f32_16x16x32_bf16 v[60:63], v[112:115], v[120:123], v[60:63]\n" \
    "v_cvt_pk_bf16_f32 v118, v4, v5\n" \
    "v_cvt_pk_bf16_f32 v119, v6, v7\n" \
    "s_waitcnt lgkmcnt(1)\n" \
    "v_mfma_f32_16x16x32_bf16 v[0:3], v[96:99], v[72:75], 0\n" \
    "v_exp_f32 v8, v8\n" \
    "v_exp_f32 v9, v9\n" \
    "v_mfma_f32_16x16x32_bf16 v[4:7], v[100:103], v[72:75], 0\n" \
    "ds_read_b128 v[84:87], v124 offset:9216\n" \
    "v_exp_f32 v10, v10\n" \
    "v_exp_f32 v11, v11\n" \
    "v_mfma_f32_16x16x32_bf16 v[16:19], v[104:107], v[116:119], v[16:19]\n" \
    "v_cvt_pk_bf16_f32 v120, v8, v9\n" \
    "v_exp_f32 v12, v12\n" \
    "v_exp_f32 v13, v13\n" \
    "v_mfma_f32_16x16x32_bf16 v[20:23], v[108:111], v[116:119], v[20:23]\n" \
    "v_cvt_pk_bf16_f32 v121, v10, v11\n" \
    "v_exp_f32 v14, v14\n" \
    "v_exp_f32 v15, v15\n" \
    "v_mfma_f32_16x16x32_bf16 v[48:51], v[112:115], v[116:119], v[48:51]\n" \
    "v_cvt_pk_bf16_f32 v122, v12, v13\n" \
    "v_cvt_pk_bf16_f32 v123, v14, v15\n" \
    "v_mfma_f32_16x16x32_bf16 v[8:11], v[96:99], v[76:79], 0\n" \
    "v_exp_f32 v0, v0\n" \
    "v_exp_f32 v1, v1\n" \
    "v_mfma_f32_16x16x32_bf16 v[12:15], v[100:103], v[76:79], 0\n" \
    "ds_read_b128 v[88:91], v124 offset:10240\n" \
    "v_exp_f32 v2, v2\n" \
    "v_exp_f32 v3, v3\n" \
    "v_mfma_f32_16x16x32_bf16 v[24:27], v[104:107], v[120:123], v[24:27]\n" \
    "v_cvt_pk_bf16_f32 v116, v0, v1\n" \
    "v_exp_f32 v4, v4\n" \
    "v_exp_f32 v5, v5\n" \
    "v_mfma_f32_16x16x32_bf16 v[28:31], v[108:111], v[120:123], v[28:31]\n" \
    "v_cvt_pk_bf16_f32 v117, v2, v3\n" \
    "v_exp_f32 v6, v6\n" \
    "v_exp_f32 v7, v7\n" \
    "v_mfma_f32_16x16x32_bf16 v[52:55], v[112:115], v[120:123], v[52:55]\n" \
    "v_cvt_pk_bf16_f32 v118, v4, v5\n" \
    "v_cvt_pk_bf16_f32 v119, v6, v7\n" \
    "s_waitcnt lgkmcnt(1)\n" \
    "v_mfma_f32_16x16x32_bf16 v[0:3], v[80:83], v[64:67], 0\n" \
    "v_exp_f32 v8, v8\n" \
    "v_exp_f32 v9, v9\n" \
    "v_mfma_f32_16x16x32_bf16 v[4:7], v[84:87], v[64:67], 0\n" \
    "ds_read_b128 v[92:95], v124 offset:11264\n" \
    "v_exp_f32 v10, v10\n" \
    "v_exp_f32 v11, v11\n" \
    "v_mfma_f32_16x16x32_bf16 v[32:35], v[104:107], v[116:119], v[32:35]\n" \
    "v_cvt_pk_bf16_f32 v120, v8, v9\n" \
    "v_exp_f32 v12, v12\n" \
    "v_exp_f32 v13, v13\n" \
    "v_mfma_f32_16x16x32_bf16 v[36:39], v[108:111], v[116:119], v[36:39]\n" \
    "v_cvt_pk_bf16_f32 v121, v10, v11\n" \
    "v_exp_f32 v14, v14\n" \
    "v_exp_f32 v15, v15\n" \
    "v_mfma_f32_16x16x32_bf16 v[56:59], v[112:115], v[116:119], v[56:59]\n" \
    "v_cvt_pk_bf16_f32 v122, v12, v13\n" \
    "v_cvt_pk_bf16_f32 v123, v14, v15\n" \
    "v_mfma_f32_16x16x32_bf16 v[8:11], v[80:83], v[68:71], 0\n" \
    "v_exp_f32 v0, v0\n" \
    "v_exp_f32 v1, v1\n" \
    "v_mfma_f32_16x16x32_bf16 v[12:15], v[84:87], v[68:71], 0\n" \
    "ds_read_b128 v[96:99], v124 offset:12288\n" \
    "v_exp_f32 v2, v2\n" \
    "v_exp_f32 v3, v3\n" \
    "v_mfma_f32_16x16x32_bf16 v[40:43], v[104:107], v[120:123], v[40:43]\n" \
    "v_cvt_pk_bf16_f32 v116, v0, v1\n" \
    "v_exp_f32 v4, v4\n" \
    "v_exp_f32 v5, v5\n" \
    "v_mfma_f32_16x16x32_bf16 v[44:47], v[108:111], v[120:123], v[44:47]\n" \
    "v_cvt_pk_bf16_f32 v117, v2, v3\n" \
    "v_exp_f32 v6, v6\n" \
    "v_exp_f32 v7, v7\n" \
    "v_mfma_f32_16x16x32_bf16 v[60:63], v[112:115], v[120:123], v[60:63]\n" \
    "v_cvt_pk_bf16_f32 v118, v4, v5\n" \
    "v_cvt_pk_bf16_f32 v119, v6, v7\n" \
    "s_waitcnt lgkmcnt(1)\n" \
    "v_mfma_f32_16x16x32_bf16 v[0:3], v[80:83], v[72:75], 0\n" \
    "v_exp_f32 v8, v8\n" \
    "v_exp_f32 v9, v9\n" \
    "v_mfma_f32_16x16x32_bf16 v[4:7], v[84:87], v[72:75], 0\n" \
    "ds_read_b128 v[100:103], v124 offset:13312\n" \
    "v_exp_f32 v10, v10\n" \
    "v_exp_f32 v11, v11\n" \
    "v_mfma_f32_16x16x32_bf16 v[16:19], v[88:91], v[116:119], v[16:19]\n" \
    "v_cvt_pk_bf16_f32 v120, v8, v9\n" \
    "v_exp_f32 v12, v12\n" \
    "v_exp_f32 v13, v13\n" \
    "v_mfma_f32_16x16x32_bf16 v[20:23], v[92:95], v[116:119], v[20:23]\n" \
    "v_cvt_pk_bf16_f32 v121, v10, v11\n" \
    "v_exp_f32 v14, v14\n" \
    "v_exp_f32 v15, v15\n" \
    "v_mfma_f32_16x16x32_bf16 v[48:51], v[112:115], v[116:119], v[48:51]\n" \
    "v_cvt_pk_bf16_f32 v122, v12, v13\n" \
    "v_cvt_pk_bf16_f32 v123, v14, v15\n" \
    "v_mfma_f32_16x16x32_bf16 v[8:11], v[80:83], v[76:79], 0\n" \
    "v_exp_f32 v0, v0\n" \
    "v_exp_f32 v1, v1\n" \
    "v_mfma_f32_16x16x32_bf16 v[12:15], v[84:87], v[76:79], 0\n" \
    "ds_read_b128 v[104:107], v124 offset:14336\n" \
    "v_exp_f32 v2, v2\n" \
    "v_exp_f32 v3, v3\n" \
    "v_mfma_f32_16x16x32_bf16 v[24:27], v[88:91], v[120:123], v[24:27]\n" \
    "v_cvt_pk_bf16_f32 v116, v0, v1\n" \
    "v_exp_f32 v4, v4\n" \
    "v_exp_f32 v5, v5\n" \
    "v_mfma_f32_16x16x32_bf16 v[28:31], v[92:95], v[120:123], v[28:31]\n" \
    "v_cvt_pk_bf16_f32 v117, v2, v3\n" \
    "v_exp_f32 v6, v6\n" \
    "v_exp_f32 v7, v7\n" \
    "v_mfma_f32_16x16x32_bf16 v[52:55], v[112:115], v[120:123], v[52:55]\n" \
    "v_cvt_pk_bf16_f32 v118, v4, v5\n" \
    "v_cvt_pk_bf16_f32 v119, v6, v7\n" \
    "s_waitcnt lgkmcnt(1)\n" \
    "v_mfma_f32_16x16x32_bf16 v[0:3], v[96:99], v[64:67], 0\n" \
    "v_exp_f32 v8, v8\n" \
    "v_exp_f32 v9, v9\n" \
    "v_mfma_f32_16x16x32_bf16 v[4:7], v[100:103], v[64:67], 0\n" \
    "ds_read_b128 v[108:111], v124 offset:15360\n" \
    "v_exp_f32 v10, v10\n" \
    "v_exp_f32 v11, v11\n" \
    "v_mfma_f32_16x16x32_bf16 v[32:35], v[88:91], v[116:119], v[32:35]\n" \
    "v_cvt_pk_bf16_f32 v120, v8, v9\n" \
    "v_exp_f32 v12, v12\n" \
    "v_exp_f32 v13, v13\n" \
    "v_mfma_f32_16x16x32_bf16 v[36:39], v[92:95], v[116:119], v[36:39]\n" \
    "v_cvt_pk_bf16_f32 v121, v10, v11\n" \
    "v_exp_f32 v14, v14\n" \
    "v_exp_f32 v15, v15\n" \
    "v_mfma_f32_16x16x32_bf16 v[56:59], v[112:115], v[116:119], v[56:59]\n" \
    "v_cvt_pk_bf16_f32 v122, v12, v13\n" \
    "v_cvt_pk_bf16_f32 v123, v14, v15\n" \
    "s_waitcnt vmcnt(4)\n" \
    "s_barrier\n" \
    "v_mfma_f32_16x16x32_bf16 v[8:11], v[96:99], v[68:71], 0\n" \
    "v_exp_f32 v0, v0\n" \
    "v_exp_f32 v1, v1\n" \
    "v_mfma_f32_16x16x32_bf16 v[12:15], v[100:103], v[68:71], 0\n" \
    "ds_read_b128 v[80:83], v125 offset:0\n" \
    "v_exp_f32 v2, v2\n" \
    "v_exp_f32 v3, v3\n" \
    "v_mfma_f32_16x16x32_bf16 v[40:43], v[88:91], v[120:123], v[40:43]\n" \
    "v_cvt_pk_bf16_f32 v116, v0, v1\n" \
    "v_exp_f32 v4, v4\n" \
    "v_exp_f32 v5, v5\n" \
    "v_mfma_f32_16x16x32_bf16 v[44:47], v[92:95], v[120:123], v[44:47]\n" \
    "s_mov_b32 m0, s20\n" \
    "s_add_u32 s20, s20, 0x400\n" \
    "buffer_load_dwordx4 v126, %[srd], s21 offen lds\n" \
    "s_add_u32 s21, s21, 0x400\n" \
    "v_cvt_pk_bf16_f32 v117, v2, v3\n" \
    "v_exp_f32 v6, v6\n" \
    "v_exp_f32 v7, v7\n" \
    "v_mfma_f32_16x16x32_bf16 v[60:63], v[112:115], v[120:123], v[60:63]\n" \
    "v_cvt_pk_bf16_f32 v118, v4, v5\n" \
    "v_cvt_pk_bf16_f32 v119, v6, v7\n" \
    "s_waitcnt lgkmcnt(1)\n" \
    "v_mfma_f32_16x16x32_bf16 v[0:3], v[96:99], v[72:75], 0\n" \
    "v_exp_f32 v8, v8\n" \
    "v_exp_f32 v9, v9\n" \
    "v_mfma_f32_16x16x32_bf16 v[4:7], v[100:103], v[72:75], 0\n" \
    "ds_read_b128 v[84:87], v125 offset:1024\n" \
    "v_exp_f32 v10, v10\n" \
    "v_exp_f32 v11, v11\n" \
    "v_mfma_f32_16x16x32_bf16 v[16:19], v[104:107], v[116:119], v[16:19]\n" \
    "v_cvt_pk_bf16_f32 v120, v8, v9\n" \
    "v_exp_f32 v12, v12\n" \
    "v_exp_f32 v13, v13\n" \
    "v_mfma_f32_16x16x32_bf16 v[20:23], v[108:111], v[116:119], v[20:23]\n" \
    "v_cvt_pk_bf16_f32 v121, v10, v11\n" \
    "v_exp_f32 v14, v14\n" \
    "v_exp_f32 v15, v15\n" \
    "v_mfma_f32_16x16x32_bf16 v[48:51], v[112:115], v[116:119], v[48:51]\n" \
    "v_cvt_pk_bf16_f32 v122, v12, v13\n" \
    "v_cvt_pk_bf16_f32 v123, v14, v15\n" \
    "v_mfma_f32_16x16x32_bf16 v[8:11], v[96:99], v[76:79], 0\n" \
    "v_exp_f32 v0, v0\n" \
    "v_exp_f32 v1, v1\n" \
    "v_mfma_f32_16x16x32_bf16 v[12:15], v[100:103], v[76:79], 0\n" \
    "ds_read_b128 v[88:91], v125 offset:2048\n" \
    "v_exp_f32 v2, v2\n" \
    "v_exp_f32 v3, v3\n" \
    "v_mfma_f32_16x16x32_bf16 v[24:27], v[104:107], v[120:123], v[24:27]\n" \
    "v_cvt_pk_bf16_f32 v116, v0, v1\n" \
    "v_exp_f32 v4, v4\n" \
    "v_exp_f32 v5, v5\n" \
    "v_mfma_f32_16x16x32_bf16 v[28:31], v[108:111], v[120:123], v[28:31]\n" \
    "s_mov_b32 m0, s20\n" \
    "s_add_u32 s20, s20, 0x400\n" \
    "buffer_load_dwordx4 v126, %[srd], s21 offen lds\n" \
    "s_add_u32 s21, s21, 0x400\n" \
    "v_cvt_pk_bf16_f32 v117, v2, v3\n" \
    "v_exp_f32 v6, v6\n" \
    "v_exp_f32 v7, v7\n" \
    "v_mfma_f32_16x16x32_bf16 v[52:55], v[112:115], v[120:123], v[52:55]\n" \
    "v_cvt_pk_bf16_f32 v118, v4, v5\n" \
    "v_cvt_pk_bf16_f32 v119, v6, v7\n" \
    "s_waitcnt lgkmcnt(1)\n" \
    "v_mfma_f32_16x16x32_bf16 v[0:3], v[80:83], v[64:67], 0\n" \
    "v_exp_f32 v8, v8\n" \
    "v_exp_f32 v9, v9\n" \
    "v_mfma_f32_16x16x32_bf16 v[4:7], v[84:87], v[64:67], 0\n" \
    "ds_read_b128 v[92:95], v125 offset:3072\n" \
    "v_exp_f32 v10, v10\n" \
    "v_exp_f32 v11, v11\n" \
    "v_mfma_f32_16x16x32_bf16 v[32:35], v[104:107], v[116:119], v[32:35]\n" \
    "v_cvt_pk_bf16_f32 v120, v8, v9\n" \
    "v_exp_f32 v12, v12\n" \
    "v_exp_f32 v13, v13\n" \
    "v_mfma_f32_16x16x32_bf16 v[36:39], v[108:111], v[116:119], v[36:39]\n" \
    "v_cvt_pk_bf16_f32 v121, v10, v11\n" \
    "v_exp_f32 v14, v14\n" \
    "v_exp_f32 v15, v15\n" \
    "v_mfma_f32_16x16x32_bf16 v[56:59], v[112:115], v[116:119], v[56:59]\n" \
    "v_cvt_pk_bf16_f32 v122, v12, v13\n" \
    "v_cvt_pk_bf16_f32 v123, v14, v15\n" \
    "v_mov_b32 v124, v125\n" \
    "s_add_u32 s24, s24, 0x4000\n" \
    "s_and_b32 s24, s24, 0xffff\n" \
    "v_add_u32 v125, s24, v127\n" \
    "s_sub_u32 s23, s23, 1\n" \
    "s_cmp_lg_u32 s23, 0\n" \
    "s_cbranch_scc1 ATT_LOOP_%=\n" \
    "v_mfma_f32_16x16x32_bf16 v[40:43], v[104:107], v[120:123], v[40:43]\n" \
    "v_mfma_f32_16x16x32_bf16 v[44:47], v[108:111], v[120:123], v[44:47]\n" \
    "v_mfma_f32_16x16x32_bf16 v[60:63], v[112:115], v[120:123], v[60:63]\n" \
    "s_waitcnt vmcnt(0) lgkmcnt(0)\n" \
    "s_nop 15\n"

constexpr int Q_NS = 3, Q_SLOT = 24576;
__device__ __forceinline__ void attention(int b, const char* x, const char* wq, char* ws, lds_ptr lds, int w, int lane) {
    const int xc = b & 7, t = b >> 3, st128 = (xc >> 1) * 4 + (t & 3), hp = (xc & 1) * 8 + (t >> 2);
    const int head = 2 * hp + (w & 1), s0 = st128 * 128 + (w >> 1) * 64, g = lane >> 4, c = lane & 15;
    const u32x4 srd = make_srd(ws + WS_KV, S * 128);
    const u32 voff = w * 4096 + lane * 16, ldst = (u32)(uintptr_t)lds + w * 4096;
    auto dma = [&](int st, int p0, int p1) {
#pragma unroll
        for (int p = p0; p < p1; ++p) dma16(ldst + ((st + 2) & 3) * KV_STAGE + p * 1024, voff, srd, st * KV_STAGE + p * 1024);
    };
    __builtin_amdgcn_s_setprio(Q_PRIO);
    float gate[4];
#pragma unroll
    for (int i = 0; i < 4; ++i) asm volatile("global_load_dword %0, %1, off" : "=v"(gate[i]) : "v"(ws + WS_G + (s0 + i * 16 + c) * 4) : "memory");
    f32x4 kb2;
    asm volatile("global_load_dwordx4 %0, %1, off" : "=v"(kb2) : "v"(ws + WS_BND + lane * 16) : "memory");
    u32x4 q[4];
    {
        OpTr<64, D * 2, 2, true, 2> a; a.init(wq, D * D * 2, hp * 64, 0, lds, w, (w & 1) * 2, lane);
        OpRow bb; bb.init(x, S * D * 2, st128 * 128, lds + a.BYTES, w, (w >> 1) * 64, lane);
        Engine<Q_NS, 2, 4, 2> e;
        static_assert((D / 64 - 1) % Q_NS == 0 && Q_SLOT <= 2 * KV_STAGE, "the last Q slot lies below the first two K/V stages");
        gemm_run<Q_SLOT, D / 64, 2>(e, a, bb, [] {}, [](int) {}, [&](auto p) { if constexpr (p.v == Rounds<Q_NS, D / 64>::TAIL - 2) { dma(0, 0, 4); dma(1, 0, 4); } });
        wait_bar<4>();
        dma(2, 0, 2);
#pragma unroll
        for (int i = 0; i < 4; ++i) q[i] = pack8(e.acc[0][i] * QSCALE, e.acc[1][i] * QSCALE);
    }
    PIN(kb2);
    const float k2 = wave_max(fmaxf(fmaxf(kb2[0], kb2[1]), fmaxf(kb2[2], kb2[3])));
    bool ok = true;
#pragma unroll
    for (int i = 0; i < 4; ++i) {
        float n = 0.f;
#pragma unroll
        for (int m = 0; m < 4; ++m) { const float a = bf_lo(q[i][m]), h = bf_hi(q[i][m]); n += a * a + h * h; }
        ok = ok && group_sum(n) * k2 * 1.02f <= SAFE_LOG2 * SAFE_LOG2;
        PIN(gate[i]);
    }
    const bool safe = __all(ok);
    __builtin_amdgcn_s_setprio(ATT_PRIO);
    f32x4 o[8];
#pragma unroll
    for (int i = 0; i < 8; ++i) o[i] = f32x4{};
    float lsum[4];
    const lds_ptr base = lds + lane * 16;
    const f32x4 z = {};
    STAMP(1, 1);
    if (safe) {
        const u32 one = c == 0 ? 0x3f803f80u : 0u;
        const u32x4 ones = {one, one, one, one};
        const lds_ptr first = base + 2 * KV_STAGE;
        u32x4 ka = lds_rd128(first), kb = lds_rd128(first + 1024), v0 = lds_rd128(first + 2048), v1 = lds_rd128(first + 3072);
        f32x4 sa = M16(ka, q[0], z), sb = M16(kb, q[0], z);
        f32x16 oa = {}, ob = {}, l = {};
        u32x4 vz0 = {}, vz1 = {}, pz = {};
        u32 cur = (u32)(uintptr_t)first, nxt = cur + KV_STAGE;
        asm volatile("s_nop 7\n" ATT_LOOP_ASM
                     : "+{v[0:3]}"(sa), "+{v[4:7]}"(sb), "+{v[16:31]}"(oa), "+{v[32:47]}"(ob), "+{v[48:63]}"(l), "+{v[80:83]}"(ka), "+{v[84:87]}"(kb),
                       "+{v[88:91]}"(v0), "+{v[92:95]}"(v1), "+{v[104:107]}"(vz0), "+{v[108:111]}"(vz1), "+{v[120:123]}"(pz), "+{v124}"(cur), "+{v125}"(nxt)
                     : "{v[64:67]}"(q[0]), "{v[68:71]}"(q[1]), "{v[72:75]}"(q[2]), "{v[76:79]}"(q[3]), "{v[112:115]}"(ones), "{v126}"(voff),
                       "{v127}"((u32)(uintptr_t)base), [srd] "s"(srd), [ldst] "s"(ldst)
                     : "v8", "v9", "v10", "v11", "v12", "v13", "v14", "v15", "v96", "v97", "v98", "v99", "v100", "v101", "v102", "v103",
                       "v116", "v117", "v118", "v119", "s20", "s21", "s22", "s23", "s24", "scc", "memory");
#pragma unroll
        for (int i = 0; i < 4; ++i) {
            lsum[i] = __shfl(l[4 * i], c);
            o[i] = f32x4{oa[4 * i], oa[4 * i + 1], oa[4 * i + 2], oa[4 * i + 3]};
            o[4 + i] = f32x4{ob[4 * i], ob[4 * i + 1], ob[4 * i + 2], ob[4 * i + 3]};
        }
    } else {
        float mx[4];
#pragma unroll
        for (int i = 0; i < 4; ++i) { mx[i] = -__builtin_inff(); lsum[i] = 0.f; }
        dma(2, 2, 4);
        for (int st = 0; st < KV_NSTAGE; ++st) {
            wait_bar<4>();
            dma(st + 3, 0, 4);
#pragma unroll 1
            for (int kb = 0; kb < 4; ++kb) {
                const lds_ptr p = base + ((st + 2) & 3) * KV_STAGE + kb * 4096;
                const u32x4 k0 = lds_rd128(p), k1 = lds_rd128(p + 1024), v0 = lds_rd128(p + 2048), v1 = lds_rd128(p + 3072);
#pragma unroll
                for (int i = 0; i < 4; ++i) {
                    f32x4 sa = M16(k0, q[i], z), sb = M16(k1, q[i], z);
                    float m = mx[i];
#pragma unroll
                    for (int r = 0; r < 4; ++r) m = fmaxf(m, fmaxf(sa[r], sb[r]));
                    m = fmaxf(m, __shfl_xor(m, 16)); m = fmaxf(m, __shfl_xor(m, 32));
                    const float alpha = EX2(mx[i] - m);
                    mx[i] = m;
                    float t = 0.f;
#pragma unroll
                    for (int r = 0; r < 4; ++r) { sa[r] = EX2(sa[r] - m); sb[r] = EX2(sb[r] - m); t += sa[r] + sb[r]; }
                    lsum[i] = lsum[i] * alpha + t;
                    const u32x4 pp = pack8(sa, sb);
                    o[2 * i] = M16(v0, pp, o[2 * i] * alpha);
                    o[2 * i + 1] = M16(v1, pp, o[2 * i + 1] * alpha);
                }
            }
        }
        asm volatile("s_waitcnt vmcnt(0)" ::: "memory");
#pragma unroll
        for (int i = 0; i < 4; ++i) lsum[i] = group_sum(lsum[i]);
    }
    STAMP(1, 6);
#pragma unroll
    for (int i = 0; i < 4; ++i) {
        const float r = gate[i] / lsum[i];
        store_wt(ws + WS_OF, S * D * 2, (u32)((head * 4 + g) * S + s0 + i * 16 + c) * 16u, pack8(o[2 * i] * r, o[2 * i + 1] * r));
    }
    publish(ws, CNT_O + st128);
}

__device__ __forceinline__ f32x2 gelu_tanh(f32x2 x) {
    constexpr float C0 = -2.f * 0.7978845608028654f * 1.4426950408889634f, C1 = C0 * 0.044715f;
    const f32x2 y = x * __builtin_elementwise_fma(x * x, f32x2{C1, C1}, f32x2{C0, C0});
    const f32x2 d = f32x2{EX2(y[0]), EX2(y[1])} + 1.f;
    return x * f32x2{__builtin_amdgcn_rcpf(d[0]), __builtin_amdgcn_rcpf(d[1])};
}

__device__ __forceinline__ void gemm1_tile(int b, const void* w1, const char* b1, char* ws, lds_ptr lds, int w, int lane) {
    const int x = b & 7, t = b >> 3;
    const int dt = (x & 1) * 4 + (t & 3), d0 = dt * 128, f0 = ((x >> 1) * 7 + (t >> 2)) * 128;
    const int g = lane >> 4, c = lane & 15;
    u32x2 bias[8];
#pragma unroll
    for (int j = 0; j < 8; ++j) bias[j] = *(const u32x2*)(b1 + (f0 + j * 16 + g * 4) * 2);
    OpReg<2, D, 1, false> a; a.init(ws + WS_HF, S * D * 2, d0 + w * 32, 0, lane);
    OpTr<128, F * 2, 1, false, 8> bb; bb.init(w1, S * F * 2, f0, 0, lds, w, 0, lane);
    Engine<G1_NS, 2, 8> e;
    const u32 scratch = (u32)(uintptr_t)lds + G1_NS * 8192 + w * 256;
    gemm_run<8192, S / 32, 0>(e, a, bb, [] {}, [&](int r) { if (r == W1_ROUND) touch<S * F * 2 - W1_PRE, MID_G1, 256>((const char*)w1 + W1_PRE, b, scratch); }, [](auto) {});
    auto& acc = e.acc;
    STAMP(1, 1);
#pragma unroll
    for (int jp = 0; jp < 4; ++jp)
#pragma unroll
        for (int i = 0; i < 2; ++i) {
            f32x4 u[2];
#pragma unroll
            for (int h = 0; h < 2; ++h) {
                const u32x2 bv = bias[2 * jp + h];
                const f32x4 v = acc[i][2 * jp + h] + f32x4{bf_lo(bv[0]), bf_hi(bv[0]), bf_lo(bv[1]), bf_hi(bv[1])};
                const f32x2 lo = gelu_tanh(f32x2{v[0], v[1]}), hi = gelu_tanh(f32x2{v[2], v[3]});
                u[h] = f32x4{lo[0], lo[1], hi[0], hi[1]};
            }
            store_wt(ws + WS_UF, F * D * 2, (u32)(((f0 / 32 + jp) * 4 + g) * D + d0 + w * 32 + i * 16 + c) * 16u, pack8(u[0], u[1]));
        }
    publish(ws, CNT_U + dt * 2 + (x >> 2));
}

constexpr int P1_NS = 8, P1_BASE = 8192, P2_NS = 6, P2_SLOT = 12288;

__device__ __forceinline__ void post_tile(int b, const char* w2, const char* b2, const char* wo, char* ws, char* out, lds_ptr lds, int w, int lane) {
    const int x = b & 7, t = b >> 3, h = t & 1;
    const int s0 = ((x >> 1) * 8 + (t & 7)) * 64, dt = (x & 1) * 4 + (t >> 3), d0 = dt * 128;
    const int g = lane >> 4, c = lane & 15;
    constexpr int N1 = F / 64, N2 = D / 32;
    static_assert(N1 % P1_NS == 0 && P1_BASE + (P1_NS - 1) * 8192 >= (P2_NS - 1) * P2_SLOT, "the last w2 slot lies in the last wo slot");
    float omg[4], bs[4];
#pragma unroll
    for (int j = 0; j < 4; ++j) {
        const int s = s0 + j * 16 + c;
        asm volatile("global_load_dword %0, %1, off" : "=v"(omg[j]) : "v"(ws + WS_G + s * 4) : "memory");
        asm volatile("global_load_ushort %0, %1, off" : "=v"(bs[j]) : "v"(b2 + s * 2) : "memory");
    }
    OpTr<128, D * 2, 1, false, 2> a2; a2.init(wo, D * D * 2, d0, 0, lds, w, w * 2, lane);
    OpFrag<64, S, 1> b2o; b2o.init(ws + WS_OF, S * D * 2, s0, 0, lds + 8192, w, 0, lane);
    Engine<P2_NS, 2, 4> e;
    auto& acc = e.acc;
    u32 seen = 0;
    {
        OpReg<2, D, 1, !PLAIN_U> a1; a1.init(ws + WS_UF, F * D * 2, d0 + w * 32, h * N1, lane);
        OpTr<128, S * 2, 1, false, 8> b1; b1.init(w2, F * S * 2, s0 & ~127, h * N1, lds + P1_BASE, w, 4 * h, lane);
        Engine<P1_NS, 2, 8> e1;
        constexpr int TAIL = Rounds<P1_NS, N1>::TAIL;
        gemm_run<8192, N1, 2>(e1, a1, b1,
            [&] { if (w == 0) { await(ws, CNT_U + dt * 2 + h, F / 256); if (PLAIN_U) { acquire(); asm volatile("s_waitcnt vmcnt(0)" ::: "memory"); } } __builtin_amdgcn_s_barrier(); STAMP(1, 3); },
            [&](int r) { if (r == PRIO_SWITCH) __builtin_amdgcn_s_setprio(1); if (r == Rounds<P1_NS, N1>::N - 1) sample(seen, ws, CNT_O + (s0 >> 7)); },
            [&](auto p) { if constexpr (p.v == TAIL - 2) { confirm(seen, ws, CNT_O + (s0 >> 7), 16); if (PLAIN_O) acquire(); STAMP(1, 6); gemm_fill<P2_SLOT>(a2, b2o, 0, P2_NS - 1); } });
        STAMP(1, 4);
#pragma unroll
        for (int j = 0; j < 4; ++j) {
            PIN(omg[j]); PIN(bs[j]);
            omg[j] = bf_round(1.f - omg[j]); bs[j] = __uint_as_float(__float_as_uint(bs[j]) << 16);
#pragma unroll
            for (int i = 0; i < 2; ++i) {
                store_wt(ws + WS_X, 256u << 15, (u32)(((b * 4 + w) * 8 + i * 4 + j) * 64 + lane) * 16u, __builtin_bit_cast(u32x4, e1.acc[i][4 + j]));
                acc[i][j] = (e1.acc[i][j] + bs[j]) * omg[j];
            }
        }
    }
    constexpr int PPW = 3, XW = 8;
    wait_bar<(P2_NS - 2) * PPW + XW>();
    gemm_fill<P2_SLOT>(a2, b2o, P2_NS - 1, P2_NS);
    gemm_first(e, a2, b2o);
    gemm_steps<0, P2_NS, MANY, P2_SLOT, 2, XW>(e, a2, b2o, P2_NS, [&](auto p) { if constexpr (p.v == P2_NS - 2) signal(ws, CNT_X + b); });
    for (int r = 1; r < 3; ++r) gemm_steps<0, P2_NS, MANY, P2_SLOT, 2, 0>(e, a2, b2o, (r + 1) * P2_NS, [](auto) {});
    seen = 0;
    u32x4 got[8];
    const u32x4 xsrd = make_srd(ws + WS_X, 256u << 15);
    gemm_steps<0, N2 - 3 * P2_NS, N2 - 3 * P2_NS, P2_SLOT, 2, 0>(e, a2, b2o, 4 * P2_NS, [&](auto p) {
        if constexpr (p.v == 0) sample(seen, ws, CNT_X + (b ^ 8));
        if constexpr (p.v == P2_NS - 2) {
            confirm(seen, ws, CNT_X + (b ^ 8), 1);
#pragma unroll
            for (int i = 0; i < 8; ++i) {
                got[i] = u32x4{};
                load_coh(got[i], lane * 16, xsrd, (u32)(((b ^ 8) * 4 + w) * 8 + i) * 1024u);
            }
        }
    });
    asm volatile("s_waitcnt vmcnt(0)" ::: "memory");
#pragma unroll
    for (int i = 0; i < 8; ++i) { PIN(got[i]); acc[i >> 2][i & 3] += __builtin_bit_cast(f32x4, got[i]) * omg[i & 3]; }
    STAMP(1, 5);
#pragma unroll
    for (int j = 0; j < 4; ++j) {
        const auto r0 = __builtin_amdgcn_permlane16_swap(cvtpk(acc[0][j][0], acc[0][j][1]), cvtpk(acc[1][j][0], acc[1][j][1]), false, false);
        const auto r1 = __builtin_amdgcn_permlane16_swap(cvtpk(acc[0][j][2], acc[0][j][3]), cvtpk(acc[1][j][2], acc[1][j][3]), false, false);
        const int s = s0 + j * 16 + c, d = d0 + w * 32 + (g & 1) * 16 + (g >> 1) * 8;
        *(u32x4*)(out + ((size_t)s * D + d) * 2) = u32x4{r0[0], r1[0], r0[1], r1[1]};
    }
}

constexpr int MAIN_LDS = Q_NS * Q_SLOT;
static_assert(MAIN_LDS >= P2_NS * P2_SLOT && MAIN_LDS >= P1_BASE + P1_NS * 8192 && MAIN_LDS >= 4 * KV_STAGE && 2 * MAIN_LDS <= 160 * 1024, "two workgroups per CU");

__global__ void __launch_bounds__(256, 2) k_main(const char* __restrict__ x, const char* __restrict__ wq, const char* __restrict__ w1, const char* __restrict__ b1, const char* __restrict__ w2, const char* __restrict__ b2,
                                                 const char* __restrict__ wo, char* __restrict__ ws, char* __restrict__ out) {
    extern __shared__ __attribute__((aligned(16))) char smem[];
    const int lane = threadIdx.x & 63, w = __builtin_amdgcn_readfirstlane(threadIdx.x >> 6), b = blockIdx.x;
    asm volatile("main_code_begin:");
    STAMP(1, 0);
    if (b < MID_ATT) {
        attention(b, x, wq, ws, (lds_ptr)smem, w, lane);
    } else {
        const int gb = b - MID_ATT;
        __builtin_amdgcn_s_setprio(G1_PRIO);
        if (gb < MID_G1) gemm1_tile(gb, w1, b1, ws, (lds_ptr)smem, w, lane);
        else {
            const u32 scratch = (u32)(uintptr_t)(lds_ptr)smem + w * 256;
            for (int i = 0; i < PF_SLEEP; ++i) __builtin_amdgcn_s_sleep(64);
            for (int i = 0; i < 4; ++i) {
                touch<F * S * 2, MID_PF * 4, 256>(w2, (gb - MID_G1) * 4 + i, scratch);
                asm volatile("s_waitcnt vmcnt(0)" ::: "memory");
            }
            touch<D * D * 2, MID_PF, 256>(wo, gb - MID_G1, scratch);
            wait_bar<0>();
        }
        STAMP(1, 2);
        __builtin_amdgcn_s_setprio(POST_PRIO);
        post_tile(gb, w2, b2, wo, ws, out, (lds_ptr)smem, w, lane);
    }
    STAMP(1, 7);
    asm volatile("main_code_end:");
}

extern "C" void kernel_launch(void* const* d_in, const int* in_sizes, int n_in, void* d_out, int out_size, void* d_ws, size_t ws_size,
                              hipStream_t stream) {
    const char* x = (const char*)d_in[0];
    const char *wq = (const char*)d_in[1], *wk = (const char*)d_in[2], *wv = (const char*)d_in[3], *wo = (const char*)d_in[4];
    const char *w1 = (const char*)d_in[6], *b1 = (const char*)d_in[7], *w2 = (const char*)d_in[8], *b2 = (const char*)d_in[9];
    const char* gw = (const char*)d_in[10];
    char* ws = (char*)d_ws;
    (void)hipFuncSetAttribute((const void*)k_pre, hipFuncAttributeMaxDynamicSharedMemorySize, PRE_LDS);
    (void)hipFuncSetAttribute((const void*)k_main, hipFuncAttributeMaxDynamicSharedMemorySize, MAIN_LDS);
    k_pre<<<PRE_KV + PRE_LN, 512, PRE_LDS, stream>>>(x, wq, wk, wv, gw, w1, ws);
    k_main<<<MID_ATT + MID_G1 + MID_PF, 256, MAIN_LDS, stream>>>(x, wq, w1, b1, w2, b2, wo, ws, (char*)d_out);
}
```

```cpp
#include <hip/hip_runtime.h>
#include <stdint.h>

typedef unsigned u32;
typedef u32 u32x4 __attribute__((ext_vector_type(4)));
typedef u32 u32x2 __attribute__((ext_vector_type(2)));
typedef float f32x2 __attribute__((ext_vector_type(2)));
typedef float f32x4 __attribute__((ext_vector_type(4)));
typedef float f32x16 __attribute__((ext_vector_type(16)));
typedef __bf16 bf16x2 __attribute__((ext_vector_type(2)));
typedef __bf16 bf16x8 __attribute__((ext_vector_type(8)));
typedef short s16x4 __attribute__((ext_vector_type(4)));
#define LDS __attribute__((address_space(3)))
typedef LDS char* lds_ptr;

constexpr int S = 2048, D = 1024, F = 3584;
constexpr u32 WS_HF = 0, WS_OF = 8u << 20, WS_UF = 12u << 20, WS_KV = 20u << 20;
constexpr u32 WS_G = WS_KV + (256u << 10), WS_BND = WS_G + (8u << 10), WS_CNT = WS_BND + (4u << 10);
constexpr u32 WS_X = 24u << 20;
constexpr int CNT_U = 0, CNT_O = 16, CNT_X = 32, CNT_N = 288, CNT_STRIDE = 256;
constexpr float QSCALE = 0.17677669529663687f * 1.4426950408889634f;
constexpr float SAFE_LOG2 = 60.f;
constexpr float SAFE_V = 1.0e12f;

#define PLAIN_U 0
#define PLAIN_O 0
#define CODE_PF 8
#define W1_ROWS 1024
#define W1_ROUND 1
#define HF_WT 2
#define SB() __builtin_amdgcn_sched_barrier(0)
#define PIN(x) asm volatile("" : "+v"(x))
#define M16(a, b, c) __builtin_amdgcn_mfma_f32_16x16x32_bf16(__builtin_bit_cast(bf16x8, a), __builtin_bit_cast(bf16x8, b), c, 0, 0, 0)
#define M32(a, b, c) __builtin_amdgcn_mfma_f32_32x32x16_bf16(__builtin_bit_cast(bf16x8, a), __builtin_bit_cast(bf16x8, b), c, 0, 0, 0)
__device__ __forceinline__ void mma16(f32x4& c, const u32x4& a, const u32x4& b) { asm volatile("v_mfma_f32_16x16x32_bf16 %0, %1, %2, %0" : "+v"(c) : "v"(a), "v"(b)); }
__device__ __forceinline__ void mfma_settle() { asm volatile("s_nop 7\n\ts_nop 1" ::: "memory"); SB(); }
#define EX2(x) __builtin_amdgcn_exp2f(x)

__device__ __forceinline__ u32 cvtpk(float lo, float hi) { f32x2 v = {lo, hi}; return __builtin_bit_cast(u32, __builtin_convertvector(v, bf16x2)); }
__device__ __forceinline__ float bf_lo(u32 v) { return __uint_as_float(v << 16); }
__device__ __forceinline__ float bf_hi(u32 v) { return __uint_as_float(v & 0xffff0000u); }
__device__ __forceinline__ float bf_round(float x) { return bf_lo(cvtpk(x, x)); }
__device__ __forceinline__ u32x4 pack8(const f32x4& a, const f32x4& b) { return u32x4{cvtpk(a[0], a[1]), cvtpk(a[2], a[3]), cvtpk(b[0], b[1]), cvtpk(b[2], b[3])}; }

__device__ __forceinline__ u32x4 make_srd(const void* p, u32 bytes) {
    const uint64_t a = (uint64_t)p;
    return u32x4{(u32)a, (u32)(a >> 32) & 0xffffu, bytes, 0x00020000u};
}
__device__ __forceinline__ void dma16(u32 lds, u32 voff, const u32x4& srd, u32 soff) {
    asm volatile("s_mov_b32 m0, %0\n\ts_nop 0\n\tbuffer_load_dwordx4 %1, %2, %3 offen lds" ::"s"(lds), "v"(voff), "s"(srd), "s"(soff) : "memory");
}
__device__ __forceinline__ void dma16_coh(u32 lds, u32 voff, const u32x4& srd, u32 soff) {
    asm volatile("s_mov_b32 m0, %0\n\ts_nop 0\n\tbuffer_load_dwordx4 %1, %2, %3 offen sc1 lds" ::"s"(lds), "v"(voff), "s"(srd), "s"(soff) : "memory");
}
template <int N> __device__ __forceinline__ void wait_bar() {
    __builtin_amdgcn_s_waitcnt(0xc07f);
    asm volatile("s_waitcnt vmcnt(%0)\n\ts_barrier" ::"n"(N) : "memory");
}
__device__ __forceinline__ u32x4 lds_rd128(lds_ptr p) { return *(LDS const u32x4*)p; }
__device__ __forceinline__ u32x2 lds_rdtr(lds_ptr p) { return __builtin_bit_cast(u32x2, __builtin_amdgcn_ds_read_tr16_b64_v4i16((LDS s16x4*)p)); }

typedef __attribute__((address_space(1))) u32 gu32;
__device__ __forceinline__ void store_wt(char* base, u32 bytes, u32 off, const u32x4& v) {
    __builtin_amdgcn_raw_buffer_store_b128(v, __builtin_amdgcn_make_buffer_rsrc(base, 0, bytes, 0x00020000), off, 0, 16);
}
__device__ __forceinline__ void load_coh(u32x4& f, u32 voff, const u32x4& srd, u32 soff) {
    asm volatile("buffer_load_dwordx4 %0, %1, %2, %3 offen sc1" : "+v"(f) : "v"(voff), "s"(srd), "s"(soff) : "memory");
}
__device__ __forceinline__ void signal(char* ws, int cnt) {
    if (threadIdx.x == 0) __hip_atomic_fetch_add((gu32*)(ws + WS_CNT + cnt * CNT_STRIDE), 1u, __ATOMIC_RELAXED, __HIP_MEMORY_SCOPE_AGENT);
}
__device__ __forceinline__ void publish(char* ws, int cnt) {
    asm volatile("s_waitcnt vmcnt(0)\n\ts_barrier" ::: "memory");
    signal(ws, cnt);
}
__device__ __forceinline__ void await(char* ws, int cnt, u32 target) {
    gu32* p = (gu32*)(ws + WS_CNT + cnt * CNT_STRIDE);
    for (int spins = 0; spins < (1 << 20); ++spins) {
        if (__builtin_amdgcn_readfirstlane(__hip_atomic_load(p, __ATOMIC_RELAXED, __HIP_MEMORY_SCOPE_AGENT)) >= target) break;
        __builtin_amdgcn_s_sleep(1);
    }
    asm volatile("" ::: "memory");
}
__device__ __forceinline__ void acquire() { asm volatile("buffer_inv sc1" ::: "memory"); }

__device__ __forceinline__ void sample(u32& seen, char* ws, int cnt) {
    asm volatile("global_load_dword %0, %1, off sc1" : "+v"(seen) : "v"(ws + WS_CNT + cnt * CNT_STRIDE) : "memory");
}
__device__ __forceinline__ void confirm(u32& seen, char* ws, int cnt, u32 target) {
    PIN(seen);
    if (__builtin_amdgcn_readfirstlane(seen) < target) await(ws, cnt, target);
}

#define STAMP(k, i)

template <int R, int NR, int KSTR> struct OpFrag {
    static constexpr int BYTES = 4 * R * 16, PPW = BYTES / 4096, RPF = 1;
    static constexpr bool REG = false;
    u32x4 srd; u32 voff, ldst; lds_ptr rd;
    __device__ __forceinline__ void init(const void* base, u32 bytes, int row0, int k0, lds_ptr img, int w, int wrow, int lane) {
        srd = make_srd(base, bytes);
        voff = (u32)(row0 + lane + (w + 4 * k0) * NR) * 16u;
        ldst = (u32)(uintptr_t)img + w * (R * 16);
        rd = img + ((lane >> 4) * R + wrow + (lane & 15)) * 16;
    }
    __device__ __forceinline__ void issue(int slot, u32 pos, int i) const { (PLAIN_O ? dma16 : dma16_coh)(ldst + pos + i * 1024, voff, srd, (u32)slot * (KSTR * 4u * NR * 16u) + i * 1024u); }
    __device__ __forceinline__ void read(u32x4& f, int pos, int i, int) const { f = lds_rd128(rd + pos + i * 256); }
};

template <int C, int RS, int KS, bool NAT, int NF> struct OpTr {
    static constexpr int BYTES = KS * 32 * C * 2, PPW = BYTES / 4096, RPF = 2;
    static constexpr bool REG = false;
    u32x4 srd; u32 voff, ldst; lds_ptr rd[NF];
    __device__ __forceinline__ void init(const void* base, u32 bytes, int col0, int k0, lds_ptr img, int w, int wch, int lane) {
        srd = make_srd(base, bytes);
        ldst = (u32)(uintptr_t)img + w * 1024;
        const int g = lane >> 4, q = (lane >> 2) & 3, p = lane & 3;
        if constexpr (C == 128) {
            const int f = NAT ? (g | (((w >> 1) & 1) << 2)) : (4 * (w & 1) + g);
            voff = (u32)(32 * k0 + 4 * w + g) * RS + col0 * 2 + ((((lane & 15) >> 1) ^ f) * 32) + (lane & 1) * 16;
            const int row = NAT ? 8 * g + q : 4 * g + q, fr = NAT ? (q | ((g & 1) << 2)) : (row & 7);
#pragma unroll
            for (int i = 0; i < NF; ++i) rd[i] = img + row * 256 + (((wch ^ i) ^ fr) * 32) + p * 8;
        } else {
            static_assert(C == 128 || C == 64, "unsupported transposed operand");
            const int f = NAT ? ((g & 1) | ((w & 1) << 1)) : g;
            voff = (u32)(32 * k0 + 8 * w + (lane >> 3)) * RS + col0 * 2 + ((((lane & 7) >> 1) ^ f) * 32) + (lane & 1) * 16;
            const int row = NAT ? 8 * g + q : 4 * g + q, fr = NAT ? ((q >> 1) | ((g & 1) << 1)) : ((row >> 1) & 3);
#pragma unroll
            for (int i = 0; i < NF; ++i) rd[i] = img + row * 128 + (((wch ^ i) ^ fr) * 32) + p * 8;
        }
    }
    __device__ __forceinline__ void issue(int slot, u32 pos, int i) const { dma16(ldst + pos + i * 4096, voff, srd, (u32)(slot * KS * 32 + i * (2048 / C)) * RS); }
    __device__ __forceinline__ void read(u32x4& f, int pos, int i, int t) const {
        const u32x2 v = lds_rdtr(rd[i % NF] + pos + i / NF * (32 * C * 2) + t * ((NAT ? 4 : 16) * C * 2));
        f[2 * t] = v[0]; f[2 * t + 1] = v[1];
    }
};

struct OpRow {
    static constexpr int BYTES = 128 * 128, PPW = 4, RPF = 1;
    static constexpr bool REG = false;
    u32x4 srd; u32 voff, ldst; lds_ptr rd[2];
    __device__ __forceinline__ void init(const void* base, u32 bytes, int row0, lds_ptr img, int w, int wrow, int lane) {
        srd = make_srd(base, bytes);
        ldst = (u32)(uintptr_t)img + w * 1024;
        const int g = lane >> 4, c = lane & 15;
        voff = (u32)(row0 + 8 * w + (lane >> 3)) * (D * 2) + (((lane & 7) ^ (4 * (w & 1) + g)) * 16);
#pragma unroll
        for (int ks = 0; ks < 2; ++ks) rd[ks] = img + (wrow + c) * 128 + (((4 * ks + g) ^ (c >> 1)) * 16);
    }
    __device__ __forceinline__ void issue(int slot, u32 pos, int i) const { dma16(ldst + pos + i * 4096, voff, srd, (u32)slot * 128u + i * (32u * D * 2u)); }
    __device__ __forceinline__ void read(u32x4& f, int pos, int i, int) const { f = lds_rd128(rd[i >> 2] + pos + (i & 3) * 2048); }
};

template <int NF, int NR, int KSTR, bool COH> struct OpReg {
    static constexpr int PPW = NF, RPF = 0;
    static constexpr bool REG = true;
    u32x4 srd; u32 voff;
    __device__ __forceinline__ void init(const void* base, u32 bytes, int row0, int k0, int lane) {
        srd = make_srd(base, bytes);
        voff = (u32)(row0 + (lane & 15) + ((lane >> 4) + 4 * k0) * NR) * 16u;
    }
    __device__ __forceinline__ void load(u32x4& f, int slot, int i) const {
        const u32 soff = (u32)slot * (KSTR * 4u * NR * 16u);
        if (COH) asm volatile("buffer_load_dwordx4 %0, %1, %2, %3 offen offset:%4 sc1" : "+v"(f) : "v"(voff), "s"(srd), "s"(soff), "n"(i * 256) : "memory");
        else asm volatile("buffer_load_dwordx4 %0, %1, %2, %3 offen offset:%4" : "+v"(f) : "v"(voff), "s"(srd), "s"(soff), "n"(i * 256) : "memory");
    }
};

template <int NA, int NB> struct Frags { u32x4 a[NA], b[NB]; };
template <int NS, int NA, int NB, int KS = 1> struct Engine { f32x4 acc[NA][NB]; Frags<NA * KS, NB * KS> f[2]; u32x4 ring[NS][NA]; };

#define STAG 1
#define RDX 2
template <int KS, int SWAP, bool READ, bool ISSUE, bool RISSUE, int NA, int NB, class A, class B>
__device__ __forceinline__ void gemm_step(f32x4 (&acc)[NA][NB], const Frags<NA * KS, NB * KS>& cur, Frags<NA * KS, NB * KS>& nxt, u32x4 (&ring)[NA],
                                          u32x4 (&prev)[NA], const A& a, const B& b, int rpos, int islot, u32 ipos) {
    constexpr int NM = NA * NB * KS, NRA = NA * A::RPF, NRK = NRA + NB * B::RPF, NRD = NRK * KS, RPM = (RDX * NRD + NM - 1) / NM;
    constexpr int ND = A::PPW + B::PPW, DSTEP = NM / ND, DOFF = STAG % DSTEP;
    if constexpr (A::REG) {
#pragma unroll
        for (int i = 0; i < NA; ++i) PIN(ring[i]);
    }
#pragma unroll
    for (int m = 0; m < NM; ++m) {
        const int i = m % NA, j = m / NA % NB, ks = m / (NA * NB);
        const u32x4 av = A::REG ? ring[i] : cur.a[ks * NA + i];
        if (i >= SWAP) mma16(acc[i][j], cur.b[ks * NB + j], av);
        else mma16(acc[i][j], av, cur.b[ks * NB + j]);
        if constexpr (READ) {
#pragma unroll
            for (int rr = m * RPM; rr < (m + 1) * RPM && rr < NRD; ++rr) {
                const int k = rr / NRK, r = rr % NRK;
                if (r < B::RPF) b.read(nxt.b[k * NB], rpos, k * NB, r);
                else if (r >= B::RPF + NRA) b.read(nxt.b[k * NB + (r - NRA) / B::RPF], rpos, k * NB + (r - NRA) / B::RPF, (r - NRA) % B::RPF);
                else if constexpr (!A::REG) a.read(nxt.a[k * NA + (r - B::RPF) / A::RPF], rpos, k * NA + (r - B::RPF) / A::RPF, (r - B::RPF) % A::RPF);
            }
        }
        if (m % DSTEP == DOFF && m / DSTEP < ND) {
            const int d = m / DSTEP;
            if (d < B::PPW) { if constexpr (ISSUE) b.issue(islot, ipos, d); }
            else if constexpr (A::REG) { if constexpr (RISSUE) a.load(prev[d - B::PPW], islot - 1, d - B::PPW); }
            else if constexpr (ISSUE) a.issue(islot, ipos, d - B::PPW);
        }
        SB();
    }
}

template <int V> struct IC { static constexpr int v = V; };
template <int NS, int PA, int PB, bool REG> constexpr int younger(int rem) {
    if (rem == 2) return 0;
    int n = 0;
    for (int k = 1; k <= NS - 2; ++k) n += (rem + k > NS ? PB + (REG ? 0 : PA) : 0) + (REG && rem + k > NS - 1 ? PA : 0);
    return n;
}
template <int P, int COUNT, int REM, int SLOTB, int SWAP, int XW, int NS, int NA, int NB, int KS, class A, class B, class Hook>
__device__ __forceinline__ void gemm_steps(Engine<NS, NA, NB, KS>& e, const A& a, const B& b, int islot, const Hook& hook) {
    constexpr bool READ = REM > 1;
    if constexpr (READ) wait_bar<younger<NS, A::PPW, B::PPW, A::REG>(REM) + (P < NS - 2 ? XW : 0)>();
    hook(IC<P>{});
    gemm_step<KS, SWAP, READ, (REM > NS), (A::REG && REM > NS - 1)>(e.acc, e.f[P & 1], e.f[(P & 1) ^ 1], e.ring[P % NS], e.ring[(P + NS - 1) % NS], a, b,
                                                                   (P + 1) % NS * SLOTB, islot + P, P % NS * SLOTB);
    if constexpr (COUNT > 1) gemm_steps<P + 1, COUNT - 1, REM - 1, SLOTB, SWAP, XW>(e, a, b, islot, hook);
    else if constexpr (REM == 1) mfma_settle();
}

template <int SLOTB, class A, class B> __device__ __forceinline__ void gemm_fill(const A& a, const B& b, int p0, int p1) {
#pragma unroll
    for (int p = p0; p < p1; ++p) {
#pragma unroll
        for (int i = 0; i < B::PPW; ++i) b.issue(p, p * SLOTB, i);
        if constexpr (!A::REG) {
#pragma unroll
            for (int i = 0; i < A::PPW; ++i) a.issue(p, p * SLOTB, i);
        }
    }
}
template <int NS, int NA, int NB, int KS, class A, class B> __device__ __forceinline__ void gemm_first(Engine<NS, NA, NB, KS>& e, const A& a, const B& b) {
    if constexpr (!A::REG) {
#pragma unroll
        for (int i = 0; i < KS * NA * A::RPF; ++i) a.read(e.f[0].a[i / A::RPF], 0, i / A::RPF, i % A::RPF);
    }
#pragma unroll
    for (int i = 0; i < KS * NB * B::RPF; ++i) b.read(e.f[0].b[i / B::RPF], 0, i / B::RPF, i % B::RPF);
    SB();
}

constexpr int MANY = 1 << 20;
template <int NS, int NSLOTS> struct Rounds { static constexpr int RL = NS % 2 ? 2 * NS : NS, N = (NSLOTS - NS) / RL, TAIL = NSLOTS - N * RL; };
template <int SLOTB, int NSLOTS, int SWAP, int NS, int NA, int NB, int KS, class A, class B, class Gate, class Round, class Tail>
__device__ __forceinline__ void gemm_run(Engine<NS, NA, NB, KS>& e, const A& a, const B& b, const Gate& gate, const Round& round, const Tail& tail) {
    using R = Rounds<NS, NSLOTS>;
#pragma unroll
    for (int i = 0; i < NA; ++i)
#pragma unroll
        for (int j = 0; j < NB; ++j) e.acc[i][j] = f32x4{};
    gemm_fill<SLOTB>(a, b, 0, NS);
    if constexpr (A::REG) {
        gate();
#pragma unroll
        for (int p = 0; p < NS; ++p)
#pragma unroll
            for (int i = 0; i < NA; ++i) { e.ring[p][i] = u32x4{}; if (p < NS - 1) a.load(e.ring[p][i], p, i); }
        wait_bar<NA>();
    } else {
        wait_bar<(NS - 1) * (A::PPW + B::PPW)>();
    }
    gemm_first(e, a, b);
    for (int r = 0; r < R::N; ++r) { round(r); gemm_steps<0, R::RL, MANY, SLOTB, SWAP, 0>(e, a, b, r * R::RL + NS, [](auto) {}); }
    gemm_steps<0, R::TAIL, R::TAIL, SLOTB, SWAP, 0>(e, a, b, R::N * R::RL + NS, tail);
}

constexpr int PRE_KV = 128, PRE_LN = 128, PRE_LDS = 65536 + 1024 + 2048, W1_PRE = W1_ROWS * F * 2;

template <int CTRL> __device__ __forceinline__ float dpp_add(float v) {
    return v + __builtin_bit_cast(float, __builtin_amdgcn_update_dpp(0, __builtin_bit_cast(int, v), CTRL, 0xf, 0xf, true));
}
__device__ __forceinline__ float group_sum(float v) {
    const auto a = __builtin_amdgcn_permlane16_swap(__float_as_uint(v), __float_as_uint(v), false, false);
    v = __uint_as_float(a[0]) + __uint_as_float(a[1]);
    const auto b = __builtin_amdgcn_permlane32_swap(__float_as_uint(v), __float_as_uint(v), false, false);
    return __uint_as_float(b[0]) + __uint_as_float(b[1]);
}

__device__ __forceinline__ float row_sum(float v) { return dpp_add<0x140>(dpp_add<0x141>(dpp_add<0x4e>(dpp_add<0xb1>(v)))); }
__device__ __forceinline__ float fold32(float a, float b) {
    const auto r = __builtin_amdgcn_permlane32_swap(__float_as_uint(a), __float_as_uint(b), false, false);
    return __uint_as_float(r[0]) + __uint_as_float(r[1]);
}
__device__ __forceinline__ float fold16(float a, float b) {
    const auto r = __builtin_amdgcn_permlane16_swap(__float_as_uint(a), __float_as_uint(b), false, false);
    return __uint_as_float(r[0]) + __uint_as_float(r[1]);
}
template <int CTRL> __device__ __forceinline__ float dpp_max(float v) {
    return fmaxf(v, __builtin_bit_cast(float, __builtin_amdgcn_update_dpp(0, __builtin_bit_cast(int, v), CTRL, 0xf, 0xf, true)));
}
__device__ __forceinline__ float wave_max(float v) {
    v = dpp_max<0xb1>(v); v = dpp_max<0x4e>(v); v = dpp_max<0x141>(v); v = dpp_max<0x140>(v);
    const auto a = __builtin_amdgcn_permlane16_swap(__float_as_uint(v), __float_as_uint(v), false, false);
    v = fmaxf(__uint_as_float(a[0]), __uint_as_float(a[1]));
    const auto b = __builtin_amdgcn_permlane32_swap(__float_as_uint(v), __float_as_uint(v), false, false);
    return fmaxf(__uint_as_float(b[0]), __uint_as_float(b[1]));
}

template <u32 BYTES, int NB, int NTHR> __device__ __forceinline__ void touch(const char* p, int b, u32 scratch) {
    constexpr u32 LINES = BYTES / 128, PER = (LINES + NB - 1) / NB, N = (PER + NTHR - 1) / NTHR;
    const u32x4 srd = make_srd(p, BYTES);
#pragma unroll
    for (u32 i = 0; i < N; ++i) {
        const u32 l = i * NTHR + threadIdx.x, off = l < PER ? (b * PER + l) * 128u : 0x80000000u;
        asm volatile("s_mov_b32 m0, %0\n\ts_nop 0\n\tbuffer_load_dword %1, %2, 0 offen lds" ::"s"(scratch), "v"(off), "s"(srd) : "memory");
    }
}

#define CODE_ADDR(v, mark) asm volatile("s_getpc_b64 s[40:41]\n\ts_add_u32 s40, s40, " mark "@rel32@lo+4\n\ts_addc_u32 s41, s41, " mark "@rel32@hi+12" : "={s[40:41]}"(v) :: "scc")
__device__ __forceinline__ void touch_code(uint64_t a, uint64_t e, u32 scratch) {
    const u32x4 srd = make_srd((const void*)a, e > a ? (u32)(e - a) & ~3u : 0u);
    asm volatile("s_mov_b32 m0, %0\n\ts_nop 0\n\tbuffer_load_dword %1, %2, 0 offen lds" ::"s"(scratch), "v"(threadIdx.x * 128u), "s"(srd) : "memory");
}

template <bool V> __device__ __forceinline__ void kv_block(int t, const char* x, const char* wm, char* ws, lds_ptr lds, int w8, int lane) {
    const int g = lane >> 4, c = lane & 15, q = (lane >> 2) & 3, p = lane & 3;
    const lds_ptr img = lds + w8 * 8192;
    const u32x4 srd = make_srd(wm, D * 64);
    const u32 voff = (u32)(lane >> 2) * 64 + ((((lane >> 1) & 1) ^ (lane >> 5)) * 32) + (lane & 1) * 16;
    u32x4 xf[2][4];
#pragma unroll
    for (int ks = 0; ks < 4; ++ks)
#pragma unroll
        for (int j = 0; j < 2; ++j) xf[j][ks] = *(const u32x4*)(x + (size_t)(t * 32 + j * 16 + c) * (D * 2) + (w8 * 128 + ks * 32 + g * 8) * 2);
#pragma unroll
    for (int i = 0; i < 8; ++i) dma16((u32)(uintptr_t)img + i * 1024, voff, srd, w8 * 8192 + i * 1024);
    asm volatile("s_waitcnt vmcnt(0)" ::: "memory");
    f32x4 acc[4];
#pragma unroll
    for (int i = 0; i < 4; ++i) acc[i] = f32x4{};
#pragma unroll
    for (int ks = 0; ks < 4; ++ks)
#pragma unroll
        for (int i = 0; i < 2; ++i) {
            const lds_ptr rd = img + (ks * 32 + 8 * g + q) * 64 + ((i ^ (g & 1)) * 32) + p * 8;
            const u32x2 lo = lds_rdtr(rd), hi = lds_rdtr(rd + 256);
            const u32x4 wf = {lo[0], lo[1], hi[0], hi[1]};
#pragma unroll
            for (int j = 0; j < 2; ++j) acc[2 * i + j] = V ? M16(xf[j][ks], wf, acc[2 * i + j]) : M16(wf, xf[j][ks], acc[2 * i + j]);
        }
#pragma unroll
    for (int i = 0; i < 4; ++i) *(LDS f32x4*)(img + i * 1024 + lane * 16) = acc[i];
    wait_bar<0>();
    STAMP(0, 6);
    if (w8 >= 2) return;
    const int ta = V ? 2 * w8 : w8, tb = V ? 2 * w8 + 1 : w8 + 2, fr = t * 4 + 2 * V + w8;
    f32x4 a = {}, b = {};
#pragma unroll
    for (int v = 0; v < 8; ++v) { a += *(LDS const f32x4*)(lds + v * 8192 + ta * 1024 + lane * 16); b += *(LDS const f32x4*)(lds + v * 8192 + tb * 1024 + lane * 16); }
    if (HF_WT > 1) store_wt(ws + WS_KV, S * 128, fr * 1024 + lane * 16, pack8(a, b));
    else *(u32x4*)(ws + WS_KV + fr * 1024 + lane * 16) = pack8(a, b);
    float bnd = 0.f;
    if (!V) {
#pragma unroll
        for (int r = 0; r < 4; ++r) bnd += a[r] * a[r] + b[r] * b[r];
        bnd = group_sum(bnd);
        bnd = bnd == bnd ? bnd : __builtin_inff();
    } else {
#pragma unroll
        for (int r = 0; r < 4; ++r)
            if (!(fabsf(a[r]) <= SAFE_V && fabsf(b[r]) <= SAFE_V)) bnd = __builtin_inff();
    }
    bnd = wave_max(bnd);
    if (lane == 0) ((float*)(ws + WS_BND))[fr] = bnd;
}

__device__ __forceinline__ void ln_rows(int r, const char* x, const char* gw, const char* wq, const char* w1, char* ws, lds_ptr lds, u32 scratch, int w8, int lane) {
    const int u = w8 >> 2, cq = w8 & 3, chunk = 2 * r + u, row0 = 32 * (chunk >> 2) + 4 * (chunk & 3);
    u32x2 xv[8];
    float sh[8];
#pragma unroll
    for (int e = 0; e < 8; ++e) {
        const char* row = x + (size_t)(row0 + (e & 3) + 16 * (e >> 2)) * (D * 2);
        xv[e] = *(const u32x2*)(row + cq * 512 + lane * 8);
        sh[e] = bf_lo(*(const u32*)row);
    }
    u32x2 gv = *(const u32x2*)(gw + cq * 512 + lane * 8);
    PIN(gv);
#pragma unroll
    for (int e = 0; e < 8; ++e) PIN(xv[e]);
    if (r < 16) {
        uint64_t a, e;
        CODE_ADDR(a, "main_code_begin"); CODE_ADDR(e, "main_code_end");
        touch_code(a, e, scratch);
    }
    touch<D * D * 2, PRE_LN, 512>(wq, r, scratch);
    touch<W1_PRE, PRE_LN, 512>(w1, r, scratch);
    float part[3][8];
#pragma unroll
    for (int e = 0; e < 8; ++e) {
        const float a0 = bf_lo(xv[e][0]), a1 = bf_hi(xv[e][0]), a2 = bf_lo(xv[e][1]), a3 = bf_hi(xv[e][1]);
        const float d0 = a0 - sh[e], d1 = a1 - sh[e], d2 = a2 - sh[e], d3 = a3 - sh[e];
        part[0][e] = (d0 + d1) + (d2 + d3);
        part[1][e] = (d0 * d0 + d1 * d1) + (d2 * d2 + d3 * d3);
        part[2][e] = (a0 * bf_lo(gv[0]) + a1 * bf_hi(gv[0])) + (a2 * bf_lo(gv[1]) + a3 * bf_hi(gv[1]));
    }
    LDS float* red = (LDS float*)(lds + 65536);
#pragma unroll
    for (int k = 0; k < 3; ++k)
#pragma unroll
        for (int h = 0; h < 2; ++h) {
            const float v = row_sum(fold16(fold32(part[k][4 * h], part[k][4 * h + 2]), fold32(part[k][4 * h + 1], part[k][4 * h + 3])));
            if ((lane & 15) == 0) red[w8 * 24 + k * 8 + 4 * h + (lane >> 4)] = v;
        }
    wait_bar<63>();
    STAMP(0, 6);
    const int e8 = lane & 7;
    float st[3], mysh = 0.f;
#pragma unroll
    for (int k = 0; k < 3; ++k) st[k] = (red[(4 * u) * 24 + k * 8 + e8] + red[(4 * u + 1) * 24 + k * 8 + e8]) + (red[(4 * u + 2) * 24 + k * 8 + e8] + red[(4 * u + 3) * 24 + k * 8 + e8]);
#pragma unroll
    for (int e = 0; e < 8; ++e) mysh = e8 == e ? sh[e] : mysh;
    const float dm = st[0] * (1.f / D), var = fmaxf(st[1] * (1.f / D) - dm * dm, 0.f);
    const float mean = bf_round(mysh + dm), rs = 1.f / bf_round(sqrtf(bf_round(bf_round(var) + 1e-5f)));
    if (cq == 0 && lane < 8) ((float*)(ws + WS_G))[row0 + (lane & 3) + 16 * (lane >> 2)] = bf_round(1.f / (1.f + __expf(-bf_round(st[2]))));
    float h[4][8];
#pragma unroll
    for (int e = 0; e < 8; ++e) {
        const float m = __uint_as_float(__builtin_amdgcn_readlane(__float_as_uint(mean), e)), q = __uint_as_float(__builtin_amdgcn_readlane(__float_as_uint(rs), e));
        h[0][e] = (bf_lo(xv[e][0]) - m) * q; h[1][e] = (bf_hi(xv[e][0]) - m) * q; h[2][e] = (bf_lo(xv[e][1]) - m) * q; h[3][e] = (bf_hi(xv[e][1]) - m) * q;
    }
    const lds_ptr tb = lds + w8 * 4096;
#pragma unroll
    for (int m = 0; m < 4; ++m)
        *(LDS u32x4*)(tb + lane * 64 + ((m ^ ((lane >> 1) & 3)) * 16)) = u32x4{cvtpk(h[m][0], h[m][1]), cvtpk(h[m][2], h[m][3]), cvtpk(h[m][4], h[m][5]), cvtpk(h[m][6], h[m][7])};
    const u32 dst = chunk * (D * 16) + cq * 4096 + lane * 16;
#pragma unroll
    for (int i = 0; i < 4; ++i) {
        const u32x4 v = lds_rd128(tb + (i * 16 + (lane >> 2)) * 64 + (((lane & 3) ^ ((lane >> 3) & 3)) * 16));
        if (HF_WT) store_wt(ws + WS_HF, S * D * 2, dst + i * 1024, v);
        else *(u32x4*)(ws + WS_HF + dst + i * 1024) = v;
    }
}

__global__ void __launch_bounds__(512, 2) k_pre(const char* __restrict__ x, const char* __restrict__ wq, const char* __restrict__ wk, const char* __restrict__ wv,
                                                const char* __restrict__ gw, const char* __restrict__ w1, char* __restrict__ ws) {
    extern __shared__ __attribute__((aligned(16))) char smem[];
    const int lane = threadIdx.x & 63, w8 = __builtin_amdgcn_readfirstlane(threadIdx.x >> 6), b = blockIdx.x;
    STAMP(0, 0);
    const u32 scratch = (u32)(uintptr_t)(lds_ptr)smem + 65536 + 1024 + w8 * 256;
    asm volatile("pre_code_begin:");
    if (w8 == 0) {
        uint64_t a, e;
        CODE_ADDR(a, "pre_code_begin"); CODE_ADDR(e, "pre_code_end");
        touch_code(a, e, scratch);
    }
    const int t = (b & 7) + 8 * ((b >> 4) & 7), half = (b >> 3) & 1;
    if (b >= PRE_KV) {
        ln_rows(2 * t + half, x, gw, wq, w1, ws, (lds_ptr)smem, scratch, w8, lane);
        if (b == PRE_KV && threadIdx.x < CNT_N) *(u32*)(ws + WS_CNT + threadIdx.x * CNT_STRIDE) = 0u;
        asm volatile("s_waitcnt vmcnt(0)" ::: "memory");
    } else if (half) kv_block<true>(t, x, wv, ws, (lds_ptr)smem, w8, lane);
    else kv_block<false>(t, x, wk, ws, (lds_ptr)smem, w8, lane);
    STAMP(0, 7);
    asm volatile("pre_code_end:");
}

#define G1_PRIO 1
#define G1_NS_ 6
#define POST_PRIO 0
#define G_SLEEP 0
#define PF_SLEEP 5
#define Q_PRIO 2
#define PRIO_SWITCH 99
#define ATT_PRIO 0
constexpr int MID_ATT = 256, MID_G1 = 224, MID_PF = 32, G1_NS = G1_NS_;
constexpr int KV_STAGE = 16384, KV_NSTAGE = 16;

#define ATT_LOOP_ASM \
    "s_mov_b32 s23, 16\n" \
    "s_mov_b32 s24, 0xc000\n" \
    "s_mov_b32 s22, 0\n" \
    "s_add_u32 s20, %[ldst], 0x800\n" \
    "s_mov_b32 s21, 0x8800\n" \
    "ATT_LOOP_%=:\n" \
    "v_mfma_f32_16x16x32_bf16 v[8:11], v[80:83], v[68:71], 0\n" \
    "v_exp_f32 v0, v0\n" \
    "v_exp_f32 v1, v1\n" \
    "v_mfma_f32_16x16x32_bf16 v[12:15], v[84:87], v[68:71], 0\n" \
    "ds_read_b128 v[96:99], v124 offset:4096\n" \
    "v_exp_f32 v2, v2\n" \
    "v_exp_f32 v3, v3\n" \
    "v_mfma_f32_16x16x32_bf16 v[40:43], v[104:107], v[120:123], v[40:43]\n" \
    "v_cvt_pk_bf16_f32 v116, v0, v1\n" \
    "v_exp_f32 v4, v4\n" \
    "v_exp_f32 v5, v5\n" \
    "v_mfma_f32_16x16x32_bf16 v[44:47], v[108:111], v[120:123], v[44:47]\n" \
    "s_mov_b32 m0, s20\n" \
    "s_add_u32 s20, s20, 0x400\n" \
    "buffer_load_dwordx4 v126, %[srd], s21 offen lds\n" \
    "s_add_u32 s21, s21, 0x400\n" \
    "v_cvt_pk_bf16_f32 v117, v2, v3\n" \
    "v_exp_f32 v6, v6\n" \
    "v_exp_f32 v7, v7\n" \
    "v_mfma_f32_16x16x32_bf16 v[60:63], v[112:115], v[120:123], v[60:63]\n" \
    "v_cvt_pk_bf16_f32 v118, v4, v5\n" \
    "v_cvt_pk_bf16_f32 v119, v6, v7\n" \
    "s_waitcnt lgkmcnt(1)\n" \
    "v_mfma_f32_16x16x32_bf16 v[0:3], v[80:83], v[72:75], 0\n" \
    "v_exp_f32 v8, v8\n" \
    "v_exp_f32 v9, v9\n" \
    "v_mfma_f32_16x16x32_bf16 v[4:7], v[84:87], v[72:75], 0\n" \
    "ds_read_b128 v[100:103], v124 offset:5120\n" \
    "v_exp_f32 v10, v10\n" \
    "v_exp_f32 v11, v11\n" \
    "v_mfma_f32_16x16x32_bf16 v[16:19], v[88:91], v[116:119], v[16:19]\n" \
    "v_cvt_pk_bf16_f32 v120, v8, v9\n" \
    "v_exp_f32 v12, v12\n" \
    "v_exp_f32 v13, v13\n" \
    "v_mfma_f32_16x16x32_bf16 v[20:23], v[92:95], v[116:119], v[20:23]\n" \
    "v_cvt_pk_bf16_f32 v121, v10, v11\n" \
    "v_exp_f32 v14, v14\n" \
    "v_exp_f32 v15, v15\n" \
    "v_mfma_f32_16x16x32_bf16 v[48:51], v[112:115], v[116:119], v[48:51]\n" \
    "v_cvt_pk_bf16_f32 v122, v12, v13\n" \
    "v_cvt_pk_bf16_f32 v123, v14, v15\n" \
    "v_mfma_f32_16x16x32_bf16 v[8:11], v[80:83], v[76:79], 0\n" \
    "v_exp_f32 v0, v0\n" \
    "v_exp_f32 v1, v1\n" \
    "v_mfma_f32_16x16x32_bf16 v[12:15], v[84:87], v[76:79], 0\n" \
    "ds_read_b128 v[104:107], v124 offset:6144\n" \
    "v_exp_f32 v2, v2\n" \
    "v_exp_f32 v3, v3\n" \
    "v_mfma_f32_16x16x32_bf16 v[24:27], v[88:91], v[120:123], v[24:27]\n" \
    "v_cvt_pk_bf16_f32 v116, v0, v1\n" \
    "v_exp_f32 v4, v4\n" \
    "v_exp_f32 v5, v5\n" \
    "v_mfma_f32_16x16x32_bf16 v[28:31], v[92:95], v[120:123], v[28:31]\n" \
    "s_mov_b32 m0, s20\n" \
    "s_add_u32 s20, s20, 0x400\n" \
    "buffer_load_dwordx4 v126, %[srd], s21 offen lds\n" \
    "s_add_u32 s21, s21, 0x400\n" \
    "s_add_u32 s22, s22, 0x4000\n" \
    "s_and_b32 s22, s22, 0xffff\n" \
    "s_add_u32 s20, s22, %[ldst]\n" \
    "s_add_u32 s21, s21, 0x3000\n" \
    "v_cvt_pk_bf16_f32 v117, v2, v3\n" \
    "v_exp_f32 v6, v6\n" \
    "v_exp_f32 v7, v7\n" \
    "v_mfma_f32_16x16x32_bf16 v[52:55], v[112:115], v[120:123], v[52:55]\n" \
    "v_cvt_pk_bf16_f32 v118, v4, v5\n" \
    "v_cvt_pk_bf16_f32 v119, v6, v7\n" \
    "s_waitcnt lgkmcnt(1)\n" \
    "v_mfma_f32_16x16x32_bf16 v[0:3], v[96:99], v[64:67], 0\n" \
    "v_exp_f32 v8, v8\n" \
    "v_exp_f32 v9, v9\n" \
    "v_mfma_f32_16x16x32_bf16 v[4:7], v[100:103], v[64:67], 0\n" \
    "ds_read_b128 v[108:111], v124 offset:7168\n" \
    "v_exp_f32 v10, v10\n" \
    "v_exp_f32 v11, v11\n" \
    "v_mfma_f32_16x16x32_bf16 v[32:35], v[88:91], v[116:119], v[32:35]\n" \
    "v_cvt_pk_bf16_f32 v120, v8, v9\n" \
    "v_exp_f32 v12, v12\n" \
    "v_exp_f32 v13, v13\n" \
    "v_mfma_f32_16x16x32_bf16 v[36:39], v[92:95], v[116:119], v[36:39]\n" \
    "v_cvt_pk_bf16_f32 v121, v10, v11\n" \
    "v_exp_f32 v14, v14\n" \
    "v_exp_f32 v15, v15\n" \
    "v_mfma_f32_16x16x32_bf16 v[56:59], v[112:115], v[116:119], v[56:59]\n" \
    "v_cvt_pk_bf16_f32 v122, v12, v13\n" \
    "v_cvt_pk_bf16_f32 v123, v14, v15\n" \
    "v_mfma_f32_16x16x32_bf16 v[8:11], v[96:99], v[68:71], 0\n" \
    "v_exp_f32 v0, v0\n" \
    "v_exp_f32 v1, v1\n" \
    "v_mfma_f32_16x16x32_bf16 v[12:15], v[100:103], v[68:71], 0\n" \
    "ds_read_b128 v[80:83], v124 offset:8192\n" \
    "v_exp_f32 v2, v2\n" \
    "v_exp_f32 v3, v3\n" \
    "v_mfma_f32_16x16x32_bf16 v[40:43], v[88:91], v[120:123], v[40:43]\n" \
    "v_cvt_pk_bf16_f32 v116, v0, v1\n" \
    "v_exp_f32 v4, v4\n" \
    "v_exp_f32 v5, v5\n" \
    "v_mfma_f32_16x16x32_bf16 v[44:47], v[92:95], v[120:123], v[44:47]\n" \
    "v_cvt_pk_bf16_f32 v117, v2, v3\n" \
    "v_exp_f32 v6, v6\n" \
    "v_exp_f32 v7, v7\n" \
    "v_mfma_f32_16x16x32_bf16 v[60:63], v[112:115], v[120:123], v[60:63]\n" \
    "v_cvt_pk_bf16_f32 v118, v4, v5\n" \
    "v_cvt_pk_bf16_f32 v119, v6, v7\n" \
    "s_waitcnt lgkmcnt(1)\n" \
    "v_mfma_f32_16x16x32_bf16 v[0:3], v[96:99], v[72:75], 0\n" \
    "v_exp_f32 v8, v8\n" \
    "v_exp_f32 v9, v9\n" \
    "v_mfma_f32_16x16x32_bf16 v[4:7], v[100:103], v[72:75], 0\n" \
    "ds_read_b128 v[84:87], v124 offset:9216\n" \
    "v_exp_f32 v10, v10\n" \
    "v_exp_f32 v11, v11\n" \
    "v_mfma_f32_16x16x32_bf16 v[16:19], v[104:107], v[116:119], v[16:19]\n" \
    "v_cvt_pk_bf16_f32 v120, v8, v9\n" \
    "v_exp_f32 v12, v12\n" \
    "v_exp_f32 v13, v13\n" \
    "v_mfma_f32_16x16x32_bf16 v[20:23], v[108:111], v[116:119], v[20:23]\n" \
    "v_cvt_pk_bf16_f32 v121, v10, v11\n" \
    "v_exp_f32 v14, v14\n" \
    "v_exp_f32 v15, v15\n" \
    "v_mfma_f32_16x16x32_bf16 v[48:51], v[112:115], v[116:119], v[48:51]\n" \
    "v_cvt_pk_bf16_f32 v122, v12, v13\n" \
    "v_cvt_pk_bf16_f32 v123, v14, v15\n" \
    "v_mfma_f32_16x16x32_bf16 v[8:11], v[96:99], v[76:79], 0\n" \
    "v_exp_f32 v0, v0\n" \
    "v_exp_f32 v1, v1\n" \
    "v_mfma_f32_16x16x32_bf16 v[12:15], v[100:103], v[76:79], 0\n" \
    "ds_read_b128 v[88:91], v124 offset:10240\n" \
    "v_exp_f32 v2, v2\n" \
    "v_exp_f32 v3, v3\n" \
    "v_mfma_f32_16x16x32_bf16 v[24:27], v[104:107], v[120:123], v[24:27]\n" \
    "v_cvt_pk_bf16_f32 v116, v0, v1\n" \
    "v_exp_f32 v4, v4\n" \
    "v_exp_f32 v5, v5\n" \
    "v_mfma_f32_16x16x32_bf16 v[28:31], v[108:111], v[120:123], v[28:31]\n" \
    "v_cvt_pk_bf16_f32 v117, v2, v3\n" \
    "v_exp_f32 v6, v6\n" \
    "v_exp_f32 v7, v7\n" \
    "v_mfma_f32_16x16x32_bf16 v[52:55], v[112:115], v[120:123], v[52:55]\n" \
    "v_cvt_pk_bf16_f32 v118, v4, v5\n" \
    "v_cvt_pk_bf16_f32 v119, v6, v7\n" \
    "s_waitcnt lgkmcnt(1)\n" \
    "v_mfma_f32_16x16x32_bf16 v[0:3], v[80:83], v[64:67], 0\n" \
    "v_exp_f32 v8, v8\n" \
    "v_exp_f32 v9, v9\n" \
    "v_mfma_f32_16x16x32_bf16 v[4:7], v[84:87], v[64:67], 0\n" \
    "ds_read_b128 v[92:95], v124 offset:11264\n" \
    "v_exp_f32 v10, v10\n" \
    "v_exp_f32 v11, v11\n" \
    "v_mfma_f32_16x16x32_bf16 v[32:35], v[104:107], v[116:119], v[32:35]\n" \
    "v_cvt_pk_bf16_f32 v120, v8, v9\n" \
    "v_exp_f32 v12, v12\n" \
    "v_exp_f32 v13, v13\n" \
    "v_mfma_f32_16x16x32_bf16 v[36:39], v[108:111], v[116:119], v[36:39]\n" \
    "v_cvt_pk_bf16_f32 v121, v10, v11\n" \
    "v_exp_f32 v14, v14\n" \
    "v_exp_f32 v15, v15\n" \
    "v_mfma_f32_16x16x32_bf16 v[56:59], v[112:115], v[116:119], v[56:59]\n" \
    "v_cvt_pk_bf16_f32 v122, v12, v13\n" \
    "v_cvt_pk_bf16_f32 v123, v14, v15\n" \
    "v_mfma_f32_16x16x32_bf16 v[8:11], v[80:83], v[68:71], 0\n" \
    "v_exp_f32 v0, v0\n" \
    "v_exp_f32 v1, v1\n" \
    "v_mfma_f32_16x16x32_bf16 v[12:15], v[84:87], v[68:71], 0\n" \
    "ds_read_b128 v[96:99], v124 offset:12288\n" \
    "v_exp_f32 v2, v2\n" \
    "v_exp_f32 v3, v3\n" \
    "v_mfma_f32_16x16x32_bf16 v[40:43], v[104:107], v[120:123], v[40:43]\n" \
    "v_cvt_pk_bf16_f32 v116, v0, v1\n" \
    "v_exp_f32 v4, v4\n" \
    "v_exp_f32 v5, v5\n" \
    "v_mfma_f32_16x16x32_bf16 v[44:47], v[108:111], v[120:123], v[44:47]\n" \
    "v_cvt_pk_bf16_f32 v117, v2, v3\n" \
    "v_exp_f32 v6, v6\n" \
    "v_exp_f32 v7, v7\n" \
    "v_mfma_f32_16x16x32_bf16 v[60:63], v[112:115], v[120:123], v[60:63]\n" \
    "v_cvt_pk_bf16_f32 v118, v4, v5\n" \
    "v_cvt_pk_bf16_f32 v119, v6, v7\n" \
    "s_waitcnt lgkmcnt(1)\n" \
    "v_mfma_f32_16x16x32_bf16 v[0:3], v[80:83], v[72:75], 0\n" \
    "v_exp_f32 v8, v8\n" \
    "v_exp_f32 v9, v9\n" \
    "v_mfma_f32_16x16x32_bf16 v[4:7], v[84:87], v[72:75], 0\n" \
    "ds_read_b128 v[100:103], v124 offset:13312\n" \
    "v_exp_f32 v10, v10\n" \
    "v_exp_f32 v11, v11\n" \
    "v_mfma_f32_16x16x32_bf16 v[16:19], v[88:91], v[116:119], v[16:19]\n" \
    "v_cvt_pk_bf16_f32 v120, v8, v9\n" \
    "v_exp_f32 v12, v12\n" \
    "v_exp_f32 v13, v13\n" \
    "v_mfma_f32_16x16x32_bf16 v[20:23], v[92:95], v[116:119], v[20:23]\n" \
    "v_cvt_pk_bf16_f32 v121, v10, v11\n" \
    "v_exp_f32 v14, v14\n" \
    "v_exp_f32 v15, v15\n" \
    "v_mfma_f32_16x16x32_bf16 v[48:51], v[112:115], v[116:119], v[48:51]\n" \
    "v_cvt_pk_bf16_f32 v122, v12, v13\n" \
    "v_cvt_pk_bf16_f32 v123, v14, v15\n" \
    "v_mfma_f32_16x16x32_bf16 v[8:11], v[80:83], v[76:79], 0\n" \
    "v_exp_f32 v0, v0\n" \
    "v_exp_f32 v1, v1\n" \
    "v_mfma_f32_16x16x32_bf16 v[12:15], v[84:87], v[76:79], 0\n" \
    "ds_read_b128 v[104:107], v124 offset:14336\n" \
    "v_exp_f32 v2, v2\n" \
    "v_exp_f32 v3, v3\n" \
    "v_mfma_f32_16x16x32_bf16 v[24:27], v[88:91], v[120:123], v[24:27]\n" \
    "v_cvt_pk_bf16_f32 v116, v0, v1\n" \
    "v_exp_f32 v4, v4\n" \
    "v_exp_f32 v5, v5\n" \
    "v_mfma_f32_16x16x32_bf16 v[28:31], v[92:95], v[120:123], v[28:31]\n" \
    "v_cvt_pk_bf16_f32 v117, v2, v3\n" \
    "v_exp_f32 v6, v6\n" \
    "v_exp_f32 v7, v7\n" \
    "v_mfma_f32_16x16x32_bf16 v[52:55], v[112:115], v[120:123], v[52:55]\n" \
    "v_cvt_pk_bf16_f32 v118, v4, v5\n" \
    "v_cvt_pk_bf16_f32 v119, v6, v7\n" \
    "s_waitcnt lgkmcnt(1)\n" \
    "v_mfma_f32_16x16x32_bf16 v[0:3], v[96:99], v[64:67], 0\n" \
    "v_exp_f32 v8, v8\n" \
    "v_exp_f32 v9, v9\n" \
    "v_mfma_f32_16x16x32_bf16 v[4:7], v[100:103], v[64:67], 0\n" \
    "ds_read_b128 v[108:111], v124 offset:15360\n" \
    "v_exp_f32 v10, v10\n" \
    "v_exp_f32 v11, v11\n" \
    "v_mfma_f32_16x16x32_bf16 v[32:35], v[88:91], v[116:119], v[32:35]\n" \
    "v_cvt_pk_bf16_f32 v120, v8, v9\n" \
    "v_exp_f32 v12, v12\n" \
    "v_exp_f32 v13, v13\n" \
    "v_mfma_f32_16x16x32_bf16 v[36:39], v[92:95], v[116:119], v[36:39]\n" \
    "v_cvt_pk_bf16_f32 v121, v10, v11\n" \
    "v_exp_f32 v14, v14\n" \
    "v_exp_f32 v15, v15\n" \
    "v_mfma_f32_16x16x32_bf16 v[56:59], v[112:115], v[116:119], v[56:59]\n" \
    "v_cvt_pk_bf16_f32 v122, v12, v13\n" \
    "v_cvt_pk_bf16_f32 v123, v14, v15\n" \
    "s_waitcnt vmcnt(4)\n" \
    "s_barrier\n" \
    "v_mfma_f32_16x16x32_bf16 v[8:11], v[96:99], v[68:71], 0\n" \
    "v_exp_f32 v0, v0\n" \
    "v_exp_f32 v1, v1\n" \
    "v_mfma_f32_16x16x32_bf16 v[12:15], v[100:103], v[68:71], 0\n" \
    "ds_read_b128 v[80:83], v125 offset:0\n" \
    "v_exp_f32 v2, v2\n" \
    "v_exp_f32 v3, v3\n" \
    "v_mfma_f32_16x16x32_bf16 v[40:43], v[88:91], v[120:123], v[40:43]\n" \
    "v_cvt_pk_bf16_f32 v116, v0, v1\n" \
    "v_exp_f32 v4, v4\n" \
    "v_exp_f32 v5, v5\n" \
    "v_mfma_f32_16x16x32_bf16 v[44:47], v[92:95], v[120:123], v[44:47]\n" \
    "s_mov_b32 m0, s20\n" \
    "s_add_u32 s20, s20, 0x400\n" \
    "buffer_load_dwordx4 v126, %[srd], s21 offen lds\n" \
    "s_add_u32 s21, s21, 0x400\n" \
    "v_cvt_pk_bf16_f32 v117, v2, v3\n" \
    "v_exp_f32 v6, v6\n" \
    "v_exp_f32 v7, v7\n" \
    "v_mfma_f32_16x16x32_bf16 v[60:63], v[112:115], v[120:123], v[60:63]\n" \
    "v_cvt_pk_bf16_f32 v118, v4, v5\n" \
    "v_cvt_pk_bf16_f32 v119, v6, v7\n" \
    "s_waitcnt lgkmcnt(1)\n" \
    "v_mfma_f32_16x16x32_bf16 v[0:3], v[96:99], v[72:75], 0\n" \
    "v_exp_f32 v8, v8\n" \
    "v_exp_f32 v9, v9\n" \
    "v_mfma_f32_16x16x32_bf16 v[4:7], v[100:103], v[72:75], 0\n" \
    "ds_read_b128 v[84:87], v125 offset:1024\n" \
    "v_exp_f32 v10, v10\n" \
    "v_exp_f32 v11, v11\n" \
    "v_mfma_f32_16x16x32_bf16 v[16:19], v[104:107], v[116:119], v[16:19]\n" \
    "v_cvt_pk_bf16_f32 v120, v8, v9\n" \
    "v_exp_f32 v12, v12\n" \
    "v_exp_f32 v13, v13\n" \
    "v_mfma_f32_16x16x32_bf16 v[20:23], v[108:111], v[116:119], v[20:23]\n" \
    "v_cvt_pk_bf16_f32 v121, v10, v11\n" \
    "v_exp_f32 v14, v14\n" \
    "v_exp_f32 v15, v15\n" \
    "v_mfma_f32_16x16x32_bf16 v[48:51], v[112:115], v[116:119], v[48:51]\n" \
    "v_cvt_pk_bf16_f32 v122, v12, v13\n" \
    "v_cvt_pk_bf16_f32 v123, v14, v15\n" \
    "v_mfma_f32_16x16x32_bf16 v[8:11], v[96:99], v[76:79], 0\n" \
    "v_exp_f32 v0, v0\n" \
    "v_exp_f32 v1, v1\n" \
    "v_mfma_f32_16x16x32_bf16 v[12:15], v[100:103], v[76:79], 0\n" \
    "ds_read_b128 v[88:91], v125 offset:2048\n" \
    "v_exp_f32 v2, v2\n" \
    "v_exp_f32 v3, v3\n" \
    "v_mfma_f32_16x16x32_bf16 v[24:27], v[104:107], v[120:123], v[24:27]\n" \
    "v_cvt_pk_bf16_f32 v116, v0, v1\n" \
    "v_exp_f32 v4, v4\n" \
    "v_exp_f32 v5, v5\n" \
    "v_mfma_f32_16x16x32_bf16 v[28:31], v[108:111], v[120:123], v[28:31]\n" \
    "s_mov_b32 m0, s20\n" \
    "s_add_u32 s20, s20, 0x400\n" \
    "buffer_load_dwordx4 v126, %[srd], s21 offen lds\n" \
    "s_add_u32 s21, s21, 0x400\n" \
    "v_cvt_pk_bf16_f32 v117, v2, v3\n" \
    "v_exp_f32 v6, v6\n" \
    "v_exp_f32 v7, v7\n" \
    "v_mfma_f32_16x16x32_bf16 v[52:55], v[112:115], v[120:123], v[52:55]\n" \
    "v_cvt_pk_bf16_f32 v118, v4, v5\n" \
    "v_cvt_pk_bf16_f32 v119, v6, v7\n" \
    "s_waitcnt lgkmcnt(1)\n" \
    "v_mfma_f32_16x16x32_bf16 v[0:3], v[80:83], v[64:67], 0\n" \
    "v_exp_f32 v8, v8\n" \
    "v_exp_f32 v9, v9\n" \
    "v_mfma_f32_16x16x32_bf16 v[4:7], v[84:87], v[64:67], 0\n" \
    "ds_read_b128 v[92:95], v125 offset:3072\n" \
    "v_exp_f32 v10, v10\n" \
    "v_exp_f32 v11, v11\n" \
    "v_mfma_f32_16x16x32_bf16 v[32:35], v[104:107], v[116:119], v[32:35]\n" \
    "v_cvt_pk_bf16_f32 v120, v8, v9\n" \
    "v_exp_f32 v12, v12\n" \
    "v_exp_f32 v13, v13\n" \
    "v_mfma_f32_16x16x32_bf16 v[36:39], v[108:111], v[116:119], v[36:39]\n" \
    "v_cvt_pk_bf16_f32 v121, v10, v11\n" \
    "v_exp_f32 v14, v14\n" \
    "v_exp_f32 v15, v15\n" \
    "v_mfma_f32_16x16x32_bf16 v[56:59], v[112:115], v[116:119], v[56:59]\n" \
    "v_cvt_pk_bf16_f32 v122, v12, v13\n" \
    "v_cvt_pk_bf16_f32 v123, v14, v15\n" \
    "v_mov_b32 v124, v125\n" \
    "s_add_u32 s24, s24, 0x4000\n" \
    "s_and_b32 s24, s24, 0xffff\n" \
    "v_add_u32 v125, s24, v127\n" \
    "s_sub_u32 s23, s23, 1\n" \
    "s_cmp_lg_u32 s23, 0\n" \
    "s_cbranch_scc1 ATT_LOOP_%=\n" \
    "v_mfma_f32_16x16x32_bf16 v[40:43], v[104:107], v[120:123], v[40:43]\n" \
    "v_mfma_f32_16x16x32_bf16 v[44:47], v[108:111], v[120:123], v[44:47]\n" \
    "v_mfma_f32_16x16x32_bf16 v[60:63], v[112:115], v[120:123], v[60:63]\n" \
    "s_waitcnt vmcnt(0) lgkmcnt(0)\n" \
    "s_nop 15\n"

constexpr int Q_NS = 3, Q_SLOT = 24576;
__device__ __forceinline__ void attention(int b, const char* x, const char* wq, char* ws, lds_ptr lds, int w, int lane) {
    const int xc = b & 7, t = b >> 3, st128 = (xc >> 1) * 4 + (t & 3), hp = (xc & 1) * 8 + (t >> 2);
    const int head = 2 * hp + (w & 1), s0 = st128 * 128 + (w >> 1) * 64, g = lane >> 4, c = lane & 15;
    const u32x4 srd = make_srd(ws + WS_KV, S * 128);
    const u32 voff = w * 4096 + lane * 16, ldst = (u32)(uintptr_t)lds + w * 4096;
    auto dma = [&](int st, int p0, int p1) {
#pragma unroll
        for (int p = p0; p < p1; ++p) dma16(ldst + ((st + 2) & 3) * KV_STAGE + p * 1024, voff, srd, st * KV_STAGE + p * 1024);
    };
    __builtin_amdgcn_s_setprio(Q_PRIO);
    float gate[4];
#pragma unroll
    for (int i = 0; i < 4; ++i) asm volatile("global_load_dword %0, %1, off" : "=v"(gate[i]) : "v"(ws + WS_G + (s0 + i * 16 + c) * 4) : "memory");
    f32x4 kb2;
    asm volatile("global_load_dwordx4 %0, %1, off" : "=v"(kb2) : "v"(ws + WS_BND + lane * 16) : "memory");
    u32x4 q[4];
    {
        OpTr<64, D * 2, 2, true, 2> a; a.init(wq, D * D * 2, hp * 64, 0, lds, w, (w & 1) * 2, lane);
        OpRow bb; bb.init(x, S * D * 2, st128 * 128, lds + a.BYTES, w, (w >> 1) * 64, lane);
        Engine<Q_NS, 2, 4, 2> e;
        static_assert((D / 64 - 1) % Q_NS == 0 && Q_SLOT <= 2 * KV_STAGE, "the last Q slot lies below the first two K/V stages");
        gemm_run<Q_SLOT, D / 64, 2>(e, a, bb, [] {}, [](int) {}, [&](auto p) { if constexpr (p.v == Rounds<Q_NS, D / 64>::TAIL - 2) { dma(0, 0, 4); dma(1, 0, 4); } });
        wait_bar<4>();
        dma(2, 0, 2);
#pragma unroll
        for (int i = 0; i < 4; ++i) q[i] = pack8(e.acc[0][i] * QSCALE, e.acc[1][i] * QSCALE);
    }
    PIN(kb2);
    const float k2 = wave_max(fmaxf(fmaxf(kb2[0], kb2[1]), fmaxf(kb2[2], kb2[3])));
    bool ok = true;
#pragma unroll
    for (int i = 0; i < 4; ++i) {
        float n = 0.f;
#pragma unroll
        for (int m = 0; m < 4; ++m) { const float a = bf_lo(q[i][m]), h = bf_hi(q[i][m]); n += a * a + h * h; }
        ok = ok && group_sum(n) * k2 * 1.02f <= SAFE_LOG2 * SAFE_LOG2;
        PIN(gate[i]);
    }
    const bool safe = __all(ok);
    __builtin_amdgcn_s_setprio(ATT_PRIO);
    f32x4 o[8];
#pragma unroll
    for (int i = 0; i < 8; ++i) o[i] = f32x4{};
    float lsum[4];
    const lds_ptr base = lds + lane * 16;
    const f32x4 z = {};
    STAMP(1, 1);
    if (safe) {
        const u32 one = c == 0 ? 0x3f803f80u : 0u;
        const u32x4 ones = {one, one, one, one};
        const lds_ptr first = base + 2 * KV_STAGE;
        u32x4 ka = lds_rd128(first), kb = lds_rd128(first + 1024), v0 = lds_rd128(first + 2048), v1 = lds_rd128(first + 3072);
        f32x4 sa = M16(ka, q[0], z), sb = M16(kb, q[0], z);
        f32x16 oa = {}, ob = {}, l = {};
        u32x4 vz0 = {}, vz1 = {}, pz = {};
        u32 cur = (u32)(uintptr_t)first, nxt = cur + KV_STAGE;
        asm volatile("s_nop 7\n" ATT_LOOP_ASM
                     : "+{v[0:3]}"(sa), "+{v[4:7]}"(sb), "+{v[16:31]}"(oa), "+{v[32:47]}"(ob), "+{v[48:63]}"(l), "+{v[80:83]}"(ka), "+{v[84:87]}"(kb),
                       "+{v[88:91]}"(v0), "+{v[92:95]}"(v1), "+{v[104:107]}"(vz0), "+{v[108:111]}"(vz1), "+{v[120:123]}"(pz), "+{v124}"(cur), "+{v125}"(nxt)
                     : "{v[64:67]}"(q[0]), "{v[68:71]}"(q[1]), "{v[72:75]}"(q[2]), "{v[76:79]}"(q[3]), "{v[112:115]}"(ones), "{v126}"(voff),
                       "{v127}"((u32)(uintptr_t)base), [srd] "s"(srd), [ldst] "s"(ldst)
                     : "v8", "v9", "v10", "v11", "v12", "v13", "v14", "v15", "v96", "v97", "v98", "v99", "v100", "v101", "v102", "v103",
                       "v116", "v117", "v118", "v119", "s20", "s21", "s22", "s23", "s24", "scc", "memory");
#pragma unroll
        for (int i = 0; i < 4; ++i) {
            lsum[i] = __shfl(l[4 * i], c);
            o[i] = f32x4{oa[4 * i], oa[4 * i + 1], oa[4 * i + 2], oa[4 * i + 3]};
            o[4 + i] = f32x4{ob[4 * i], ob[4 * i + 1], ob[4 * i + 2], ob[4 * i + 3]};
        }
    } else {
        float mx[4];
#pragma unroll
        for (int i = 0; i < 4; ++i) { mx[i] = -__builtin_inff(); lsum[i] = 0.f; }
        dma(2, 2, 4);
        for (int st = 0; st < KV_NSTAGE; ++st) {
            wait_bar<4>();
            dma(st + 3, 0, 4);
#pragma unroll 1
            for (int kb = 0; kb < 4; ++kb) {
                const lds_ptr p = base + ((st + 2) & 3) * KV_STAGE + kb * 4096;
                const u32x4 k0 = lds_rd128(p), k1 = lds_rd128(p + 1024), v0 = lds_rd128(p + 2048), v1 = lds_rd128(p + 3072);
#pragma unroll
                for (int i = 0; i < 4; ++i) {
                    f32x4 sa = M16(k0, q[i], z), sb = M16(k1, q[i], z);
                    float m = mx[i];
#pragma unroll
                    for (int r = 0; r < 4; ++r) m = fmaxf(m, fmaxf(sa[r], sb[r]));
                    m = fmaxf(m, __shfl_xor(m, 16)); m = fmaxf(m, __shfl_xor(m, 32));
                    const float alpha = EX2(mx[i] - m);
                    mx[i] = m;
                    float t = 0.f;
#pragma unroll
                    for (int r = 0; r < 4; ++r) { sa[r] = EX2(sa[r] - m); sb[r] = EX2(sb[r] - m); t += sa[r] + sb[r]; }
                    lsum[i] = lsum[i] * alpha + t;
                    const u32x4 pp = pack8(sa, sb);
                    o[2 * i] = M16(v0, pp, o[2 * i] * alpha);
                    o[2 * i + 1] = M16(v1, pp, o[2 * i + 1] * alpha);
                }
            }
        }
        asm volatile("s_waitcnt vmcnt(0)" ::: "memory");
#pragma unroll
        for (int i = 0; i < 4; ++i) lsum[i] = group_sum(lsum[i]);
    }
    STAMP(1, 6);
#pragma unroll
    for (int i = 0; i < 4; ++i) {
        const float r = gate[i] / lsum[i];
        store_wt(ws + WS_OF, S * D * 2, (u32)((head * 4 + g) * S + s0 + i * 16 + c) * 16u, pack8(o[2 * i] * r, o[2 * i + 1] * r));
    }
    publish(ws, CNT_O + st128);
}

__device__ __forceinline__ f32x2 gelu_tanh(f32x2 x) {
    constexpr float C0 = -2.f * 0.7978845608028654f * 1.4426950408889634f, C1 = C0 * 0.044715f;
    const f32x2 y = x * __builtin_elementwise_fma(x * x, f32x2{C1, C1}, f32x2{C0, C0});
    const f32x2 d = f32x2{EX2(y[0]), EX2(y[1])} + 1.f;
    return x * f32x2{__builtin_amdgcn_rcpf(d[0]), __builtin_amdgcn_rcpf(d[1])};
}

__device__ __forceinline__ void gemm1_tile(int b, const void* w1, const char* b1, char* ws, lds_ptr lds, int w, int lane) {
    const int x = b & 7, t = b >> 3;
    const int dt = (x & 1) * 4 + (t & 3), d0 = dt * 128, f0 = ((x >> 1) * 7 + (t >> 2)) * 128;
    const int g = lane >> 4, c = lane & 15;
    u32x2 bias[8];
#pragma unroll
    for (int j = 0; j < 8; ++j) bias[j] = *(const u32x2*)(b1 + (f0 + j * 16 + g * 4) * 2);
    OpReg<2, D, 1, false> a; a.init(ws + WS_HF, S * D * 2, d0 + w * 32, 0, lane);
    OpTr<128, F * 2, 1, false, 8> bb; bb.init(w1, S * F * 2, f0, 0, lds, w, 0, lane);
    Engine<G1_NS, 2, 8> e;
    const u32 scratch = (u32)(uintptr_t)lds + G1_NS * 8192 + w * 256;
    gemm_run<8192, S / 32, 0>(e, a, bb, [] {}, [&](int r) { if (r == W1_ROUND) touch<S * F * 2 - W1_PRE, MID_G1, 256>((const char*)w1 + W1_PRE, b, scratch); }, [](auto) {});
    auto& acc = e.acc;
    STAMP(1, 1);
#pragma unroll
    for (int jp = 0; jp < 4; ++jp)
#pragma unroll
        for (int i = 0; i < 2; ++i) {
            f32x4 u[2];
#pragma unroll
            for (int h = 0; h < 2; ++h) {
                const u32x2 bv = bias[2 * jp + h];
                const f32x4 v = acc[i][2 * jp + h] + f32x4{bf_lo(bv[0]), bf_hi(bv[0]), bf_lo(bv[1]), bf_hi(bv[1])};
                const f32x2 lo = gelu_tanh(f32x2{v[0], v[1]}), hi = gelu_tanh(f32x2{v[2], v[3]});
                u[h] = f32x4{lo[0], lo[1], hi[0], hi[1]};
            }
            store_wt(ws + WS_UF, F * D * 2, (u32)(((f0 / 32 + jp) * 4 + g) * D + d0 + w * 32 + i * 16 + c) * 16u, pack8(u[0], u[1]));
        }
    publish(ws, CNT_U + dt * 2 + (x >> 2));
}

constexpr int P1_NS = 8, P1_BASE = 8192, P2_NS = 6, P2_SLOT = 12288;

__device__ __forceinline__ void post_tile(int b, const char* w2, const char* b2, const char* wo, char* ws, char* out, lds_ptr lds, int w, int lane) {
    const int x = b & 7, t = b >> 3, h = t & 1;
    const int s0 = ((x >> 1) * 8 + (t & 7)) * 64, dt = (x & 1) * 4 + (t >> 3), d0 = dt * 128;
    const int g = lane >> 4, c = lane & 15;
    constexpr int N1 = F / 64, N2 = D / 32;
    static_assert(N1 % P1_NS == 0 && P1_BASE + (P1_NS - 1) * 8192 >= (P2_NS - 1) * P2_SLOT, "the last w2 slot lies in the last wo slot");
    float omg[4], bs[4];
#pragma unroll
    for (int j = 0; j < 4; ++j) {
        const int s = s0 + j * 16 + c;
        asm volatile("global_load_dword %0, %1, off" : "=v"(omg[j]) : "v"(ws + WS_G + s * 4) : "memory");
        asm volatile("global_load_ushort %0, %1, off" : "=v"(bs[j]) : "v"(b2 + s * 2) : "memory");
    }
    OpTr<128, D * 2, 1, false, 2> a2; a2.init(wo, D * D * 2, d0, 0, lds, w, w * 2, lane);
    OpFrag<64, S, 1> b2o; b2o.init(ws + WS_OF, S * D * 2, s0, 0, lds + 8192, w, 0, lane);
    Engine<P2_NS, 2, 4> e;
    auto& acc = e.acc;
    u32 seen = 0;
    {
        OpReg<2, D, 1, !PLAIN_U> a1; a1.init(ws + WS_UF, F * D * 2, d0 + w * 32, h * N1, lane);
        OpTr<128, S * 2, 1, false, 8> b1; b1.init(w2, F * S * 2, s0 & ~127, h * N1, lds + P1_BASE, w, 4 * h, lane);
        Engine<P1_NS, 2, 8> e1;
        constexpr int TAIL = Rounds<P1_NS, N1>::TAIL;
        gemm_run<8192, N1, 2>(e1, a1, b1,
            [&] { if (w == 0) { await(ws, CNT_U + dt * 2 + h, F / 256); if (PLAIN_U) { acquire(); asm volatile("s_waitcnt vmcnt(0)" ::: "memory"); } } __builtin_amdgcn_s_barrier(); STAMP(1, 3); },
            [&](int r) { if (r == PRIO_SWITCH) __builtin_amdgcn_s_setprio(1); if (r == Rounds<P1_NS, N1>::N - 1) sample(seen, ws, CNT_O + (s0 >> 7)); },
            [&](auto p) { if constexpr (p.v == TAIL - 2) { confirm(seen, ws, CNT_O + (s0 >> 7), 16); if (PLAIN_O) acquire(); STAMP(1, 6); gemm_fill<P2_SLOT>(a2, b2o, 0, P2_NS - 1); } });
        STAMP(1, 4);
#pragma unroll
        for (int j = 0; j < 4; ++j) {
            PIN(omg[j]); PIN(bs[j]);
            omg[j] = bf_round(1.f - omg[j]); bs[j] = __uint_as_float(__float_as_uint(bs[j]) << 16);
#pragma unroll
            for (int i = 0; i < 2; ++i) {
                store_wt(ws + WS_X, 256u << 15, (u32)(((b * 4 + w) * 8 + i * 4 + j) * 64 + lane) * 16u, __builtin_bit_cast(u32x4, e1.acc[i][4 + j]));
                acc[i][j] = (e1.acc[i][j] + bs[j]) * omg[j];
            }
        }
    }
    constexpr int PPW = 3, XW = 8;
    wait_bar<(P2_NS - 2) * PPW + XW>();
    gemm_fill<P2_SLOT>(a2, b2o, P2_NS - 1, P2_NS);
    gemm_first(e, a2, b2o);
    gemm_steps<0, P2_NS, MANY, P2_SLOT, 2, XW>(e, a2, b2o, P2_NS, [&](auto p) { if constexpr (p.v == P2_NS - 2) signal(ws, CNT_X + b); });
    for (int r = 1; r < 3; ++r) gemm_steps<0, P2_NS, MANY, P2_SLOT, 2, 0>(e, a2, b2o, (r + 1) * P2_NS, [](auto) {});
    seen = 0;
    u32x4 got[8];
    const u32x4 xsrd = make_srd(ws + WS_X, 256u << 15);
    gemm_steps<0, N2 - 3 * P2_NS, N2 - 3 * P2_NS, P2_SLOT, 2, 0>(e, a2, b2o, 4 * P2_NS, [&](auto p) {
        if constexpr (p.v == 0) sample(seen, ws, CNT_X + (b ^ 8));
        if constexpr (p.v == P2_NS - 2) {
            confirm(seen, ws, CNT_X + (b ^ 8), 1);
#pragma unroll
            for (int i = 0; i < 8; ++i) {
                got[i] = u32x4{};
                load_coh(got[i], lane * 16, xsrd, (u32)(((b ^ 8) * 4 + w) * 8 + i) * 1024u);
            }
        }
    });
    asm volatile("s_waitcnt vmcnt(0)" ::: "memory");
#pragma unroll
    for (int i = 0; i < 8; ++i) { PIN(got[i]); acc[i >> 2][i & 3] += __builtin_bit_cast(f32x4, got[i]) * omg[i & 3]; }
    STAMP(1, 5);
#pragma unroll
    for (int j = 0; j < 4; ++j) {
        const auto r0 = __builtin_amdgcn_permlane16_swap(cvtpk(acc[0][j][0], acc[0][j][1]), cvtpk(acc[1][j][0], acc[1][j][1]), false, false);
        const auto r1 = __builtin_amdgcn_permlane16_swap(cvtpk(acc[0][j][2], acc[0][j][3]), cvtpk(acc[1][j][2], acc[1][j][3]), false, false);
        const int s = s0 + j * 16 + c, d = d0 + w * 32 + (g & 1) * 16 + (g >> 1) * 8;
        *(u32x4*)(out + ((size_t)s * D + d) * 2) = u32x4{r0[0], r1[0], r0[1], r1[1]};
    }
}

constexpr int MAIN_LDS = Q_NS * Q_SLOT;
static_assert(MAIN_LDS >= P2_NS * P2_SLOT && MAIN_LDS >= P1_BASE + P1_NS * 8192 && MAIN_LDS >= 4 * KV_STAGE && 2 * MAIN_LDS <= 160 * 1024, "two workgroups per CU");

__global__ void __launch_bounds__(256, 2) k_main(const char* __restrict__ x, const char* __restrict__ wq, const char* __restrict__ w1, const char* __restrict__ b1, const char* __restrict__ w2, const char* __restrict__ b2,
                                                 const char* __restrict__ wo, char* __restrict__ ws, char* __restrict__ out) {
    extern __shared__ __attribute__((aligned(16))) char smem[];
    const int lane = threadIdx.x & 63, w = __builtin_amdgcn_readfirstlane(threadIdx.x >> 6), b = blockIdx.x;
    asm volatile("main_code_begin:");
    STAMP(1, 0);
    if (b < MID_ATT) {
        attention(b, x, wq, ws, (lds_ptr)smem, w, lane);
    } else {
        const int gb = b - MID_ATT;
        __builtin_amdgcn_s_setprio(G1_PRIO);
        if (gb < MID_G1) gemm1_tile(gb, w1, b1, ws, (lds_ptr)smem, w, lane);
        else {
            const u32 scratch = (u32)(uintptr_t)(lds_ptr)smem + w * 256;
            for (int i = 0; i < PF_SLEEP; ++i) __builtin_amdgcn_s_sleep(64);
            for (int i = 0; i < 4; ++i) {
                touch<F * S * 2, MID_PF * 4, 256>(w2, (gb - MID_G1) * 4 + i, scratch);
                asm volatile("s_waitcnt vmcnt(0)" ::: "memory");
            }
            touch<D * D * 2, MID_PF, 256>(wo, gb - MID_G1, scratch);
            wait_bar<0>();
        }
        STAMP(1, 2);
        __builtin_amdgcn_s_setprio(POST_PRIO);
        post_tile(gb, w2, b2, wo, ws, out, (lds_ptr)smem, w, lane);
    }
    STAMP(1, 7);
    asm volatile("main_code_end:");
}

extern "C" void kernel_launch(void* const* d_in, const int* in_sizes, int n_in, void* d_out, int out_size, void* d_ws, size_t ws_size,
                              hipStream_t stream) {
    const char* x = (const char*)d_in[0];
    const char *wq = (const char*)d_in[1], *wk = (const char*)d_in[2], *wv = (const char*)d_in[3], *wo = (const char*)d_in[4];
    const char *w1 = (const char*)d_in[6], *b1 = (const char*)d_in[7], *w2 = (const char*)d_in[8], *b2 = (const char*)d_in[9];
    const char* gw = (const char*)d_in[10];
    char* ws = (char*)d_ws;
    (void)hipFuncSetAttribute((const void*)k_pre, hipFuncAttributeMaxDynamicSharedMemorySize, PRE_LDS);
    (void)hipFuncSetAttribute((const void*)k_main, hipFuncAttributeMaxDynamicSharedMemorySize, MAIN_LDS);
    k_pre<<<PRE_KV + PRE_LN, 512, PRE_LDS, stream>>>(x, wq, wk, wv, gw, w1, ws);
    k_main<<<MID_ATT + MID_G1 + MID_PF, 256, MAIN_LDS, stream>>>(x, wq, w1, b1, w2, b2, wo, ws, (char*)d_out);
}
```

```cpp
#include <hip/hip_runtime.h>
#include <stdint.h>

typedef unsigned u32;
typedef u32 u32x4 __attribute__((ext_vector_type(4)));
typedef u32 u32x2 __attribute__((ext_vector_type(2)));
typedef float f32x2 __attribute__((ext_vector_type(2)));
typedef float f32x4 __attribute__((ext_vector_type(4)));
typedef float f32x16 __attribute__((ext_vector_type(16)));
typedef __bf16 bf16x2 __attribute__((ext_vector_type(2)));
typedef __bf16 bf16x8 __attribute__((ext_vector_type(8)));
typedef short s16x4 __attribute__((ext_vector_type(4)));
#define LDS __attribute__((address_space(3)))
typedef LDS char* lds_ptr;

constexpr int S = 2048, D = 1024, F = 3584;
constexpr u32 WS_HF = 0, WS_OF = 8u << 20, WS_UF = 12u << 20, WS_KV = 20u << 20;
constexpr u32 WS_G = WS_KV + (256u << 10), WS_BND = WS_G + (8u << 10), WS_CNT = WS_BND + (4u << 10);
constexpr u32 WS_X = 24u << 20;
constexpr int CNT_U = 0, CNT_O = 16, CNT_X = 32, CNT_N = 288, CNT_STRIDE = 256;
constexpr float QSCALE = 0.17677669529663687f * 1.4426950408889634f;
constexpr float SAFE_LOG2 = 60.f;
constexpr float SAFE_V = 1.0e12f;

#define SB() __builtin_amdgcn_sched_barrier(0)
#define PIN(x) asm volatile("" : "+v"(x))
#define M16(a, b, c) __builtin_amdgcn_mfma_f32_16x16x32_bf16(__builtin_bit_cast(bf16x8, a), __builtin_bit_cast(bf16x8, b), c, 0, 0, 0)
__device__ __forceinline__ void mma16(f32x4& c, const u32x4& a, const u32x4& b) { asm volatile("v_mfma_f32_16x16x32_bf16 %0, %1, %2, %0" : "+v"(c) : "v"(a), "v"(b)); }
__device__ __forceinline__ void mfma_settle() { asm volatile("s_nop 7\n\ts_nop 1" ::: "memory"); SB(); }
#define EX2(x) __builtin_amdgcn_exp2f(x)

__device__ __forceinline__ u32 cvtpk(float lo, float hi) { f32x2 v = {lo, hi}; return __builtin_bit_cast(u32, __builtin_convertvector(v, bf16x2)); }
__device__ __forceinline__ float bf_lo(u32 v) { return __uint_as_float(v << 16); }
__device__ __forceinline__ float bf_hi(u32 v) { return __uint_as_float(v & 0xffff0000u); }
__device__ __forceinline__ float bf_round(float x) { return bf_lo(cvtpk(x, x)); }
__device__ __forceinline__ u32x4 pack8(const f32x4& a, const f32x4& b) { return u32x4{cvtpk(a[0], a[1]), cvtpk(a[2], a[3]), cvtpk(b[0], b[1]), cvtpk(b[2], b[3])}; }

__device__ __forceinline__ u32x4 make_srd(const void* p, u32 bytes) {
    const uint64_t a = (uint64_t)p;
    return u32x4{(u32)a, (u32)(a >> 32) & 0xffffu, bytes, 0x00020000u};
}
__device__ __forceinline__ void dma16(u32 lds, u32 voff, const u32x4& srd, u32 soff) {
    asm volatile("s_mov_b32 m0, %0\n\ts_nop 0\n\tbuffer_load_dwordx4 %1, %2, %3 offen lds" ::"s"(lds), "v"(voff), "s"(srd), "s"(soff) : "memory");
}
__device__ __forceinline__ void dma16_coh(u32 lds, u32 voff, const u32x4& srd, u32 soff) {
    asm volatile("s_mov_b32 m0, %0\n\ts_nop 0\n\tbuffer_load_dwordx4 %1, %2, %3 offen sc1 lds" ::"s"(lds), "v"(voff), "s"(srd), "s"(soff) : "memory");
}
template <int N> __device__ __forceinline__ void wait_bar() {
    __builtin_amdgcn_s_waitcnt(0xc07f);
    asm volatile("s_waitcnt vmcnt(%0)\n\ts_barrier" ::"n"(N) : "memory");
}
__device__ __forceinline__ u32x4 lds_rd128(lds_ptr p) { return *(LDS const u32x4*)p; }
__device__ __forceinline__ u32x2 lds_rdtr(lds_ptr p) { return __builtin_bit_cast(u32x2, __builtin_amdgcn_ds_read_tr16_b64_v4i16((LDS s16x4*)p)); }

typedef __attribute__((address_space(1))) u32 gu32;
__device__ __forceinline__ void store_wt(char* base, u32 bytes, u32 off, const u32x4& v) {
    __builtin_amdgcn_raw_buffer_store_b128(v, __builtin_amdgcn_make_buffer_rsrc(base, 0, bytes, 0x00020000), off, 0, 16);
}
__device__ __forceinline__ void load_coh(u32x4& f, u32 voff, const u32x4& srd, u32 soff) {
    asm volatile("buffer_load_dwordx4 %0, %1, %2, %3 offen sc1" : "+v"(f) : "v"(voff), "s"(srd), "s"(soff) : "memory");
}
__device__ __forceinline__ void signal(char* ws, int cnt) {
    if (threadIdx.x == 0) __hip_atomic_fetch_add((gu32*)(ws + WS_CNT + cnt * CNT_STRIDE), 1u, __ATOMIC_RELAXED, __HIP_MEMORY_SCOPE_AGENT);
}
__device__ __forceinline__ void publish(char* ws, int cnt) {
    asm volatile("s_waitcnt vmcnt(0)\n\ts_barrier" ::: "memory");
    signal(ws, cnt);
}
__device__ __forceinline__ void await(char* ws, int cnt, u32 target) {
    gu32* p = (gu32*)(ws + WS_CNT + cnt * CNT_STRIDE);
    for (int spins = 0; spins < (1 << 20); ++spins) {
        if (__builtin_amdgcn_readfirstlane(__hip_atomic_load(p, __ATOMIC_RELAXED, __HIP_MEMORY_SCOPE_AGENT)) >= target) break;
        __builtin_amdgcn_s_sleep(1);
    }
    asm volatile("" ::: "memory");
}

__device__ __forceinline__ void sample(u32& seen, char* ws, int cnt) {
    asm volatile("global_load_dword %0, %1, off sc1" : "+v"(seen) : "v"(ws + WS_CNT + cnt * CNT_STRIDE) : "memory");
}
__device__ __forceinline__ void confirm(u32& seen, char* ws, int cnt, u32 target) {
    PIN(seen);
    if (__builtin_amdgcn_readfirstlane(seen) < target) await(ws, cnt, target);
}


template <int R, int NR> struct OpFrag {
    static constexpr int BYTES = 4 * R * 16, PPW = BYTES / 4096, RPF = 1;
    static constexpr bool REG = false;
    u32x4 srd; u32 voff, ldst; lds_ptr rd;
    __device__ __forceinline__ void init(const void* base, u32 bytes, int row0, lds_ptr img, int w, int wrow, int lane) {
        srd = make_srd(base, bytes);
        voff = (u32)(row0 + lane + w * NR) * 16u;
        ldst = (u32)(uintptr_t)img + w * (R * 16);
        rd = img + ((lane >> 4) * R + wrow + (lane & 15)) * 16;
    }
    __device__ __forceinline__ void issue(int slot, u32 pos, int i) const { dma16_coh(ldst + pos + i * 1024, voff, srd, (u32)slot * (4u * NR * 16u) + i * 1024u); }
    __device__ __forceinline__ void read(u32x4& f, int pos, int i, int) const { f = lds_rd128(rd + pos + i * 256); }
};

template <int C, int RS, int KS, bool NAT, int NF> struct OpTr {
    static constexpr int BYTES = KS * 32 * C * 2, PPW = BYTES / 4096, RPF = 2;
    static constexpr bool REG = false;
    u32x4 srd; u32 voff, ldst; lds_ptr rd[NF];
    __device__ __forceinline__ void init(const void* base, u32 bytes, int col0, int k0, lds_ptr img, int w, int wch, int lane) {
        srd = make_srd(base, bytes);
        ldst = (u32)(uintptr_t)img + w * 1024;
        const int g = lane >> 4, q = (lane >> 2) & 3, p = lane & 3;
        if constexpr (C == 128) {
            const int f = NAT ? (g | (((w >> 1) & 1) << 2)) : (4 * (w & 1) + g);
            voff = (u32)(32 * k0 + 4 * w + g) * RS + col0 * 2 + ((((lane & 15) >> 1) ^ f) * 32) + (lane & 1) * 16;
            const int row = NAT ? 8 * g + q : 4 * g + q, fr = NAT ? (q | ((g & 1) << 2)) : (row & 7);
#pragma unroll
            for (int i = 0; i < NF; ++i) rd[i] = img + row * 256 + (((wch ^ i) ^ fr) * 32) + p * 8;
        } else {
            static_assert(C == 128 || C == 64, "unsupported transposed operand");
            const int f = NAT ? ((g & 1) | ((w & 1) << 1)) : g;
            voff = (u32)(32 * k0 + 8 * w + (lane >> 3)) * RS + col0 * 2 + ((((lane & 7) >> 1) ^ f) * 32) + (lane & 1) * 16;
            const int row = NAT ? 8 * g + q : 4 * g + q, fr = NAT ? ((q >> 1) | ((g & 1) << 1)) : ((row >> 1) & 3);
#pragma unroll
            for (int i = 0; i < NF; ++i) rd[i] = img + row * 128 + (((wch ^ i) ^ fr) * 32) + p * 8;
        }
    }
    __device__ __forceinline__ void issue(int slot, u32 pos, int i) const { dma16(ldst + pos + i * 4096, voff, srd, (u32)(slot * KS * 32 + i * (2048 / C)) * RS); }
    __device__ __forceinline__ void read(u32x4& f, int pos, int i, int t) const {
        const u32x2 v = lds_rdtr(rd[i % NF] + pos + i / NF * (32 * C * 2) + t * ((NAT ? 4 : 16) * C * 2));
        f[2 * t] = v[0]; f[2 * t + 1] = v[1];
    }
};

struct OpRow {
    static constexpr int BYTES = 128 * 128, PPW = 4, RPF = 1;
    static constexpr bool REG = false;
    u32x4 srd; u32 voff, ldst; lds_ptr rd[2];
    __device__ __forceinline__ void init(const void* base, u32 bytes, int row0, lds_ptr img, int w, int wrow, int lane) {
        srd = make_srd(base, bytes);
        ldst = (u32)(uintptr_t)img + w * 1024;
        const int g = lane >> 4, c = lane & 15;
        voff = (u32)(row0 + 8 * w + (lane >> 3)) * (D * 2) + (((lane & 7) ^ (4 * (w & 1) + g)) * 16);
#pragma unroll
        for (int ks = 0; ks < 2; ++ks) rd[ks] = img + (wrow + c) * 128 + (((4 * ks + g) ^ (c >> 1)) * 16);
    }
    __device__ __forceinline__ void issue(int slot, u32 pos, int i) const { dma16(ldst + pos + i * 4096, voff, srd, (u32)slot * 128u + i * (32u * D * 2u)); }
    __device__ __forceinline__ void read(u32x4& f, int pos, int i, int) const { f = lds_rd128(rd[i >> 2] + pos + (i & 3) * 2048); }
};

template <int NF, int NR, bool COH> struct OpReg {
    static constexpr int PPW = NF, RPF = 0;
    static constexpr bool REG = true;
    u32x4 srd; u32 voff;
    __device__ __forceinline__ void init(const void* base, u32 bytes, int row0, int k0, int lane) {
        srd = make_srd(base, bytes);
        voff = (u32)(row0 + (lane & 15) + ((lane >> 4) + 4 * k0) * NR) * 16u;
    }
    __device__ __forceinline__ void load(u32x4& f, int slot, int i) const {
        const u32 soff = (u32)slot * (4u * NR * 16u);
        if (COH) asm volatile("buffer_load_dwordx4 %0, %1, %2, %3 offen offset:%4 sc1" : "+v"(f) : "v"(voff), "s"(srd), "s"(soff), "n"(i * 256) : "memory");
        else asm volatile("buffer_load_dwordx4 %0, %1, %2, %3 offen offset:%4" : "+v"(f) : "v"(voff), "s"(srd), "s"(soff), "n"(i * 256) : "memory");
    }
};

template <int NA, int NB> struct Frags { u32x4 a[NA], b[NB]; };
template <int NS, int NA, int NB, int KS = 1> struct Engine { f32x4 acc[NA][NB]; Frags<NA * KS, NB * KS> f[2]; u32x4 ring[NS][NA]; };

template <int KS, int SWAP, bool READ, bool ISSUE, bool RISSUE, int NA, int NB, class A, class B>
__device__ __forceinline__ void gemm_step(f32x4 (&acc)[NA][NB], const Frags<NA * KS, NB * KS>& cur, Frags<NA * KS, NB * KS>& nxt, u32x4 (&ring)[NA],
                                          u32x4 (&prev)[NA], const A& a, const B& b, int rpos, int islot, u32 ipos) {
    constexpr int NM = NA * NB * KS, NRA = NA * A::RPF, NRK = NRA + NB * B::RPF, NRD = NRK * KS, RPM = (2 * NRD + NM - 1) / NM;
    constexpr int ND = A::PPW + B::PPW, DSTEP = NM / ND, DOFF = 1 % DSTEP;
    if constexpr (A::REG) {
#pragma unroll
        for (int i = 0; i < NA; ++i) PIN(ring[i]);
    }
#pragma unroll
    for (int m = 0; m < NM; ++m) {
        const int i = m % NA, j = m / NA % NB, ks = m / (NA * NB);
        const u32x4 av = A::REG ? ring[i] : cur.a[ks * NA + i];
        if (i >= SWAP) mma16(acc[i][j], cur.b[ks * NB + j], av);
        else mma16(acc[i][j], av, cur.b[ks * NB + j]);
        if constexpr (READ) {
#pragma unroll
            for (int rr = m * RPM; rr < (m + 1) * RPM && rr < NRD; ++rr) {
                const int k = rr / NRK, r = rr % NRK;
                if (r < B::RPF) b.read(nxt.b[k * NB], rpos, k * NB, r);
                else if (r >= B::RPF + NRA) b.read(nxt.b[k * NB + (r - NRA) / B::RPF], rpos, k * NB + (r - NRA) / B::RPF, (r - NRA) % B::RPF);
                else if constexpr (!A::REG) a.read(nxt.a[k * NA + (r - B::RPF) / A::RPF], rpos, k * NA + (r - B::RPF) / A::RPF, (r - B::RPF) % A::RPF);
            }
        }
        if (m % DSTEP == DOFF && m / DSTEP < ND) {
            const int d = m / DSTEP;
            if (d < B::PPW) { if constexpr (ISSUE) b.issue(islot, ipos, d); }
            else if constexpr (A::REG) { if constexpr (RISSUE) a.load(prev[d - B::PPW], islot - 1, d - B::PPW); }
            else if constexpr (ISSUE) a.issue(islot, ipos, d - B::PPW);
        }
        SB();
    }
}

template <int V> struct IC { static constexpr int v = V; };
template <int NS, int PA, int PB, bool REG> constexpr int younger(int rem) {
    if (rem == 2) return 0;
    int n = 0;
    for (int k = 1; k <= NS - 2; ++k) n += (rem + k > NS ? PB + (REG ? 0 : PA) : 0) + (REG && rem + k > NS - 1 ? PA : 0);
    return n;
}
template <int P, int COUNT, int REM, int SLOTB, int SWAP, int XW, int NS, int NA, int NB, int KS, class A, class B, class Hook>
__device__ __forceinline__ void gemm_steps(Engine<NS, NA, NB, KS>& e, const A& a, const B& b, int islot, const Hook& hook) {
    constexpr bool READ = REM > 1;
    if constexpr (READ) wait_bar<younger<NS, A::PPW, B::PPW, A::REG>(REM) + (P < NS - 2 ? XW : 0)>();
    hook(IC<P>{});
    gemm_step<KS, SWAP, READ, (REM > NS), (A::REG && REM > NS - 1)>(e.acc, e.f[P & 1], e.f[(P & 1) ^ 1], e.ring[P % NS], e.ring[(P + NS - 1) % NS], a, b,
                                                                   (P + 1) % NS * SLOTB, islot + P, P % NS * SLOTB);
    if constexpr (COUNT > 1) gemm_steps<P + 1, COUNT - 1, REM - 1, SLOTB, SWAP, XW>(e, a, b, islot, hook);
    else if constexpr (REM == 1) mfma_settle();
}

template <int SLOTB, class A, class B> __device__ __forceinline__ void gemm_fill(const A& a, const B& b, int p0, int p1) {
#pragma unroll
    for (int p = p0; p < p1; ++p) {
#pragma unroll
        for (int i = 0; i < B::PPW; ++i) b.issue(p, p * SLOTB, i);
        if constexpr (!A::REG) {
#pragma unroll
            for (int i = 0; i < A::PPW; ++i) a.issue(p, p * SLOTB, i);
        }
    }
}
template <int NS, int NA, int NB, int KS, class A, class B> __device__ __forceinline__ void gemm_first(Engine<NS, NA, NB, KS>& e, const A& a, const B& b) {
    if constexpr (!A::REG) {
#pragma unroll
        for (int i = 0; i < KS * NA * A::RPF; ++i) a.read(e.f[0].a[i / A::RPF], 0, i / A::RPF, i % A::RPF);
    }
#pragma unroll
    for (int i = 0; i < KS * NB * B::RPF; ++i) b.read(e.f[0].b[i / B::RPF], 0, i / B::RPF, i % B::RPF);
    SB();
}

constexpr int MANY = 1 << 20;
template <int NS, int NSLOTS> struct Rounds { static constexpr int RL = NS % 2 ? 2 * NS : NS, N = (NSLOTS - NS) / RL, TAIL = NSLOTS - N * RL; };
template <int SLOTB, int NSLOTS, int SWAP, int NS, int NA, int NB, int KS, class A, class B, class Gate, class Round, class Tail>
__device__ __forceinline__ void gemm_run(Engine<NS, NA, NB, KS>& e, const A& a, const B& b, const Gate& gate, const Round& round, const Tail& tail) {
    using R = Rounds<NS, NSLOTS>;
#pragma unroll
    for (int i = 0; i < NA; ++i)
#pragma unroll
        for (int j = 0; j < NB; ++j) e.acc[i][j] = f32x4{};
    gemm_fill<SLOTB>(a, b, 0, NS);
    if constexpr (A::REG) {
        gate();
#pragma unroll
        for (int p = 0; p < NS; ++p)
#pragma unroll
            for (int i = 0; i < NA; ++i) { e.ring[p][i] = u32x4{}; if (p < NS - 1) a.load(e.ring[p][i], p, i); }
        wait_bar<NA>();
    } else {
        wait_bar<(NS - 1) * (A::PPW + B::PPW)>();
    }
    gemm_first(e, a, b);
    for (int r = 0; r < R::N; ++r) { round(r); gemm_steps<0, R::RL, MANY, SLOTB, SWAP, 0>(e, a, b, r * R::RL + NS, [](auto) {}); }
    gemm_steps<0, R::TAIL, R::TAIL, SLOTB, SWAP, 0>(e, a, b, R::N * R::RL + NS, tail);
}

constexpr int PRE_KV = 128, PRE_LN = 128, PRE_LDS = 65536 + 1024 + 2048, W1_PRE = S / 2 * F * 2;

template <int CTRL> __device__ __forceinline__ float dpp_add(float v) {
    return v + __builtin_bit_cast(float, __builtin_amdgcn_update_dpp(0, __builtin_bit_cast(int, v), CTRL, 0xf, 0xf, true));
}
__device__ __forceinline__ float group_sum(float v) {
    const auto a = __builtin_amdgcn_permlane16_swap(__float_as_uint(v), __float_as_uint(v), false, false);
    v = __uint_as_float(a[0]) + __uint_as_float(a[1]);
    const auto b = __builtin_amdgcn_permlane32_swap(__float_as_uint(v), __float_as_uint(v), false, false);
    return __uint_as_float(b[0]) + __uint_as_float(b[1]);
}

__device__ __forceinline__ float row_sum(float v) { return dpp_add<0x140>(dpp_add<0x141>(dpp_add<0x4e>(dpp_add<0xb1>(v)))); }
__device__ __forceinline__ float fold32(float a, float b) {
    const auto r = __builtin_amdgcn_permlane32_swap(__float_as_uint(a), __float_as_uint(b), false, false);
    return __uint_as_float(r[0]) + __uint_as_float(r[1]);
}
__device__ __forceinline__ float fold16(float a, float b) {
    const auto r = __builtin_amdgcn_permlane16_swap(__float_as_uint(a), __float_as_uint(b), false, false);
    return __uint_as_float(r[0]) + __uint_as_float(r[1]);
}
template <int CTRL> __device__ __forceinline__ float dpp_max(float v) {
    return fmaxf(v, __builtin_bit_cast(float, __builtin_amdgcn_update_dpp(0, __builtin_bit_cast(int, v), CTRL, 0xf, 0xf, true)));
}
__device__ __forceinline__ float wave_max(float v) {
    v = dpp_max<0xb1>(v); v = dpp_max<0x4e>(v); v = dpp_max<0x141>(v); v = dpp_max<0x140>(v);
    const auto a = __builtin_amdgcn_permlane16_swap(__float_as_uint(v), __float_as_uint(v), false, false);
    v = fmaxf(__uint_as_float(a[0]), __uint_as_float(a[1]));
    const auto b = __builtin_amdgcn_permlane32_swap(__float_as_uint(v), __float_as_uint(v), false, false);
    return fmaxf(__uint_as_float(b[0]), __uint_as_float(b[1]));
}

template <u32 BYTES, int NB, int NTHR> __device__ __forceinline__ void touch(const char* p, int b, u32 scratch) {
    constexpr u32 LINES = BYTES / 128, PER = (LINES + NB - 1) / NB, N = (PER + NTHR - 1) / NTHR;
    const u32x4 srd = make_srd(p, BYTES);
#pragma unroll
    for (u32 i = 0; i < N; ++i) {
        const u32 l = i * NTHR + threadIdx.x, off = l < PER ? (b * PER + l) * 128u : 0x80000000u;
        asm volatile("s_mov_b32 m0, %0\n\ts_nop 0\n\tbuffer_load_dword %1, %2, 0 offen lds" ::"s"(scratch), "v"(off), "s"(srd) : "memory");
    }
}

#define CODE_ADDR(v, mark) asm volatile("s_getpc_b64 s[40:41]\n\ts_add_u32 s40, s40, " mark "@rel32@lo+4\n\ts_addc_u32 s41, s41, " mark "@rel32@hi+12" : "={s[40:41]}"(v) :: "scc")
__device__ __forceinline__ void touch_code(uint64_t a, uint64_t e, u32 scratch) {
    const u32x4 srd = make_srd((const void*)a, e > a ? (u32)(e - a) & ~3u : 0u);
    asm volatile("s_mov_b32 m0, %0\n\ts_nop 0\n\tbuffer_load_dword %1, %2, 0 offen lds" ::"s"(scratch), "v"(threadIdx.x * 128u), "s"(srd) : "memory");
}

__device__ __forceinline__ void touch_pages(const char* p, u32 bytes, u32 scratch) {
    const u32x4 srd = make_srd(p, bytes);
    asm volatile("s_mov_b32 m0, %0\n\ts_nop 0\n\tbuffer_load_dword %1, %2, 0 offen lds" ::"s"(scratch), "v"(threadIdx.x << 16), "s"(srd) : "memory");
}

template <bool V> __device__ __forceinline__ void kv_block(int t, const char* x, const char* wm, char* ws, lds_ptr lds, int w8, int lane) {
    const int g = lane >> 4, c = lane & 15, q = (lane >> 2) & 3, p = lane & 3;
    const lds_ptr img = lds + w8 * 8192;
    const u32x4 srd = make_srd(wm, D * 64);
    const u32 voff = (u32)(lane >> 2) * 64 + ((((lane >> 1) & 1) ^ (lane >> 5)) * 32) + (lane & 1) * 16;
    u32x4 xf[2][4];
#pragma unroll
    for (int ks = 0; ks < 4; ++ks)
#pragma unroll
        for (int j = 0; j < 2; ++j) xf[j][ks] = *(const u32x4*)(x + (size_t)(t * 32 + j * 16 + c) * (D * 2) + (w8 * 128 + ks * 32 + g * 8) * 2);
#pragma unroll
    for (int i = 0; i < 8; ++i) dma16((u32)(uintptr_t)img + i * 1024, voff, srd, w8 * 8192 + i * 1024);
    asm volatile("s_waitcnt vmcnt(0)" ::: "memory");
    f32x4 acc[4];
#pragma unroll
    for (int i = 0; i < 4; ++i) acc[i] = f32x4{};
#pragma unroll
    for (int ks = 0; ks < 4; ++ks)
#pragma unroll
        for (int i = 0; i < 2; ++i) {
            const lds_ptr rd = img + (ks * 32 + 8 * g + q) * 64 + ((i ^ (g & 1)) * 32) + p * 8;
            const u32x2 lo = lds_rdtr(rd), hi = lds_rdtr(rd + 256);
            const u32x4 wf = {lo[0], lo[1], hi[0], hi[1]};
#pragma unroll
            for (int j = 0; j < 2; ++j) acc[2 * i + j] = V ? M16(xf[j][ks], wf, acc[2 * i + j]) : M16(wf, xf[j][ks], acc[2 * i + j]);
        }
#pragma unroll
    for (int i = 0; i < 4; ++i) *(LDS f32x4*)(img + i * 1024 + lane * 16) = acc[i];
    wait_bar<0>();
    if (w8 >= 2) return;
    const int ta = V ? 2 * w8 : w8, tb = V ? 2 * w8 + 1 : w8 + 2, fr = t * 4 + 2 * V + w8;
    f32x4 a = {}, b = {};
#pragma unroll
    for (int v = 0; v < 8; ++v) { a += *(LDS const f32x4*)(lds + v * 8192 + ta * 1024 + lane * 16); b += *(LDS const f32x4*)(lds + v * 8192 + tb * 1024 + lane * 16); }
    store_wt(ws + WS_KV, S * 128, fr * 1024 + lane * 16, pack8(a, b));
    float bnd = 0.f;
    if (!V) {
#pragma unroll
        for (int r = 0; r < 4; ++r) bnd += a[r] * a[r] + b[r] * b[r];
        bnd = group_sum(bnd);
        bnd = bnd == bnd ? bnd : __builtin_inff();
    } else {
#pragma unroll
        for (int r = 0; r < 4; ++r)
            if (!(fabsf(a[r]) <= SAFE_V && fabsf(b[r]) <= SAFE_V)) bnd = __builtin_inff();
    }
    bnd = wave_max(bnd);
    if (lane == 0) ((float*)(ws + WS_BND))[fr] = bnd;
}

__device__ __forceinline__ void ln_rows(int r, const char* x, const char* gw, const char* wq, const char* w1, const char* w2, const char* wo, const char* out,
                                        char* ws, lds_ptr lds, u32 scratch, int w8, int lane) {
    const int u = w8 >> 2, cq = w8 & 3, chunk = 2 * r + u, row0 = 32 * (chunk >> 2) + 4 * (chunk & 3);
    u32x2 xv[8];
    float sh[8];
#pragma unroll
    for (int e = 0; e < 8; ++e) {
        const char* row = x + (size_t)(row0 + (e & 3) + 16 * (e >> 2)) * (D * 2);
        xv[e] = *(const u32x2*)(row + cq * 512 + lane * 8);
        sh[e] = bf_lo(*(const u32*)row);
    }
    u32x2 gv = *(const u32x2*)(gw + cq * 512 + lane * 8);
    PIN(gv);
#pragma unroll
    for (int e = 0; e < 8; ++e) PIN(xv[e]);
    if (r < 16) {
        uint64_t a, e;
        CODE_ADDR(a, "main_code_begin"); CODE_ADDR(e, "main_code_end");
        touch_code(a, e, scratch);
    } else if (r < 32) {
        if (r & 1) { touch_pages(w1, S * F * 2, scratch); touch_pages(w2, S * F * 2, scratch); touch_pages(wo, D * D * 2, scratch); }
        else { touch_pages(ws, WS_X + (256u << 15), scratch); touch_pages(out, S * D * 2, scratch); }
    }
    touch<D * D * 2, PRE_LN, 512>(wq, r, scratch);
    touch<W1_PRE, PRE_LN, 512>(w1, r, scratch);
    float part[3][8];
#pragma unroll
    for (int e = 0; e < 8; ++e) {
        const float a0 = bf_lo(xv[e][0]), a1 = bf_hi(xv[e][0]), a2 = bf_lo(xv[e][1]), a3 = bf_hi(xv[e][1]);
        const float d0 = a0 - sh[e], d1 = a1 - sh[e], d2 = a2 - sh[e], d3 = a3 - sh[e];
        part[0][e] = (d0 + d1) + (d2 + d3);
        part[1][e] = (d0 * d0 + d1 * d1) + (d2 * d2 + d3 * d3);
        part[2][e] = (a0 * bf_lo(gv[0]) + a1 * bf_hi(gv[0])) + (a2 * bf_lo(gv[1]) + a3 * bf_hi(gv[1]));
    }
    LDS float* red = (LDS float*)(lds + 65536);
#pragma unroll
    for (int k = 0; k < 3; ++k)
#pragma unroll
        for (int h = 0; h < 2; ++h) {
            const float v = row_sum(fold16(fold32(part[k][4 * h], part[k][4 * h + 2]), fold32(part[k][4 * h + 1], part[k][4 * h + 3])));
            if ((lane & 15) == 0) red[w8 * 24 + k * 8 + 4 * h + (lane >> 4)] = v;
        }
    wait_bar<63>();
    const int e8 = lane & 7;
    float st[3], mysh = 0.f;
#pragma unroll
    for (int k = 0; k < 3; ++k) st[k] = (red[(4 * u) * 24 + k * 8 + e8] + red[(4 * u + 1) * 24 + k * 8 + e8]) + (red[(4 * u + 2) * 24 + k * 8 + e8] + red[(4 * u + 3) * 24 + k * 8 + e8]);
#pragma unroll
    for (int e = 0; e < 8; ++e) mysh = e8 == e ? sh[e] : mysh;
    const float dm = st[0] * (1.f / D), var = fmaxf(st[1] * (1.f / D) - dm * dm, 0.f);
    const float mean = bf_round(mysh + dm), rs = 1.f / bf_round(sqrtf(bf_round(bf_round(var) + 1e-5f)));
    if (cq == 0 && lane < 8) ((float*)(ws + WS_G))[row0 + (lane & 3) + 16 * (lane >> 2)] = bf_round(1.f / (1.f + __expf(-bf_round(st[2]))));
    float h[4][8];
#pragma unroll
    for (int e = 0; e < 8; ++e) {
        const float m = __uint_as_float(__builtin_amdgcn_readlane(__float_as_uint(mean), e)), q = __uint_as_float(__builtin_amdgcn_readlane(__float_as_uint(rs), e));
        h[0][e] = (bf_lo(xv[e][0]) - m) * q; h[1][e] = (bf_hi(xv[e][0]) - m) * q; h[2][e] = (bf_lo(xv[e][1]) - m) * q; h[3][e] = (bf_hi(xv[e][1]) - m) * q;
    }
    const lds_ptr tb = lds + w8 * 4096;
#pragma unroll
    for (int m = 0; m < 4; ++m)
        *(LDS u32x4*)(tb + lane * 64 + ((m ^ ((lane >> 1) & 3)) * 16)) = u32x4{cvtpk(h[m][0], h[m][1]), cvtpk(h[m][2], h[m][3]), cvtpk(h[m][4], h[m][5]), cvtpk(h[m][6], h[m][7])};
    const u32 dst = chunk * (D * 16) + cq * 4096 + lane * 16;
#pragma unroll
    for (int i = 0; i < 4; ++i) store_wt(ws + WS_HF, S * D * 2, dst + i * 1024, lds_rd128(tb + (i * 16 + (lane >> 2)) * 64 + (((lane & 3) ^ ((lane >> 3) & 3)) * 16)));
}

__global__ void __launch_bounds__(512, 2) k_pre(const char* __restrict__ x, const char* __restrict__ wq, const char* __restrict__ wk, const char* __restrict__ wv,
                                                const char* __restrict__ gw, const char* __restrict__ w1, const char* __restrict__ w2, const char* __restrict__ wo,
                                                const char* __restrict__ out, char* __restrict__ ws) {
    extern __shared__ __attribute__((aligned(16))) char smem[];
    const int lane = threadIdx.x & 63, w8 = __builtin_amdgcn_readfirstlane(threadIdx.x >> 6), b = blockIdx.x;
    const u32 scratch = (u32)(uintptr_t)(lds_ptr)smem + 65536 + 1024 + w8 * 256;
    asm volatile("pre_code_begin:");
    if (w8 == 0) {
        uint64_t a, e;
        CODE_ADDR(a, "pre_code_begin"); CODE_ADDR(e, "pre_code_end");
        touch_code(a, e, scratch);
    }
    const int t = (b & 7) + 8 * ((b >> 4) & 7), half = (b >> 3) & 1;
    if (b >= PRE_KV) {
        ln_rows(2 * t + half, x, gw, wq, w1, w2, wo, out, ws, (lds_ptr)smem, scratch, w8, lane);
        if (b == PRE_KV && threadIdx.x < CNT_N) *(u32*)(ws + WS_CNT + threadIdx.x * CNT_STRIDE) = 0u;
        asm volatile("s_waitcnt vmcnt(0)" ::: "memory");
    } else if (half) kv_block<true>(t, x, wv, ws, (lds_ptr)smem, w8, lane);
    else kv_block<false>(t, x, wk, ws, (lds_ptr)smem, w8, lane);
    asm volatile("pre_code_end:");
}

constexpr int MID_ATT = 256, MID_G1 = 224, MID_PF = 32, G1_NS = 6;
constexpr int KV_STAGE = 16384, KV_NSTAGE = 16;

#define ATT_LOOP_ASM \
    "s_mov_b32 s23, 16\n" \
    "s_mov_b32 s24, 0xc000\n" \
    "s_mov_b32 s22, 0\n" \
    "s_add_u32 s20, %[ldst], 0x800\n" \
    "s_mov_b32 s21, 0x8800\n" \
    "ATT_LOOP_%=:\n" \
    "v_mfma_f32_16x16x32_bf16 v[8:11], v[80:83], v[68:71], 0\n" \
    "v_exp_f32 v0, v0\n" \
    "v_exp_f32 v1, v1\n" \
    "v_mfma_f32_16x16x32_bf16 v[12:15], v[84:87], v[68:71], 0\n" \
    "ds_read_b128 v[96:99], v124 offset:4096\n" \
    "v_exp_f32 v2, v2\n" \
    "v_exp_f32 v3, v3\n" \
    "v_mfma_f32_16x16x32_bf16 v[40:43], v[104:107], v[120:123], v[40:43]\n" \
    "v_cvt_pk_bf16_f32 v116, v0, v1\n" \
    "v_exp_f32 v4, v4\n" \
    "v_exp_f32 v5, v5\n" \
    "v_mfma_f32_16x16x32_bf16 v[44:47], v[108:111], v[120:123], v[44:47]\n" \
    "s_mov_b32 m0, s20\n" \
    "s_add_u32 s20, s20, 0x400\n" \
    "buffer_load_dwordx4 v126, %[srd], s21 offen lds\n" \
    "s_add_u32 s21, s21, 0x400\n" \
    "v_cvt_pk_bf16_f32 v117, v2, v3\n" \
    "v_exp_f32 v6, v6\n" \
    "v_exp_f32 v7, v7\n" \
    "v_mfma_f32_16x16x32_bf16 v[60:63], v[112:115], v[120:123], v[60:63]\n" \
    "v_cvt_pk_bf16_f32 v118, v4, v5\n" \
    "v_cvt_pk_bf16_f32 v119, v6, v7\n" \
    "s_waitcnt lgkmcnt(1)\n" \
    "v_mfma_f32_16x16x32_bf16 v[0:3], v[80:83], v[72:75], 0\n" \
    "v_exp_f32 v8, v8\n" \
    "v_exp_f32 v9, v9\n" \
    "v_mfma_f32_16x16x32_bf16 v[4:7], v[84:87], v[72:75], 0\n" \
    "ds_read_b128 v[100:103], v124 offset:5120\n" \
    "v_exp_f32 v10, v10\n" \
    "v_exp_f32 v11, v11\n" \
    "v_mfma_f32_16x16x32_bf16 v[16:19], v[88:91], v[116:119], v[16:19]\n" \
    "v_cvt_pk_bf16_f32 v120, v8, v9\n" \
    "v_exp_f32 v12, v12\n" \
    "v_exp_f32 v13, v13\n" \
    "v_mfma_f32_16x16x32_bf16 v[20:23], v[92:95], v[116:119], v[20:23]\n" \
    "v_cvt_pk_bf16_f32 v121, v10, v11\n" \
    "v_exp_f32 v14, v14\n" \
    "v_exp_f32 v15, v15\n" \
    "v_mfma_f32_16x16x32_bf16 v[48:51], v[112:115], v[116:119], v[48:51]\n" \
    "v_cvt_pk_bf16_f32 v122, v12, v13\n" \
    "v_cvt_pk_bf16_f32 v123, v14, v15\n" \
    "v_mfma_f32_16x16x32_bf16 v[8:11], v[80:83], v[76:79], 0\n" \
    "v_exp_f32 v0, v0\n" \
    "v_exp_f32 v1, v1\n" \
    "v_mfma_f32_16x16x32_bf16 v[12:15], v[84:87], v[76:79], 0\n" \
    "ds_read_b128 v[104:107], v124 offset:6144\n" \
    "v_exp_f32 v2, v2\n" \
    "v_exp_f32 v3, v3\n" \
    "v_mfma_f32_16x16x32_bf16 v[24:27], v[88:91], v[120:123], v[24:27]\n" \
    "v_cvt_pk_bf16_f32 v116, v0, v1\n" \
    "v_exp_f32 v4, v4\n" \
    "v_exp_f32 v5, v5\n" \
    "v_mfma_f32_16x16x32_bf16 v[28:31], v[92:95], v[120:123], v[28:31]\n" \
    "s_mov_b32 m0, s20\n" \
    "s_add_u32 s20, s20, 0x400\n" \
    "buffer_load_dwordx4 v126, %[srd], s21 offen lds\n" \
    "s_add_u32 s21, s21, 0x400\n" \
    "s_add_u32 s22, s22, 0x4000\n" \
    "s_and_b32 s22, s22, 0xffff\n" \
    "s_add_u32 s20, s22, %[ldst]\n" \
    "s_add_u32 s21, s21, 0x3000\n" \
    "v_cvt_pk_bf16_f32 v117, v2, v3\n" \
    "v_exp_f32 v6, v6\n" \
    "v_exp_f32 v7, v7\n" \
    "v_mfma_f32_16x16x32_bf16 v[52:55], v[112:115], v[120:123], v[52:55]\n" \
    "v_cvt_pk_bf16_f32 v118, v4, v5\n" \
    "v_cvt_pk_bf16_f32 v119, v6, v7\n" \
    "s_waitcnt lgkmcnt(1)\n" \
    "v_mfma_f32_16x16x32_bf16 v[0:3], v[96:99], v[64:67], 0\n" \
    "v_exp_f32 v8, v8\n" \
    "v_exp_f32 v9, v9\n" \
    "v_mfma_f32_16x16x32_bf16 v[4:7], v[100:103], v[64:67], 0\n" \
    "ds_read_b128 v[108:111], v124 offset:7168\n" \
    "v_exp_f32 v10, v10\n" \
    "v_exp_f32 v11, v11\n" \
    "v_mfma_f32_16x16x32_bf16 v[32:35], v[88:91], v[116:119], v[32:35]\n" \
    "v_cvt_pk_bf16_f32 v120, v8, v9\n" \
    "v_exp_f32 v12, v12\n" \
    "v_exp_f32 v13, v13\n" \
    "v_mfma_f32_16x16x32_bf16 v[36:39], v[92:95], v[116:119], v[36:39]\n" \
    "v_cvt_pk_bf16_f32 v121, v10, v11\n" \
    "v_exp_f32 v14, v14\n" \
    "v_exp_f32 v15, v15\n" \
    "v_mfma_f32_16x16x32_bf16 v[56:59], v[112:115], v[116:119], v[56:59]\n" \
    "v_cvt_pk_bf16_f32 v122, v12, v13\n" \
    "v_cvt_pk_bf16_f32 v123, v14, v15\n" \
    "v_mfma_f32_16x16x32_bf16 v[8:11], v[96:99], v[68:71], 0\n" \
    "v_exp_f32 v0, v0\n" \
    "v_exp_f32 v1, v1\n" \
    "v_mfma_f32_16x16x32_bf16 v[12:15], v[100:103], v[68:71], 0\n" \
    "ds_read_b128 v[80:83], v124 offset:8192\n" \
    "v_exp_f32 v2, v2\n" \
    "v_exp_f32 v3, v3\n" \
    "v_mfma_f32_16x16x32_bf16 v[40:43], v[88:91], v[120:123], v[40:43]\n" \
    "v_cvt_pk_bf16_f32 v116, v0, v1\n" \
    "v_exp_f32 v4, v4\n" \
    "v_exp_f32 v5, v5\n" \
    "v_mfma_f32_16x16x32_bf16 v[44:47], v[92:95], v[120:123], v[44:47]\n" \
    "v_cvt_pk_bf16_f32 v117, v2, v3\n" \
    "v_exp_f32 v6, v6\n" \
    "v_exp_f32 v7, v7\n" \
    "v_mfma_f32_16x16x32_bf16 v[60:63], v[112:115], v[120:123], v[60:63]\n" \
    "v_cvt_pk_bf16_f32 v118, v4, v5\n" \
    "v_cvt_pk_bf16_f32 v119, v6, v7\n" \
    "s_waitcnt lgkmcnt(1)\n" \
    "v_mfma_f32_16x16x32_bf16 v[0:3], v[96:99], v[72:75], 0\n" \
    "v_exp_f32 v8, v8\n" \
    "v_exp_f32 v9, v9\n" \
    "v_mfma_f32_16x16x32_bf16 v[4:7], v[100:103], v[72:75], 0\n" \
    "ds_read_b128 v[84:87], v124 offset:9216\n" \
    "v_exp_f32 v10, v10\n" \
    "v_exp_f32 v11, v11\n" \
    "v_mfma_f32_16x16x32_bf16 v[16:19], v[104:107], v[116:119], v[16:19]\n" \
    "v_cvt_pk_bf16_f32 v120, v8, v9\n" \
    "v_exp_f32 v12, v12\n" \
    "v_exp_f32 v13, v13\n" \
    "v_mfma_f32_16x16x32_bf16 v[20:23], v[108:111], v[116:119], v[20:23]\n" \
    "v_cvt_pk_bf16_f32 v121, v10, v11\n" \
    "v_exp_f32 v14, v14\n" \
    "v_exp_f32 v15, v15\n" \
    "v_mfma_f32_16x16x32_bf16 v[48:51], v[112:115], v[116:119], v[48:51]\n" \
    "v_cvt_pk_bf16_f32 v122, v12, v13\n" \
    "v_cvt_pk_bf16_f32 v123, v14, v15\n" \
    "v_mfma_f32_16x16x32_bf16 v[8:11], v[96:99], v[76:79], 0\n" \
    "v_exp_f32 v0, v0\n" \
    "v_exp_f32 v1, v1\n" \
    "v_mfma_f32_16x16x32_bf16 v[12:15], v[100:103], v[76:79], 0\n" \
    "ds_read_b128 v[88:91], v124 offset:10240\n" \
    "v_exp_f32 v2, v2\n" \
    "v_exp_f32 v3, v3\n" \
    "v_mfma_f32_16x16x32_bf16 v[24:27], v[104:107], v[120:123], v[24:27]\n" \
    "v_cvt_pk_bf16_f32 v116, v0, v1\n" \
    "v_exp_f32 v4, v4\n" \
    "v_exp_f32 v5, v5\n" \
    "v_mfma_f32_16x16x32_bf16 v[28:31], v[108:111], v[120:123], v[28:31]\n" \
    "v_cvt_pk_bf16_f32 v117, v2, v3\n" \
    "v_exp_f32 v6, v6\n" \
    "v_exp_f32 v7, v7\n" \
    "v_mfma_f32_16x16x32_bf16 v[52:55], v[112:115], v[120:123], v[52:55]\n" \
    "v_cvt_pk_bf16_f32 v118, v4, v5\n" \
    "v_cvt_pk_bf16_f32 v119, v6, v7\n" \
    "s_waitcnt lgkmcnt(1)\n" \
    "v_mfma_f32_16x16x32_bf16 v[0:3], v[80:83], v[64:67], 0\n" \
    "v_exp_f32 v8, v8\n" \
    "v_exp_f32 v9, v9\n" \
    "v_mfma_f32_16x16x32_bf16 v[4:7], v[84:87], v[64:67], 0\n" \
    "ds_read_b128 v[92:95], v124 offset:11264\n" \
    "v_exp_f32 v10, v10\n" \
    "v_exp_f32 v11, v11\n" \
    "v_mfma_f32_16x16x32_bf16 v[32:35], v[104:107], v[116:119], v[32:35]\n" \
    "v_cvt_pk_bf16_f32 v120, v8, v9\n" \
    "v_exp_f32 v12, v12\n" \
    "v_exp_f32 v13, v13\n" \
    "v_mfma_f32_16x16x32_bf16 v[36:39], v[108:111], v[116:119], v[36:39]\n" \
    "v_cvt_pk_bf16_f32 v121, v10, v11\n" \
    "v_exp_f32 v14, v14\n" \
    "v_exp_f32 v15, v15\n" \
    "v_mfma_f32_16x16x32_bf16 v[56:59], v[112:115], v[116:119], v[56:59]\n" \
    "v_cvt_pk_bf16_f32 v122, v12, v13\n" \
    "v_cvt_pk_bf16_f32 v123, v14, v15\n" \
    "v_mfma_f32_16x16x32_bf16 v[8:11], v[80:83], v[68:71], 0\n" \
    "v_exp_f32 v0, v0\n" \
    "v_exp_f32 v1, v1\n" \
    "v_mfma_f32_16x16x32_bf16 v[12:15], v[84:87], v[68:71], 0\n" \
    "ds_read_b128 v[96:99], v124 offset:12288\n" \
    "v_exp_f32 v2, v2\n" \
    "v_exp_f32 v3, v3\n" \
    "v_mfma_f32_16x16x32_bf16 v[40:43], v[104:107], v[120:123], v[40:43]\n" \
    "v_cvt_pk_bf16_f32 v116, v0, v1\n" \
    "v_exp_f32 v4, v4\n" \
    "v_exp_f32 v5, v5\n" \
    "v_mfma_f32_16x16x32_bf16 v[44:47], v[108:111], v[120:123], v[44:47]\n" \
    "v_cvt_pk_bf16_f32 v117, v2, v3\n" \
    "v_exp_f32 v6, v6\n" \
    "v_exp_f32 v7, v7\n" \
    "v_mfma_f32_16x16x32_bf16 v[60:63], v[112:115], v[120:123], v[60:63]\n" \
    "v_cvt_pk_bf16_f32 v118, v4, v5\n" \
    "v_cvt_pk_bf16_f32 v119, v6, v7\n" \
    "s_waitcnt lgkmcnt(1)\n" \
    "v_mfma_f32_16x16x32_bf16 v[0:3], v[80:83], v[72:75], 0\n" \
    "v_exp_f32 v8, v8\n" \
    "v_exp_f32 v9, v9\n" \
    "v_mfma_f32_16x16x32_bf16 v[4:7], v[84:87], v[72:75], 0\n" \
    "ds_read_b128 v[100:103], v124 offset:13312\n" \
    "v_exp_f32 v10, v10\n" \
    "v_exp_f32 v11, v11\n" \
    "v_mfma_f32_16x16x32_bf16 v[16:19], v[88:91], v[116:119], v[16:19]\n" \
    "v_cvt_pk_bf16_f32 v120, v8, v9\n" \
    "v_exp_f32 v12, v12\n" \
    "v_exp_f32 v13, v13\n" \
    "v_mfma_f32_16x16x32_bf16 v[20:23], v[92:95], v[116:119], v[20:23]\n" \
    "v_cvt_pk_bf16_f32 v121, v10, v11\n" \
    "v_exp_f32 v14, v14\n" \
    "v_exp_f32 v15, v15\n" \
    "v_mfma_f32_16x16x32_bf16 v[48:51], v[112:115], v[116:119], v[48:51]\n" \
    "v_cvt_pk_bf16_f32 v122, v12, v13\n" \
    "v_cvt_pk_bf16_f32 v123, v14, v15\n" \
    "v_mfma_f32_16x16x32_bf16 v[8:11], v[80:83], v[76:79], 0\n" \
    "v_exp_f32 v0, v0\n" \
    "v_exp_f32 v1, v1\n" \
    "v_mfma_f32_16x16x32_bf16 v[12:15], v[84:87], v[76:79], 0\n" \
    "ds_read_b128 v[104:107], v124 offset:14336\n" \
    "v_exp_f32 v2, v2\n" \
    "v_exp_f32 v3, v3\n" \
    "v_mfma_f32_16x16x32_bf16 v[24:27], v[88:91], v[120:123], v[24:27]\n" \
    "v_cvt_pk_bf16_f32 v116, v0, v1\n" \
    "v_exp_f32 v4, v4\n" \
    "v_exp_f32 v5, v5\n" \
    "v_mfma_f32_16x16x32_bf16 v[28:31], v[92:95], v[120:123], v[28:31]\n" \
    "v_cvt_pk_bf16_f32 v117, v2, v3\n" \
    "v_exp_f32 v6, v6\n" \
    "v_exp_f32 v7, v7\n" \
    "v_mfma_f32_16x16x32_bf16 v[52:55], v[112:115], v[120:123], v[52:55]\n" \
    "v_cvt_pk_bf16_f32 v118, v4, v5\n" \
    "v_cvt_pk_bf16_f32 v119, v6, v7\n" \
    "s_waitcnt lgkmcnt(1)\n" \
    "v_mfma_f32_16x16x32_bf16 v[0:3], v[96:99], v[64:67], 0\n" \
    "v_exp_f32 v8, v8\n" \
    "v_exp_f32 v9, v9\n" \
    "v_mfma_f32_16x16x32_bf16 v[4:7], v[100:103], v[64:67], 0\n" \
    "ds_read_b128 v[108:111], v124 offset:15360\n" \
    "v_exp_f32 v10, v10\n" \
    "v_exp_f32 v11, v11\n" \
    "v_mfma_f32_16x16x32_bf16 v[32:35], v[88:91], v[116:119], v[32:35]\n" \
    "v_cvt_pk_bf16_f32 v120, v8, v9\n" \
    "v_exp_f32 v12, v12\n" \
    "v_exp_f32 v13, v13\n" \
    "v_mfma_f32_16x16x32_bf16 v[36:39], v[92:95], v[116:119], v[36:39]\n" \
    "v_cvt_pk_bf16_f32 v121, v10, v11\n" \
    "v_exp_f32 v14, v14\n" \
    "v_exp_f32 v15, v15\n" \
    "v_mfma_f32_16x16x32_bf16 v[56:59], v[112:115], v[116:119], v[56:59]\n" \
    "v_cvt_pk_bf16_f32 v122, v12, v13\n" \
    "v_cvt_pk_bf16_f32 v123, v14, v15\n" \
    "s_waitcnt vmcnt(4)\n" \
    "s_barrier\n" \
    "v_mfma_f32_16x16x32_bf16 v[8:11], v[96:99], v[68:71], 0\n" \
    "v_exp_f32 v0, v0\n" \
    "v_exp_f32 v1, v1\n" \
    "v_mfma_f32_16x16x32_bf16 v[12:15], v[100:103], v[68:71], 0\n" \
    "ds_read_b128 v[80:83], v125 offset:0\n" \
    "v_exp_f32 v2, v2\n" \
    "v_exp_f32 v3, v3\n" \
    "v_mfma_f32_16x16x32_bf16 v[40:43], v[88:91], v[120:123], v[40:43]\n" \
    "v_cvt_pk_bf16_f32 v116, v0, v1\n" \
    "v_exp_f32 v4, v4\n" \
    "v_exp_f32 v5, v5\n" \
    "v_mfma_f32_16x16x32_bf16 v[44:47], v[92:95], v[120:123], v[44:47]\n" \
    "s_mov_b32 m0, s20\n" \
    "s_add_u32 s20, s20, 0x400\n" \
    "buffer_load_dwordx4 v126, %[srd], s21 offen lds\n" \
    "s_add_u32 s21, s21, 0x400\n" \
    "v_cvt_pk_bf16_f32 v117, v2, v3\n" \
    "v_exp_f32 v6, v6\n" \
    "v_exp_f32 v7, v7\n" \
    "v_mfma_f32_16x16x32_bf16 v[60:63], v[112:115], v[120:123], v[60:63]\n" \
    "v_cvt_pk_bf16_f32 v118, v4, v5\n" \
    "v_cvt_pk_bf16_f32 v119, v6, v7\n" \
    "s_waitcnt lgkmcnt(1)\n" \
    "v_mfma_f32_16x16x32_bf16 v[0:3], v[96:99], v[72:75], 0\n" \
    "v_exp_f32 v8, v8\n" \
    "v_exp_f32 v9, v9\n" \
    "v_mfma_f32_16x16x32_bf16 v[4:7], v[100:103], v[72:75], 0\n" \
    "ds_read_b128 v[84:87], v125 offset:1024\n" \
    "v_exp_f32 v10, v10\n" \
    "v_exp_f32 v11, v11\n" \
    "v_mfma_f32_16x16x32_bf16 v[16:19], v[104:107], v[116:119], v[16:19]\n" \
    "v_cvt_pk_bf16_f32 v120, v8, v9\n" \
    "v_exp_f32 v12, v12\n" \
    "v_exp_f32 v13, v13\n" \
    "v_mfma_f32_16x16x32_bf16 v[20:23], v[108:111], v[116:119], v[20:23]\n" \
    "v_cvt_pk_bf16_f32 v121, v10, v11\n" \
    "v_exp_f32 v14, v14\n" \
    "v_exp_f32 v15, v15\n" \
    "v_mfma_f32_16x16x32_bf16 v[48:51], v[112:115], v[116:119], v[48:51]\n" \
    "v_cvt_pk_bf16_f32 v122, v12, v13\n" \
    "v_cvt_pk_bf16_f32 v123, v14, v15\n" \
    "v_mfma_f32_16x16x32_bf16 v[8:11], v[96:99], v[76:79], 0\n" \
    "v_exp_f32 v0, v0\n" \
    "v_exp_f32 v1, v1\n" \
    "v_mfma_f32_16x16x32_bf16 v[12:15], v[100:103], v[76:79], 0\n" \
    "ds_read_b128 v[88:91], v125 offset:2048\n" \
    "v_exp_f32 v2, v2\n" \
    "v_exp_f32 v3, v3\n" \
    "v_mfma_f32_16x16x32_bf16 v[24:27], v[104:107], v[120:123], v[24:27]\n" \
    "v_cvt_pk_bf16_f32 v116, v0, v1\n" \
    "v_exp_f32 v4, v4\n" \
    "v_exp_f32 v5, v5\n" \
    "v_mfma_f32_16x16x32_bf16 v[28:31], v[108:111], v[120:123], v[28:31]\n" \
    "s_mov_b32 m0, s20\n" \
    "s_add_u32 s20, s20, 0x400\n" \
    "buffer_load_dwordx4 v126, %[srd], s21 offen lds\n" \
    "s_add_u32 s21, s21, 0x400\n" \
    "v_cvt_pk_bf16_f32 v117, v2, v3\n" \
    "v_exp_f32 v6, v6\n" \
    "v_exp_f32 v7, v7\n" \
    "v_mfma_f32_16x16x32_bf16 v[52:55], v[112:115], v[120:123], v[52:55]\n" \
    "v_cvt_pk_bf16_f32 v118, v4, v5\n" \
    "v_cvt_pk_bf16_f32 v119, v6, v7\n" \
    "s_waitcnt lgkmcnt(1)\n" \
    "v_mfma_f32_16x16x32_bf16 v[0:3], v[80:83], v[64:67], 0\n" \
    "v_exp_f32 v8, v8\n" \
    "v_exp_f32 v9, v9\n" \
    "v_mfma_f32_16x16x32_bf16 v[4:7], v[84:87], v[64:67], 0\n" \
    "ds_read_b128 v[92:95], v125 offset:3072\n" \
    "v_exp_f32 v10, v10\n" \
    "v_exp_f32 v11, v11\n" \
    "v_mfma_f32_16x16x32_bf16 v[32:35], v[104:107], v[116:119], v[32:35]\n" \
    "v_cvt_pk_bf16_f32 v120, v8, v9\n" \
    "v_exp_f32 v12, v12\n" \
    "v_exp_f32 v13, v13\n" \
    "v_mfma_f32_16x16x32_bf16 v[36:39], v[108:111], v[116:119], v[36:39]\n" \
    "v_cvt_pk_bf16_f32 v121, v10, v11\n" \
    "v_exp_f32 v14, v14\n" \
    "v_exp_f32 v15, v15\n" \
    "v_mfma_f32_16x16x32_bf16 v[56:59], v[112:115], v[116:119], v[56:59]\n" \
    "v_cvt_pk_bf16_f32 v122, v12, v13\n" \
    "v_cvt_pk_bf16_f32 v123, v14, v15\n" \
    "v_mov_b32 v124, v125\n" \
    "s_add_u32 s24, s24, 0x4000\n" \
    "s_and_b32 s24, s24, 0xffff\n" \
    "v_add_u32 v125, s24, v127\n" \
    "s_sub_u32 s23, s23, 1\n" \
    "s_cmp_lg_u32 s23, 0\n" \
    "s_cbranch_scc1 ATT_LOOP_%=\n" \
    "v_mfma_f32_16x16x32_bf16 v[40:43], v[104:107], v[120:123], v[40:43]\n" \
    "v_mfma_f32_16x16x32_bf16 v[44:47], v[108:111], v[120:123], v[44:47]\n" \
    "v_mfma_f32_16x16x32_bf16 v[60:63], v[112:115], v[120:123], v[60:63]\n" \
    "s_waitcnt vmcnt(0) lgkmcnt(0)\n" \
    "s_nop 15\n"

constexpr int Q_NS = 3, Q_SLOT = 24576;
__device__ __forceinline__ void attention(int b, const char* x, const char* wq, char* ws, lds_ptr lds, int w, int lane) {
    const int xc = b & 7, t = b >> 3, st128 = (xc >> 1) * 4 + (t & 3), hp = (xc & 1) * 8 + (t >> 2);
    const int head = 2 * hp + (w & 1), s0 = st128 * 128 + (w >> 1) * 64, g = lane >> 4, c = lane & 15;
    const u32x4 srd = make_srd(ws + WS_KV, S * 128);
    const u32 voff = w * 4096 + lane * 16, ldst = (u32)(uintptr_t)lds + w * 4096;
    auto dma = [&](int st, int p0, int p1) {
#pragma unroll
        for (int p = p0; p < p1; ++p) dma16(ldst + ((st + 2) & 3) * KV_STAGE + p * 1024, voff, srd, st * KV_STAGE + p * 1024);
    };
    __builtin_amdgcn_s_setprio(2);
    float gate[4];
#pragma unroll
    for (int i = 0; i < 4; ++i) asm volatile("global_load_dword %0, %1, off" : "=v"(gate[i]) : "v"(ws + WS_G + (s0 + i * 16 + c) * 4) : "memory");
    f32x4 kb2;
    asm volatile("global_load_dwordx4 %0, %1, off" : "=v"(kb2) : "v"(ws + WS_BND + lane * 16) : "memory");
    u32x4 q[4];
    {
        OpTr<64, D * 2, 2, true, 2> a; a.init(wq, D * D * 2, hp * 64, 0, lds, w, (w & 1) * 2, lane);
        OpRow bb; bb.init(x, S * D * 2, st128 * 128, lds + a.BYTES, w, (w >> 1) * 64, lane);
        Engine<Q_NS, 2, 4, 2> e;
        static_assert((D / 64 - 1) % Q_NS == 0 && Q_SLOT <= 2 * KV_STAGE, "the last Q slot lies below the first two K/V stages");
        gemm_run<Q_SLOT, D / 64, 2>(e, a, bb, [] {}, [](int) {}, [&](auto p) { if constexpr (p.v == Rounds<Q_NS, D / 64>::TAIL - 2) { dma(0, 0, 4); dma(1, 0, 4); } });
        wait_bar<4>();
        dma(2, 0, 2);
#pragma unroll
        for (int i = 0; i < 4; ++i) q[i] = pack8(e.acc[0][i] * QSCALE, e.acc[1][i] * QSCALE);
    }
    PIN(kb2);
    const float k2 = wave_max(fmaxf(fmaxf(kb2[0], kb2[1]), fmaxf(kb2[2], kb2[3])));
    bool ok = true;
#pragma unroll
    for (int i = 0; i < 4; ++i) {
        float n = 0.f;
#pragma unroll
        for (int m = 0; m < 4; ++m) { const float a = bf_lo(q[i][m]), h = bf_hi(q[i][m]); n += a * a + h * h; }
        ok = ok && group_sum(n) * k2 * 1.02f <= SAFE_LOG2 * SAFE_LOG2;
        PIN(gate[i]);
    }
    const bool safe = __all(ok);
    __builtin_amdgcn_s_setprio(0);
    f32x4 o[8];
#pragma unroll
    for (int i = 0; i < 8; ++i) o[i] = f32x4{};
    float lsum[4];
    const lds_ptr base = lds + lane * 16;
    const f32x4 z = {};
    if (safe) {
        const u32 one = c == 0 ? 0x3f803f80u : 0u;
        const u32x4 ones = {one, one, one, one};
        const lds_ptr first = base + 2 * KV_STAGE;
        u32x4 ka = lds_rd128(first), kb = lds_rd128(first + 1024), v0 = lds_rd128(first + 2048), v1 = lds_rd128(first + 3072);
        f32x4 sa = M16(ka, q[0], z), sb = M16(kb, q[0], z);
        f32x16 oa = {}, ob = {}, l = {};
        u32x4 vz0 = {}, vz1 = {}, pz = {};
        u32 cur = (u32)(uintptr_t)first, nxt = cur + KV_STAGE;
        asm volatile("s_nop 7\n" ATT_LOOP_ASM
                     : "+{v[0:3]}"(sa), "+{v[4:7]}"(sb), "+{v[16:31]}"(oa), "+{v[32:47]}"(ob), "+{v[48:63]}"(l), "+{v[80:83]}"(ka), "+{v[84:87]}"(kb),
                       "+{v[88:91]}"(v0), "+{v[92:95]}"(v1), "+{v[104:107]}"(vz0), "+{v[108:111]}"(vz1), "+{v[120:123]}"(pz), "+{v124}"(cur), "+{v125}"(nxt)
                     : "{v[64:67]}"(q[0]), "{v[68:71]}"(q[1]), "{v[72:75]}"(q[2]), "{v[76:79]}"(q[3]), "{v[112:115]}"(ones), "{v126}"(voff),
                       "{v127}"((u32)(uintptr_t)base), [srd] "s"(srd), [ldst] "s"(ldst)
                     : "v8", "v9", "v10", "v11", "v12", "v13", "v14", "v15", "v96", "v97", "v98", "v99", "v100", "v101", "v102", "v103",
                       "v116", "v117", "v118", "v119", "s20", "s21", "s22", "s23", "s24", "scc", "memory");
#pragma unroll
        for (int i = 0; i < 4; ++i) {
            lsum[i] = __shfl(l[4 * i], c);
            o[i] = f32x4{oa[4 * i], oa[4 * i + 1], oa[4 * i + 2], oa[4 * i + 3]};
            o[4 + i] = f32x4{ob[4 * i], ob[4 * i + 1], ob[4 * i + 2], ob[4 * i + 3]};
        }
    } else {
        float mx[4];
#pragma unroll
        for (int i = 0; i < 4; ++i) { mx[i] = -__builtin_inff(); lsum[i] = 0.f; }
        dma(2, 2, 4);
        for (int st = 0; st < KV_NSTAGE; ++st) {
            wait_bar<4>();
            dma(st + 3, 0, 4);
#pragma unroll 1
            for (int kb = 0; kb < 4; ++kb) {
                const lds_ptr p = base + ((st + 2) & 3) * KV_STAGE + kb * 4096;
                const u32x4 k0 = lds_rd128(p), k1 = lds_rd128(p + 1024), v0 = lds_rd128(p + 2048), v1 = lds_rd128(p + 3072);
#pragma unroll
                for (int i = 0; i < 4; ++i) {
                    f32x4 sa = M16(k0, q[i], z), sb = M16(k1, q[i], z);
                    float m = mx[i];
#pragma unroll
                    for (int r = 0; r < 4; ++r) m = fmaxf(m, fmaxf(sa[r], sb[r]));
                    m = fmaxf(m, __shfl_xor(m, 16)); m = fmaxf(m, __shfl_xor(m, 32));
                    const float alpha = EX2(mx[i] - m);
                    mx[i] = m;
                    float t = 0.f;
#pragma unroll
                    for (int r = 0; r < 4; ++r) { sa[r] = EX2(sa[r] - m); sb[r] = EX2(sb[r] - m); t += sa[r] + sb[r]; }
                    lsum[i] = lsum[i] * alpha + t;
                    const u32x4 pp = pack8(sa, sb);
                    o[2 * i] = M16(v0, pp, o[2 * i] * alpha);
                    o[2 * i + 1] = M16(v1, pp, o[2 * i + 1] * alpha);
                }
            }
        }
        asm volatile("s_waitcnt vmcnt(0)" ::: "memory");
#pragma unroll
        for (int i = 0; i < 4; ++i) lsum[i] = group_sum(lsum[i]);
    }
#pragma unroll
    for (int i = 0; i < 4; ++i) {
        const float r = gate[i] / lsum[i];
        store_wt(ws + WS_OF, S * D * 2, (u32)((head * 4 + g) * S + s0 + i * 16 + c) * 16u, pack8(o[2 * i] * r, o[2 * i + 1] * r));
    }
    publish(ws, CNT_O + st128);
}

__device__ __forceinline__ f32x2 gelu_tanh(f32x2 x) {
    constexpr float C0 = -2.f * 0.7978845608028654f * 1.4426950408889634f, C1 = C0 * 0.044715f;
    const f32x2 y = x * __builtin_elementwise_fma(x * x, f32x2{C1, C1}, f32x2{C0, C0});
    const f32x2 d = f32x2{EX2(y[0]), EX2(y[1])} + 1.f;
    return x * f32x2{__builtin_amdgcn_rcpf(d[0]), __builtin_amdgcn_rcpf(d[1])};
}

__device__ __forceinline__ void gemm1_tile(int b, const void* w1, const char* b1, char* ws, lds_ptr lds, int w, int lane) {
    const int x = b & 7, t = b >> 3;
    const int dt = (x & 1) * 4 + (t & 3), d0 = dt * 128, f0 = ((x >> 1) * 7 + (t >> 2)) * 128;
    const int g = lane >> 4, c = lane & 15;
    u32x2 bias[8];
#pragma unroll
    for (int j = 0; j < 8; ++j) bias[j] = *(const u32x2*)(b1 + (f0 + j * 16 + g * 4) * 2);
    OpReg<2, D, false> a; a.init(ws + WS_HF, S * D * 2, d0 + w * 32, 0, lane);
    OpTr<128, F * 2, 1, false, 8> bb; bb.init(w1, S * F * 2, f0, 0, lds, w, 0, lane);
    Engine<G1_NS, 2, 8> e;
    const u32 scratch = (u32)(uintptr_t)lds + G1_NS * 8192 + w * 256;
    gemm_run<8192, S / 32, 0>(e, a, bb, [] {}, [&](int r) { if (r == 1) touch<S * F * 2 - W1_PRE, MID_G1, 256>((const char*)w1 + W1_PRE, b, scratch); }, [](auto) {});
    auto& acc = e.acc;
#pragma unroll
    for (int jp = 0; jp < 4; ++jp)
#pragma unroll
        for (int i = 0; i < 2; ++i) {
            f32x4 u[2];
#pragma unroll
            for (int h = 0; h < 2; ++h) {
                const u32x2 bv = bias[2 * jp + h];
                const f32x4 v = acc[i][2 * jp + h] + f32x4{bf_lo(bv[0]), bf_hi(bv[0]), bf_lo(bv[1]), bf_hi(bv[1])};
                const f32x2 lo = gelu_tanh(f32x2{v[0], v[1]}), hi = gelu_tanh(f32x2{v[2], v[3]});
                u[h] = f32x4{lo[0], lo[1], hi[0], hi[1]};
            }
            store_wt(ws + WS_UF, F * D * 2, (u32)(((f0 / 32 + jp) * 4 + g) * D + d0 + w * 32 + i * 16 + c) * 16u, pack8(u[0], u[1]));
        }
    publish(ws, CNT_U + dt * 2 + (x >> 2));
}

constexpr int P1_NS = 8, P1_BASE = 8192, P2_NS = 6, P2_SLOT = 12288;

__device__ __forceinline__ void post_tile(int b, const char* w2, const char* b2, const char* wo, char* ws, char* out, lds_ptr lds, int w, int lane) {
    const int x = b & 7, t = b >> 3, h = t & 1;
    const int s0 = ((x >> 1) * 8 + (t & 7)) * 64, dt = (x & 1) * 4 + (t >> 3), d0 = dt * 128;
    const int g = lane >> 4, c = lane & 15;
    constexpr int N1 = F / 64, N2 = D / 32;
    static_assert(N1 % P1_NS == 0 && P1_BASE + (P1_NS - 1) * 8192 >= (P2_NS - 1) * P2_SLOT, "the last w2 slot lies in the last wo slot");
    float omg[4], bs[4];
#pragma unroll
    for (int j = 0; j < 4; ++j) {
        const int s = s0 + j * 16 + c;
        asm volatile("global_load_dword %0, %1, off" : "=v"(omg[j]) : "v"(ws + WS_G + s * 4) : "memory");
        asm volatile("global_load_ushort %0, %1, off" : "=v"(bs[j]) : "v"(b2 + s * 2) : "memory");
    }
    OpTr<128, D * 2, 1, false, 2> a2; a2.init(wo, D * D * 2, d0, 0, lds, w, w * 2, lane);
    OpFrag<64, S> b2o; b2o.init(ws + WS_OF, S * D * 2, s0, lds + 8192, w, 0, lane);
    Engine<P2_NS, 2, 4> e;
    auto& acc = e.acc;
    u32 seen = 0;
    {
        OpReg<2, D, true> a1; a1.init(ws + WS_UF, F * D * 2, d0 + w * 32, h * N1, lane);
        OpTr<128, S * 2, 1, false, 8> b1; b1.init(w2, F * S * 2, s0 & ~127, h * N1, lds + P1_BASE, w, 4 * h, lane);
        Engine<P1_NS, 2, 8> e1;
        constexpr int TAIL = Rounds<P1_NS, N1>::TAIL;
        gemm_run<8192, N1, 2>(e1, a1, b1,
            [&] { if (w == 0) await(ws, CNT_U + dt * 2 + h, F / 256); __builtin_amdgcn_s_barrier(); },
            [&](int r) { if (r == Rounds<P1_NS, N1>::N - 1) sample(seen, ws, CNT_O + (s0 >> 7)); },
            [&](auto p) { if constexpr (p.v == TAIL - 2) { confirm(seen, ws, CNT_O + (s0 >> 7), 16); gemm_fill<P2_SLOT>(a2, b2o, 0, P2_NS - 1); } });
#pragma unroll
        for (int j = 0; j < 4; ++j) {
            PIN(omg[j]); PIN(bs[j]);
            omg[j] = bf_round(1.f - omg[j]); bs[j] = __uint_as_float(__float_as_uint(bs[j]) << 16);
#pragma unroll
            for (int i = 0; i < 2; ++i) {
                store_wt(ws + WS_X, 256u << 15, (u32)(((b * 4 + w) * 8 + i * 4 + j) * 64 + lane) * 16u, __builtin_bit_cast(u32x4, e1.acc[i][4 + j]));
                acc[i][j] = (e1.acc[i][j] + bs[j]) * omg[j];
            }
        }
    }
    constexpr int PPW = 3, XW = 8;
    wait_bar<(P2_NS - 2) * PPW + XW>();
    gemm_fill<P2_SLOT>(a2, b2o, P2_NS - 1, P2_NS);
    gemm_first(e, a2, b2o);
    gemm_steps<0, P2_NS, MANY, P2_SLOT, 2, XW>(e, a2, b2o, P2_NS, [&](auto p) { if constexpr (p.v == P2_NS - 2) signal(ws, CNT_X + b); });
    for (int r = 1; r < 3; ++r) gemm_steps<0, P2_NS, MANY, P2_SLOT, 2, 0>(e, a2, b2o, (r + 1) * P2_NS, [](auto) {});
    seen = 0;
    u32x4 got[8];
    const u32x4 xsrd = make_srd(ws + WS_X, 256u << 15);
    gemm_steps<0, N2 - 3 * P2_NS, N2 - 3 * P2_NS, P2_SLOT, 2, 0>(e, a2, b2o, 4 * P2_NS, [&](auto p) {
        if constexpr (p.v == 0) sample(seen, ws, CNT_X + (b ^ 8));
        if constexpr (p.v == P2_NS - 2) {
            confirm(seen, ws, CNT_X + (b ^ 8), 1);
#pragma unroll
            for (int i = 0; i < 8; ++i) {
                got[i] = u32x4{};
                load_coh(got[i], lane * 16, xsrd, (u32)(((b ^ 8) * 4 + w) * 8 + i) * 1024u);
            }
        }
    });
    asm volatile("s_waitcnt vmcnt(0)" ::: "memory");
#pragma unroll
    for (int i = 0; i < 8; ++i) { PIN(got[i]); acc[i >> 2][i & 3] += __builtin_bit_cast(f32x4, got[i]) * omg[i & 3]; }
#pragma unroll
    for (int j = 0; j < 4; ++j) {
        const auto r0 = __builtin_amdgcn_permlane16_swap(cvtpk(acc[0][j][0], acc[0][j][1]), cvtpk(acc[1][j][0], acc[1][j][1]), false, false);
        const auto r1 = __builtin_amdgcn_permlane16_swap(cvtpk(acc[0][j][2], acc[0][j][3]), cvtpk(acc[1][j][2], acc[1][j][3]), false, false);
        const int s = s0 + j * 16 + c, d = d0 + w * 32 + (g & 1) * 16 + (g >> 1) * 8;
        *(u32x4*)(out + ((size_t)s * D + d) * 2) = u32x4{r0[0], r1[0], r0[1], r1[1]};
    }
}

constexpr int MAIN_LDS = Q_NS * Q_SLOT;
static_assert(MAIN_LDS >= P2_NS * P2_SLOT && MAIN_LDS >= P1_BASE + P1_NS * 8192 && MAIN_LDS >= 4 * KV_STAGE && 2 * MAIN_LDS <= 160 * 1024, "two workgroups per CU");

__global__ void __launch_bounds__(256, 2) k_main(const char* __restrict__ x, const char* __restrict__ wq, const char* __restrict__ w1, const char* __restrict__ b1, const char* __restrict__ w2, const char* __restrict__ b2,
                                                 const char* __restrict__ wo, char* __restrict__ ws, char* __restrict__ out) {
    extern __shared__ __attribute__((aligned(16))) char smem[];
    const int lane = threadIdx.x & 63, w = __builtin_amdgcn_readfirstlane(threadIdx.x >> 6), b = blockIdx.x;
    asm volatile("main_code_begin:");
    if (b < MID_ATT) {
        attention(b, x, wq, ws, (lds_ptr)smem, w, lane);
    } else {
        const int gb = b - MID_ATT;
        __builtin_amdgcn_s_setprio(1);
        if (gb < MID_G1) gemm1_tile(gb, w1, b1, ws, (lds_ptr)smem, w, lane);
        else {
            const u32 scratch = (u32)(uintptr_t)(lds_ptr)smem + w * 256;
            for (int i = 0; i < 5; ++i) __builtin_amdgcn_s_sleep(64);
            for (int i = 0; i < 4; ++i) {
                touch<F * S * 2, MID_PF * 4, 256>(w2, (gb - MID_G1) * 4 + i, scratch);
                asm volatile("s_waitcnt vmcnt(0)" ::: "memory");
            }
            touch<D * D * 2, MID_PF, 256>(wo, gb - MID_G1, scratch);
            wait_bar<0>();
        }
        __builtin_amdgcn_s_setprio(0);
        post_tile(gb, w2, b2, wo, ws, out, (lds_ptr)smem, w, lane);
    }
    asm volatile("main_code_end:");
}

extern "C" void kernel_launch(void* const* d_in, const int* in_sizes, int n_in, void* d_out, int out_size, void* d_ws, size_t ws_size,
                              hipStream_t stream) {
    const char* x = (const char*)d_in[0];
    const char *wq = (const char*)d_in[1], *wk = (const char*)d_in[2], *wv = (const char*)d_in[3], *wo = (const char*)d_in[4];
    const char *w1 = (const char*)d_in[6], *b1 = (const char*)d_in[7], *w2 = (const char*)d_in[8], *b2 = (const char*)d_in[9];
    const char* gw = (const char*)d_in[10];
    char* ws = (char*)d_ws;
    (void)hipFuncSetAttribute((const void*)k_pre, hipFuncAttributeMaxDynamicSharedMemorySize, PRE_LDS);
    (void)hipFuncSetAttribute((const void*)k_main, hipFuncAttributeMaxDynamicSharedMemorySize, MAIN_LDS);
    k_pre<<<PRE_KV + PRE_LN, 512, PRE_LDS, stream>>>(x, wq, wk, wv, gw, w1, w2, wo, (const char*)d_out, ws);
    k_main<<<MID_ATT + MID_G1 + MID_PF, 256, MAIN_LDS, stream>>>(x, wq, w1, b1, w2, b2, wo, ws, (char*)d_out);
}
```

```cpp
#include <hip/hip_runtime.h>
#include <stdint.h>

typedef unsigned u32;
typedef u32 u32x4 __attribute__((ext_vector_type(4)));
typedef u32 u32x2 __attribute__((ext_vector_type(2)));
typedef float f32x2 __attribute__((ext_vector_type(2)));
typedef float f32x4 __attribute__((ext_vector_type(4)));
typedef float f32x16 __attribute__((ext_vector_type(16)));
typedef __bf16 bf16x2 __attribute__((ext_vector_type(2)));
typedef __bf16 bf16x8 __attribute__((ext_vector_type(8)));
typedef short s16x4 __attribute__((ext_vector_type(4)));
#define LDS __attribute__((address_space(3)))
typedef LDS char* lds_ptr;

constexpr int S = 2048, D = 1024, F = 3584;
constexpr u32 WS_HF = 0, WS_OF = 8u << 20, WS_UF = 12u << 20, WS_KV = 20u << 20;
constexpr u32 WS_G = WS_KV + (256u << 10), WS_BND = WS_G + (8u << 10), WS_CNT = WS_BND + (4u << 10);
constexpr u32 WS_X = 24u << 20;
constexpr int CNT_U = 0, CNT_O = 16, CNT_X = 32, CNT_N = 288, CNT_STRIDE = 256;
constexpr float QSCALE = 0.17677669529663687f * 1.4426950408889634f;
constexpr float SAFE_LOG2 = 60.f;
constexpr float SAFE_V = 1.0e12f;

#define SB() __builtin_amdgcn_sched_barrier(0)
#define PIN(x) asm volatile("" : "+v"(x))
#define M16(a, b, c) __builtin_amdgcn_mfma_f32_16x16x32_bf16(__builtin_bit_cast(bf16x8, a), __builtin_bit_cast(bf16x8, b), c, 0, 0, 0)
__device__ __forceinline__ void mma16(f32x4& c, const u32x4& a, const u32x4& b) { asm volatile("v_mfma_f32_16x16x32_bf16 %0, %1, %2, %0" : "+v"(c) : "v"(a), "v"(b)); }
__device__ __forceinline__ void mfma_settle() { asm volatile("s_nop 7\n\ts_nop 1" ::: "memory"); SB(); }
#define EX2(x) __builtin_amdgcn_exp2f(x)

__device__ __forceinline__ u32 cvtpk(float lo, float hi) { f32x2 v = {lo, hi}; return __builtin_bit_cast(u32, __builtin_convertvector(v, bf16x2)); }
__device__ __forceinline__ float bf_lo(u32 v) { return __uint_as_float(v << 16); }
__device__ __forceinline__ float bf_hi(u32 v) { return __uint_as_float(v & 0xffff0000u); }
__device__ __forceinline__ float bf_round(float x) { return bf_lo(cvtpk(x, x)); }
__device__ __forceinline__ u32x4 pack8(const f32x4& a, const f32x4& b) { return u32x4{cvtpk(a[0], a[1]), cvtpk(a[2], a[3]), cvtpk(b[0], b[1]), cvtpk(b[2], b[3])}; }

__device__ __forceinline__ u32x4 make_srd(const void* p, u32 bytes) {
    const uint64_t a = (uint64_t)p;
    return u32x4{(u32)a, (u32)(a >> 32) & 0xffffu, bytes, 0x00020000u};
}
__device__ __forceinline__ void dma16(u32 lds, u32 voff, const u32x4& srd, u32 soff) {
    asm volatile("s_mov_b32 m0, %0\n\ts_nop 0\n\tbuffer_load_dwordx4 %1, %2, %3 offen lds" ::"s"(lds), "v"(voff), "s"(srd), "s"(soff) : "memory");
}
__device__ __forceinline__ void dma16_coh(u32 lds, u32 voff, const u32x4& srd, u32 soff) {
    asm volatile("s_mov_b32 m0, %0\n\ts_nop 0\n\tbuffer_load_dwordx4 %1, %2, %3 offen sc1 lds" ::"s"(lds), "v"(voff), "s"(srd), "s"(soff) : "memory");
}
template <int N> __device__ __forceinline__ void wait_bar() {
    __builtin_amdgcn_s_waitcnt(0xc07f);
    asm volatile("s_waitcnt vmcnt(%0)\n\ts_barrier" ::"n"(N) : "memory");
}
__device__ __forceinline__ u32x4 lds_rd128(lds_ptr p) { return *(LDS const u32x4*)p; }
__device__ __forceinline__ u32x2 lds_rdtr(lds_ptr p) { return __builtin_bit_cast(u32x2, __builtin_amdgcn_ds_read_tr16_b64_v4i16((LDS s16x4*)p)); }

typedef __attribute__((address_space(1))) u32 gu32;
__device__ __forceinline__ void store_wt(char* base, u32 bytes, u32 off, const u32x4& v) {
    __builtin_amdgcn_raw_buffer_store_b128(v, __builtin_amdgcn_make_buffer_rsrc(base, 0, bytes, 0x00020000), off, 0, 16);
}
__device__ __forceinline__ void load_coh(u32x4& f, u32 voff, const u32x4& srd, u32 soff) {
    asm volatile("buffer_load_dwordx4 %0, %1, %2, %3 offen sc1" : "+v"(f) : "v"(voff), "s"(srd), "s"(soff) : "memory");
}
__device__ __forceinline__ void signal(char* ws, int cnt) {
    if (threadIdx.x == 0) __hip_atomic_fetch_add((gu32*)(ws + WS_CNT + cnt * CNT_STRIDE), 1u, __ATOMIC_RELAXED, __HIP_MEMORY_SCOPE_AGENT);
}
__device__ __forceinline__ void publish(char* ws, int cnt) {
    asm volatile("s_waitcnt vmcnt(0)\n\ts_barrier" ::: "memory");
    signal(ws, cnt);
}
__device__ __forceinline__ void await(char* ws, int cnt, u32 target) {
    gu32* p = (gu32*)(ws + WS_CNT + cnt * CNT_STRIDE);
    for (int spins = 0; spins < (1 << 20); ++spins) {
        if (__builtin_amdgcn_readfirstlane(__hip_atomic_load(p, __ATOMIC_RELAXED, __HIP_MEMORY_SCOPE_AGENT)) >= target) break;
        __builtin_amdgcn_s_sleep(1);
    }
    asm volatile("" ::: "memory");
}

__device__ __forceinline__ void sample(u32& seen, char* ws, int cnt) {
    asm volatile("global_load_dword %0, %1, off sc1" : "+v"(seen) : "v"(ws + WS_CNT + cnt * CNT_STRIDE) : "memory");
}
__device__ __forceinline__ void confirm(u32& seen, char* ws, int cnt, u32 target) {
    PIN(seen);
    if (__builtin_amdgcn_readfirstlane(seen) < target) await(ws, cnt, target);
}


template <int R, int NR> struct OpFrag {
    static constexpr int BYTES = 4 * R * 16, PPW = BYTES / 4096, RPF = 1;
    static constexpr bool REG = false;
    u32x4 srd; u32 voff, ldst; lds_ptr rd;
    __device__ __forceinline__ void init(const void* base, u32 bytes, int row0, lds_ptr img, int w, int wrow, int lane) {
        srd = make_srd(base, bytes);
        voff = (u32)(row0 + lane + w * NR) * 16u;
        ldst = (u32)(uintptr_t)img + w * (R * 16);
        rd = img + ((lane >> 4) * R + wrow + (lane & 15)) * 16;
    }
    __device__ __forceinline__ void issue(int slot, u32 pos, int i) const { dma16_coh(ldst + pos + i * 1024, voff, srd, (u32)slot * (4u * NR * 16u) + i * 1024u); }
    __device__ __forceinline__ void read(u32x4& f, int pos, int i, int) const { f = lds_rd128(rd + pos + i * 256); }
};

template <int C, int RS, int KS, bool NAT, int NF> struct OpTr {
    static constexpr int BYTES = KS * 32 * C * 2, PPW = BYTES / 4096, RPF = 2;
    static constexpr bool REG = false;
    u32x4 srd; u32 voff, ldst; lds_ptr rd[NF];
    __device__ __forceinline__ void init(const void* base, u32 bytes, int col0, int k0, lds_ptr img, int w, int wch, int lane) {
        srd = make_srd(base, bytes);
        ldst = (u32)(uintptr_t)img + w * 1024;
        const int g = lane >> 4, q = (lane >> 2) & 3, p = lane & 3;
        if constexpr (C == 128) {
            const int f = NAT ? (g | (((w >> 1) & 1) << 2)) : (4 * (w & 1) + g);
            voff = (u32)(32 * k0 + 4 * w + g) * RS + col0 * 2 + ((((lane & 15) >> 1) ^ f) * 32) + (lane & 1) * 16;
            const int row = NAT ? 8 * g + q : 4 * g + q, fr = NAT ? (q | ((g & 1) << 2)) : (row & 7);
#pragma unroll
            for (int i = 0; i < NF; ++i) rd[i] = img + row * 256 + (((wch ^ i) ^ fr) * 32) + p * 8;
        } else {
            static_assert(C == 128 || C == 64, "unsupported transposed operand");
            const int f = NAT ? ((g & 1) | ((w & 1) << 1)) : g;
            voff = (u32)(32 * k0 + 8 * w + (lane >> 3)) * RS + col0 * 2 + ((((lane & 7) >> 1) ^ f) * 32) + (lane & 1) * 16;
            const int row = NAT ? 8 * g + q : 4 * g + q, fr = NAT ? ((q >> 1) | ((g & 1) << 1)) : ((row >> 1) & 3);
#pragma unroll
            for (int i = 0; i < NF; ++i) rd[i] = img + row * 128 + (((wch ^ i) ^ fr) * 32) + p * 8;
        }
    }
    __device__ __forceinline__ void issue(int slot, u32 pos, int i) const { dma16(ldst + pos + i * 4096, voff, srd, (u32)(slot * KS * 32 + i * (2048 / C)) * RS); }
    __device__ __forceinline__ void read(u32x4& f, int pos, int i, int t) const {
        const u32x2 v = lds_rdtr(rd[i % NF] + pos + i / NF * (32 * C * 2) + t * ((NAT ? 4 : 16) * C * 2));
        f[2 * t] = v[0]; f[2 * t + 1] = v[1];
    }
};

struct OpRow {
    static constexpr int BYTES = 128 * 128, PPW = 4, RPF = 1;
    static constexpr bool REG = false;
    u32x4 srd; u32 voff, ldst; lds_ptr rd[2];
    __device__ __forceinline__ void init(const void* base, u32 bytes, int row0, lds_ptr img, int w, int wrow, int lane) {
        srd = make_srd(base, bytes);
        ldst = (u32)(uintptr_t)img + w * 1024;
        const int g = lane >> 4, c = lane & 15;
        voff = (u32)(row0 + 8 * w + (lane >> 3)) * (D * 2) + (((lane & 7) ^ (4 * (w & 1) + g)) * 16);
#pragma unroll
        for (int ks = 0; ks < 2; ++ks) rd[ks] = img + (wrow + c) * 128 + (((4 * ks + g) ^ (c >> 1)) * 16);
    }
    __device__ __forceinline__ void issue(int slot, u32 pos, int i) const { dma16(ldst + pos + i * 4096, voff, srd, (u32)slot * 128u + i * (32u * D * 2u)); }
    __device__ __forceinline__ void read(u32x4& f, int pos, int i, int) const { f = lds_rd128(rd[i >> 2] + pos + (i & 3) * 2048); }
};

template <int NF, int NR, bool COH> struct OpReg {
    static constexpr int PPW = NF, RPF = 0;
    static constexpr bool REG = true;
    u32x4 srd; u32 voff;
    __device__ __forceinline__ void init(const void* base, u32 bytes, int row0, int k0, int lane) {
        srd = make_srd(base, bytes);
        voff = (u32)(row0 + (lane & 15) + ((lane >> 4) + 4 * k0) * NR) * 16u;
    }
    __device__ __forceinline__ void load(u32x4& f, int slot, int i) const {
        const u32 soff = (u32)slot * (4u * NR * 16u);
        if (COH) asm volatile("buffer_load_dwordx4 %0, %1, %2, %3 offen offset:%4 sc1" : "+v"(f) : "v"(voff), "s"(srd), "s"(soff), "n"(i * 256) : "memory");
        else asm volatile("buffer_load_dwordx4 %0, %1, %2, %3 offen offset:%4" : "+v"(f) : "v"(voff), "s"(srd), "s"(soff), "n"(i * 256) : "memory");
    }
};

template <int NA, int NB> struct Frags { u32x4 a[NA], b[NB]; };
template <int NS, int NA, int NB, int KS = 1> struct Engine { f32x4 acc[NA][NB]; Frags<NA * KS, NB * KS> f[2]; u32x4 ring[NS][NA]; };

template <int KS, int SWAP, bool READ, bool ISSUE, bool RISSUE, int NA, int NB, class A, class B>
__device__ __forceinline__ void gemm_step(f32x4 (&acc)[NA][NB], const Frags<NA * KS, NB * KS>& cur, Frags<NA * KS, NB * KS>& nxt, u32x4 (&ring)[NA],
                                          u32x4 (&prev)[NA], const A& a, const B& b, int rpos, int islot, u32 ipos) {
    constexpr int NM = NA * NB * KS, NRA = NA * A::RPF, NRK = NRA + NB * B::RPF, NRD = NRK * KS, RPM = (2 * NRD + NM - 1) / NM;
    constexpr int ND = A::PPW + B::PPW, DSTEP = NM / ND, DOFF = 1 % DSTEP;
    if constexpr (A::REG) {
#pragma unroll
        for (int i = 0; i < NA; ++i) PIN(ring[i]);
    }
#pragma unroll
    for (int m = 0; m < NM; ++m) {
        const int i = m % NA, j = m / NA % NB, ks = m / (NA * NB);
        const u32x4 av = A::REG ? ring[i] : cur.a[ks * NA + i];
        if (i >= SWAP) mma16(acc[i][j], cur.b[ks * NB + j], av);
        else mma16(acc[i][j], av, cur.b[ks * NB + j]);
        if constexpr (READ) {
#pragma unroll
            for (int rr = m * RPM; rr < (m + 1) * RPM && rr < NRD; ++rr) {
                const int k = rr / NRK, r = rr % NRK;
                if (r < B::RPF) b.read(nxt.b[k * NB], rpos, k * NB, r);
                else if (r >= B::RPF + NRA) b.read(nxt.b[k * NB + (r - NRA) / B::RPF], rpos, k * NB + (r - NRA) / B::RPF, (r - NRA) % B::RPF);
                else if constexpr (!A::REG) a.read(nxt.a[k * NA + (r - B::RPF) / A::RPF], rpos, k * NA + (r - B::RPF) / A::RPF, (r - B::RPF) % A::RPF);
            }
        }
        if (m % DSTEP == DOFF && m / DSTEP < ND) {
            const int d = m / DSTEP;
            if (d < B::PPW) { if constexpr (ISSUE) b.issue(islot, ipos, d); }
            else if constexpr (A::REG) { if constexpr (RISSUE) a.load(prev[d - B::PPW], islot - 1, d - B::PPW); }
            else if constexpr (ISSUE) a.issue(islot, ipos, d - B::PPW);
        }
        SB();
    }
}

template <int V> struct IC { static constexpr int v = V; };
template <int NS, int PA, int PB, bool REG> constexpr int younger(int rem) {
    if (rem == 2) return 0;
    int n = 0;
    for (int k = 1; k <= NS - 2; ++k) n += (rem + k > NS ? PB + (REG ? 0 : PA) : 0) + (REG && rem + k > NS - 1 ? PA : 0);
    return n;
}
template <int P, int COUNT, int REM, int SLOTB, int SWAP, int XW, int NS, int NA, int NB, int KS, class A, class B, class Hook>
__device__ __forceinline__ void gemm_steps(Engine<NS, NA, NB, KS>& e, const A& a, const B& b, int islot, const Hook& hook) {
    constexpr bool READ = REM > 1;
    if constexpr (READ) wait_bar<younger<NS, A::PPW, B::PPW, A::REG>(REM) + (P < NS - 2 ? XW : 0)>();
    hook(IC<P>{});
    gemm_step<KS, SWAP, READ, (REM > NS), (A::REG && REM > NS - 1)>(e.acc, e.f[P & 1], e.f[(P & 1) ^ 1], e.ring[P % NS], e.ring[(P + NS - 1) % NS], a, b,
                                                                   (P + 1) % NS * SLOTB, islot + P, P % NS * SLOTB);
    if constexpr (COUNT > 1) gemm_steps<P + 1, COUNT - 1, REM - 1, SLOTB, SWAP, XW>(e, a, b, islot, hook);
    else if constexpr (REM == 1) mfma_settle();
}

template <int SLOTB, class A, class B> __device__ __forceinline__ void gemm_fill(const A& a, const B& b, int p0, int p1) {
#pragma unroll
    for (int p = p0; p < p1; ++p) {
#pragma unroll
        for (int i = 0; i < B::PPW; ++i) b.issue(p, p * SLOTB, i);
        if constexpr (!A::REG) {
#pragma unroll
            for (int i = 0; i < A::PPW; ++i) a.issue(p, p * SLOTB, i);
        }
    }
}
template <int NS, int NA, int NB, int KS, class A, class B> __device__ __forceinline__ void gemm_first(Engine<NS, NA, NB, KS>& e, const A& a, const B& b) {
    if constexpr (!A::REG) {
#pragma unroll
        for (int i = 0; i < KS * NA * A::RPF; ++i) a.read(e.f[0].a[i / A::RPF], 0, i / A::RPF, i % A::RPF);
    }
#pragma unroll
    for (int i = 0; i < KS * NB * B::RPF; ++i) b.read(e.f[0].b[i / B::RPF], 0, i / B::RPF, i % B::RPF);
    SB();
}

constexpr int MANY = 1 << 20;
template <int NS, int NSLOTS> struct Rounds { static constexpr int RL = NS % 2 ? 2 * NS : NS, N = (NSLOTS - NS) / RL, TAIL = NSLOTS - N * RL; };
template <int SLOTB, int NSLOTS, int SWAP, int NS, int NA, int NB, int KS, class A, class B, class Gate, class Round, class Tail>
__device__ __forceinline__ void gemm_run(Engine<NS, NA, NB, KS>& e, const A& a, const B& b, const Gate& gate, const Round& round, const Tail& tail) {
    using R = Rounds<NS, NSLOTS>;
#pragma unroll
    for (int i = 0; i < NA; ++i)
#pragma unroll
        for (int j = 0; j < NB; ++j) e.acc[i][j] = f32x4{};
    gemm_fill<SLOTB>(a, b, 0, NS);
    if constexpr (A::REG) {
        gate();
#pragma unroll
        for (int p = 0; p < NS; ++p)
#pragma unroll
            for (int i = 0; i < NA; ++i) { e.ring[p][i] = u32x4{}; if (p < NS - 1) a.load(e.ring[p][i], p, i); }
        wait_bar<NA>();
    } else {
        wait_bar<(NS - 1) * (A::PPW + B::PPW)>();
    }
    gemm_first(e, a, b);
    for (int r = 0; r < R::N; ++r) { round(r); gemm_steps<0, R::RL, MANY, SLOTB, SWAP, 0>(e, a, b, r * R::RL + NS, [](auto) {}); }
    gemm_steps<0, R::TAIL, R::TAIL, SLOTB, SWAP, 0>(e, a, b, R::N * R::RL + NS, tail);
}

constexpr int PRE_KV = 128, PRE_LN = 128, PRE_LDS = 65536 + 1024 + 2048, W1_PRE = S / 2 * F * 2;

template <int CTRL> __device__ __forceinline__ float dpp_add(float v) {
    return v + __builtin_bit_cast(float, __builtin_amdgcn_update_dpp(0, __builtin_bit_cast(int, v), CTRL, 0xf, 0xf, true));
}
__device__ __forceinline__ float group_sum(float v) {
    const auto a = __builtin_amdgcn_permlane16_swap(__float_as_uint(v), __float_as_uint(v), false, false);
    v = __uint_as_float(a[0]) + __uint_as_float(a[1]);
    const auto b = __builtin_amdgcn_permlane32_swap(__float_as_uint(v), __float_as_uint(v), false, false);
    return __uint_as_float(b[0]) + __uint_as_float(b[1]);
}

__device__ __forceinline__ float row_sum(float v) { return dpp_add<0x140>(dpp_add<0x141>(dpp_add<0x4e>(dpp_add<0xb1>(v)))); }
__device__ __forceinline__ float fold32(float a, float b) {
    const auto r = __builtin_amdgcn_permlane32_swap(__float_as_uint(a), __float_as_uint(b), false, false);
    return __uint_as_float(r[0]) + __uint_as_float(r[1]);
}
__device__ __forceinline__ float fold16(float a, float b) {
    const auto r = __builtin_amdgcn_permlane16_swap(__float_as_uint(a), __float_as_uint(b), false, false);
    return __uint_as_float(r[0]) + __uint_as_float(r[1]);
}
template <int CTRL> __device__ __forceinline__ float dpp_max(float v) {
    return fmaxf(v, __builtin_bit_cast(float, __builtin_amdgcn_update_dpp(0, __builtin_bit_cast(int, v), CTRL, 0xf, 0xf, true)));
}
__device__ __forceinline__ float wave_max(float v) {
    v = dpp_max<0xb1>(v); v = dpp_max<0x4e>(v); v = dpp_max<0x141>(v); v = dpp_max<0x140>(v);
    const auto a = __builtin_amdgcn_permlane16_swap(__float_as_uint(v), __float_as_uint(v), false, false);
    v = fmaxf(__uint_as_float(a[0]), __uint_as_float(a[1]));
    const auto b = __builtin_amdgcn_permlane32_swap(__float_as_uint(v), __float_as_uint(v), false, false);
    return fmaxf(__uint_as_float(b[0]), __uint_as_float(b[1]));
}

template <u32 BYTES, int NB, int NTHR> __device__ __forceinline__ void touch(const char* p, int b, u32 scratch) {
    constexpr u32 LINES = BYTES / 128, PER = (LINES + NB - 1) / NB, N = (PER + NTHR - 1) / NTHR;
    const u32x4 srd = make_srd(p, BYTES);
#pragma unroll
    for (u32 i = 0; i < N; ++i) {
        const u32 l = i * NTHR + threadIdx.x, off = l < PER ? (b * PER + l) * 128u : 0x80000000u;
        asm volatile("s_mov_b32 m0, %0\n\ts_nop 0\n\tbuffer_load_dword %1, %2, 0 offen lds" ::"s"(scratch), "v"(off), "s"(srd) : "memory");
    }
}

#define CODE_ADDR(v, mark) asm volatile("s_getpc_b64 s[40:41]\n\ts_add_u32 s40, s40, " mark "@rel32@lo+4\n\ts_addc_u32 s41, s41, " mark "@rel32@hi+12" : "={s[40:41]}"(v) :: "scc")
__device__ __forceinline__ void touch_code(uint64_t a, uint64_t e, u32 scratch) {
    const u32x4 srd = make_srd((const void*)a, e > a ? (u32)(e - a) & ~3u : 0u);
    asm volatile("s_mov_b32 m0, %0\n\ts_nop 0\n\tbuffer_load_dword %1, %2, 0 offen lds" ::"s"(scratch), "v"(threadIdx.x * 128u), "s"(srd) : "memory");
}

__device__ __forceinline__ void touch_pages(const char* p, u32 bytes, u32 scratch) {
    const u32x4 srd = make_srd(p, bytes);
    asm volatile("s_mov_b32 m0, %0\n\ts_nop 0\n\tbuffer_load_dword %1, %2, 0 offen sc1 lds" ::"s"(scratch), "v"(threadIdx.x << 16), "s"(srd) : "memory");
}

template <bool V> __device__ __forceinline__ void kv_block(int t, const char* x, const char* wm, char* ws, lds_ptr lds, int w8, int lane) {
    const int g = lane >> 4, c = lane & 15, q = (lane >> 2) & 3, p = lane & 3;
    const lds_ptr img = lds + w8 * 8192;
    const u32x4 srd = make_srd(wm, D * 64);
    const u32 voff = (u32)(lane >> 2) * 64 + ((((lane >> 1) & 1) ^ (lane >> 5)) * 32) + (lane & 1) * 16;
    u32x4 xf[2][4];
#pragma unroll
    for (int ks = 0; ks < 4; ++ks)
#pragma unroll
        for (int j = 0; j < 2; ++j) xf[j][ks] = *(const u32x4*)(x + (size_t)(t * 32 + j * 16 + c) * (D * 2) + (w8 * 128 + ks * 32 + g * 8) * 2);
#pragma unroll
    for (int i = 0; i < 8; ++i) dma16((u32)(uintptr_t)img + i * 1024, voff, srd, w8 * 8192 + i * 1024);
    asm volatile("s_waitcnt vmcnt(0)" ::: "memory");
    f32x4 acc[4];
#pragma unroll
    for (int i = 0; i < 4; ++i) acc[i] = f32x4{};
#pragma unroll
    for (int ks = 0; ks < 4; ++ks)
#pragma unroll
        for (int i = 0; i < 2; ++i) {
            const lds_ptr rd = img + (ks * 32 + 8 * g + q) * 64 + ((i ^ (g & 1)) * 32) + p * 8;
            const u32x2 lo = lds_rdtr(rd), hi = lds_rdtr(rd + 256);
            const u32x4 wf = {lo[0], lo[1], hi[0], hi[1]};
#pragma unroll
            for (int j = 0; j < 2; ++j) acc[2 * i + j] = V ? M16(xf[j][ks], wf, acc[2 * i + j]) : M16(wf, xf[j][ks], acc[2 * i + j]);
        }
#pragma unroll
    for (int i = 0; i < 4; ++i) *(LDS f32x4*)(img + i * 1024 + lane * 16) = acc[i];
    wait_bar<0>();
    if (w8 >= 2) return;
    const int ta = V ? 2 * w8 : w8, tb = V ? 2 * w8 + 1 : w8 + 2, fr = t * 4 + 2 * V + w8;
    f32x4 a = {}, b = {};
#pragma unroll
    for (int v = 0; v < 8; ++v) { a += *(LDS const f32x4*)(lds + v * 8192 + ta * 1024 + lane * 16); b += *(LDS const f32x4*)(lds + v * 8192 + tb * 1024 + lane * 16); }
    store_wt(ws + WS_KV, S * 128, fr * 1024 + lane * 16, pack8(a, b));
    float bnd = 0.f;
    if (!V) {
#pragma unroll
        for (int r = 0; r < 4; ++r) bnd += a[r] * a[r] + b[r] * b[r];
        bnd = group_sum(bnd);
        bnd = bnd == bnd ? bnd : __builtin_inff();
    } else {
#pragma unroll
        for (int r = 0; r < 4; ++r)
            if (!(fabsf(a[r]) <= SAFE_V && fabsf(b[r]) <= SAFE_V)) bnd = __builtin_inff();
    }
    bnd = wave_max(bnd);
    if (lane == 0) ((float*)(ws + WS_BND))[fr] = bnd;
}

__device__ __forceinline__ void ln_rows(int r, const char* x, const char* gw, const char* wq, const char* w1, char* ws, lds_ptr lds, u32 scratch, int w8, int lane) {
    const int u = w8 >> 2, cq = w8 & 3, chunk = 2 * r + u, row0 = 32 * (chunk >> 2) + 4 * (chunk & 3);
    u32x2 xv[8];
    float sh[8];
#pragma unroll
    for (int e = 0; e < 8; ++e) {
        const char* row = x + (size_t)(row0 + (e & 3) + 16 * (e >> 2)) * (D * 2);
        xv[e] = *(const u32x2*)(row + cq * 512 + lane * 8);
        sh[e] = bf_lo(*(const u32*)row);
    }
    u32x2 gv = *(const u32x2*)(gw + cq * 512 + lane * 8);
    PIN(gv);
#pragma unroll
    for (int e = 0; e < 8; ++e) PIN(xv[e]);
    if (r < 16) {
        uint64_t a, e;
        CODE_ADDR(a, "main_code_begin"); CODE_ADDR(e, "main_code_end");
        touch_code(a, e, scratch);
    }
    touch<D * D * 2, PRE_LN, 512>(wq, r, scratch);
    touch<W1_PRE, PRE_LN, 512>(w1, r, scratch);
    float part[3][8];
#pragma unroll
    for (int e = 0; e < 8; ++e) {
        const float a0 = bf_lo(xv[e][0]), a1 = bf_hi(xv[e][0]), a2 = bf_lo(xv[e][1]), a3 = bf_hi(xv[e][1]);
        const float d0 = a0 - sh[e], d1 = a1 - sh[e], d2 = a2 - sh[e], d3 = a3 - sh[e];
        part[0][e] = (d0 + d1) + (d2 + d3);
        part[1][e] = (d0 * d0 + d1 * d1) + (d2 * d2 + d3 * d3);
        part[2][e] = (a0 * bf_lo(gv[0]) + a1 * bf_hi(gv[0])) + (a2 * bf_lo(gv[1]) + a3 * bf_hi(gv[1]));
    }
    LDS float* red = (LDS float*)(lds + 65536);
#pragma unroll
    for (int k = 0; k < 3; ++k)
#pragma unroll
        for (int h = 0; h < 2; ++h) {
            const float v = row_sum(fold16(fold32(part[k][4 * h], part[k][4 * h + 2]), fold32(part[k][4 * h + 1], part[k][4 * h + 3])));
            if ((lane & 15) == 0) red[w8 * 24 + k * 8 + 4 * h + (lane >> 4)] = v;
        }
    wait_bar<63>();
    const int e8 = lane & 7;
    float st[3], mysh = 0.f;
#pragma unroll
    for (int k = 0; k < 3; ++k) st[k] = (red[(4 * u) * 24 + k * 8 + e8] + red[(4 * u + 1) * 24 + k * 8 + e8]) + (red[(4 * u + 2) * 24 + k * 8 + e8] + red[(4 * u + 3) * 24 + k * 8 + e8]);
#pragma unroll
    for (int e = 0; e < 8; ++e) mysh = e8 == e ? sh[e] : mysh;
    const float dm = st[0] * (1.f / D), var = fmaxf(st[1] * (1.f / D) - dm * dm, 0.f);
    const float mean = bf_round(mysh + dm), rs = 1.f / bf_round(sqrtf(bf_round(bf_round(var) + 1e-5f)));
    if (cq == 0 && lane < 8) ((float*)(ws + WS_G))[row0 + (lane & 3) + 16 * (lane >> 2)] = bf_round(1.f / (1.f + __expf(-bf_round(st[2]))));
    float h[4][8];
#pragma unroll
    for (int e = 0; e < 8; ++e) {
        const float m = __uint_as_float(__builtin_amdgcn_readlane(__float_as_uint(mean), e)), q = __uint_as_float(__builtin_amdgcn_readlane(__float_as_uint(rs), e));
        h[0][e] = (bf_lo(xv[e][0]) - m) * q; h[1][e] = (bf_hi(xv[e][0]) - m) * q; h[2][e] = (bf_lo(xv[e][1]) - m) * q; h[3][e] = (bf_hi(xv[e][1]) - m) * q;
    }
    const lds_ptr tb = lds + w8 * 4096;
#pragma unroll
    for (int m = 0; m < 4; ++m)
        *(LDS u32x4*)(tb + lane * 64 + ((m ^ ((lane >> 1) & 3)) * 16)) = u32x4{cvtpk(h[m][0], h[m][1]), cvtpk(h[m][2], h[m][3]), cvtpk(h[m][4], h[m][5]), cvtpk(h[m][6], h[m][7])};
    const u32 dst = chunk * (D * 16) + cq * 4096 + lane * 16;
#pragma unroll
    for (int i = 0; i < 4; ++i) store_wt(ws + WS_HF, S * D * 2, dst + i * 1024, lds_rd128(tb + (i * 16 + (lane >> 2)) * 64 + (((lane & 3) ^ ((lane >> 3) & 3)) * 16)));
}

__global__ void __launch_bounds__(512, 2) k_pre(const char* __restrict__ x, const char* __restrict__ wq, const char* __restrict__ wk, const char* __restrict__ wv,
                                                const char* __restrict__ gw, const char* __restrict__ w1, char* __restrict__ ws) {
    extern __shared__ __attribute__((aligned(16))) char smem[];
    const int lane = threadIdx.x & 63, w8 = __builtin_amdgcn_readfirstlane(threadIdx.x >> 6), b = blockIdx.x;
    const u32 scratch = (u32)(uintptr_t)(lds_ptr)smem + 65536 + 1024 + w8 * 256;
    asm volatile("pre_code_begin:");
    if (w8 == 0) {
        uint64_t a, e;
        CODE_ADDR(a, "pre_code_begin"); CODE_ADDR(e, "pre_code_end");
        touch_code(a, e, scratch);
    }
    const int t = (b & 7) + 8 * ((b >> 4) & 7), half = (b >> 3) & 1;
    if (b >= PRE_KV) {
        ln_rows(2 * t + half, x, gw, wq, w1, ws, (lds_ptr)smem, scratch, w8, lane);
        if (b == PRE_KV && threadIdx.x < CNT_N) *(u32*)(ws + WS_CNT + threadIdx.x * CNT_STRIDE) = 0u;
        asm volatile("s_waitcnt vmcnt(0)" ::: "memory");
    } else if (half) kv_block<true>(t, x, wv, ws, (lds_ptr)smem, w8, lane);
    else kv_block<false>(t, x, wk, ws, (lds_ptr)smem, w8, lane);
    asm volatile("pre_code_end:");
}

constexpr int MID_ATT = 256, MID_G1 = 224, MID_PF = 32, G1_NS = 6;
constexpr int KV_STAGE = 16384, KV_NSTAGE = 16;

#define ATT_LOOP_ASM \
    "s_mov_b32 s23, 16\n" \
    "s_mov_b32 s24, 0xc000\n" \
    "s_mov_b32 s22, 0\n" \
    "s_add_u32 s20, %[ldst], 0x800\n" \
    "s_mov_b32 s21, 0x8800\n" \
    "ATT_LOOP_%=:\n" \
    "v_mfma_f32_16x16x32_bf16 v[8:11], v[80:83], v[68:71], 0\n" \
    "v_exp_f32 v0, v0\n" \
    "v_exp_f32 v1, v1\n" \
    "v_mfma_f32_16x16x32_bf16 v[12:15], v[84:87], v[68:71], 0\n" \
    "ds_read_b128 v[96:99], v124 offset:4096\n" \
    "v_exp_f32 v2, v2\n" \
    "v_exp_f32 v3, v3\n" \
    "v_mfma_f32_16x16x32_bf16 v[40:43], v[104:107], v[120:123], v[40:43]\n" \
    "v_cvt_pk_bf16_f32 v116, v0, v1\n" \
    "v_exp_f32 v4, v4\n" \
    "v_exp_f32 v5, v5\n" \
    "v_mfma_f32_16x16x32_bf16 v[44:47], v[108:111], v[120:123], v[44:47]\n" \
    "s_mov_b32 m0, s20\n" \
    "s_add_u32 s20, s20, 0x400\n" \
    "buffer_load_dwordx4 v126, %[srd], s21 offen lds\n" \
    "s_add_u32 s21, s21, 0x400\n" \
    "v_cvt_pk_bf16_f32 v117, v2, v3\n" \
    "v_exp_f32 v6, v6\n" \
    "v_exp_f32 v7, v7\n" \
    "v_mfma_f32_16x16x32_bf16 v[60:63], v[112:115], v[120:123], v[60:63]\n" \
    "v_cvt_pk_bf16_f32 v118, v4, v5\n" \
    "v_cvt_pk_bf16_f32 v119, v6, v7\n" \
    "s_waitcnt lgkmcnt(1)\n" \
    "v_mfma_f32_16x16x32_bf16 v[0:3], v[80:83], v[72:75], 0\n" \
    "v_exp_f32 v8, v8\n" \
    "v_exp_f32 v9, v9\n" \
    "v_mfma_f32_16x16x32_bf16 v[4:7], v[84:87], v[72:75], 0\n" \
    "ds_read_b128 v[100:103], v124 offset:5120\n" \
    "v_exp_f32 v10, v10\n" \
    "v_exp_f32 v11, v11\n" \
    "v_mfma_f32_16x16x32_bf16 v[16:19], v[88:91], v[116:119], v[16:19]\n" \
    "v_cvt_pk_bf16_f32 v120, v8, v9\n" \
    "v_exp_f32 v12, v12\n" \
    "v_exp_f32 v13, v13\n" \
    "v_mfma_f32_16x16x32_bf16 v[20:23], v[92:95], v[116:119], v[20:23]\n" \
    "v_cvt_pk_bf16_f32 v121, v10, v11\n" \
    "v_exp_f32 v14, v14\n" \
    "v_exp_f32 v15, v15\n" \
    "v_mfma_f32_16x16x32_bf16 v[48:51], v[112:115], v[116:119], v[48:51]\n" \
    "v_cvt_pk_bf16_f32 v122, v12, v13\n" \
    "v_cvt_pk_bf16_f32 v123, v14, v15\n" \
    "v_mfma_f32_16x16x32_bf16 v[8:11], v[80:83], v[76:79], 0\n" \
    "v_exp_f32 v0, v0\n" \
    "v_exp_f32 v1, v1\n" \
    "v_mfma_f32_16x16x32_bf16 v[12:15], v[84:87], v[76:79], 0\n" \
    "ds_read_b128 v[104:107], v124 offset:6144\n" \
    "v_exp_f32 v2, v2\n" \
    "v_exp_f32 v3, v3\n" \
    "v_mfma_f32_16x16x32_bf16 v[24:27], v[88:91], v[120:123], v[24:27]\n" \
    "v_cvt_pk_bf16_f32 v116, v0, v1\n" \
    "v_exp_f32 v4, v4\n" \
    "v_exp_f32 v5, v5\n" \
    "v_mfma_f32_16x16x32_bf16 v[28:31], v[92:95], v[120:123], v[28:31]\n" \
    "s_mov_b32 m0, s20\n" \
    "s_add_u32 s20, s20, 0x400\n" \
    "buffer_load_dwordx4 v126, %[srd], s21 offen lds\n" \
    "s_add_u32 s21, s21, 0x400\n" \
    "s_add_u32 s22, s22, 0x4000\n" \
    "s_and_b32 s22, s22, 0xffff\n" \
    "s_add_u32 s20, s22, %[ldst]\n" \
    "s_add_u32 s21, s21, 0x3000\n" \
    "v_cvt_pk_bf16_f32 v117, v2, v3\n" \
    "v_exp_f32 v6, v6\n" \
    "v_exp_f32 v7, v7\n" \
    "v_mfma_f32_16x16x32_bf16 v[52:55], v[112:115], v[120:123], v[52:55]\n" \
    "v_cvt_pk_bf16_f32 v118, v4, v5\n" \
    "v_cvt_pk_bf16_f32 v119, v6, v7\n" \
    "s_waitcnt lgkmcnt(1)\n" \
    "v_mfma_f32_16x16x32_bf16 v[0:3], v[96:99], v[64:67], 0\n" \
    "v_exp_f32 v8, v8\n" \
    "v_exp_f32 v9, v9\n" \
    "v_mfma_f32_16x16x32_bf16 v[4:7], v[100:103], v[64:67], 0\n" \
    "ds_read_b128 v[108:111], v124 offset:7168\n" \
    "v_exp_f32 v10, v10\n" \
    "v_exp_f32 v11, v11\n" \
    "v_mfma_f32_16x16x32_bf16 v[32:35], v[88:91], v[116:119], v[32:35]\n" \
    "v_cvt_pk_bf16_f32 v120, v8, v9\n" \
    "v_exp_f32 v12, v12\n" \
    "v_exp_f32 v13, v13\n" \
    "v_mfma_f32_16x16x32_bf16 v[36:39], v[92:95], v[116:119], v[36:39]\n" \
    "v_cvt_pk_bf16_f32 v121, v10, v11\n" \
    "v_exp_f32 v14, v14\n" \
    "v_exp_f32 v15, v15\n" \
    "v_mfma_f32_16x16x32_bf16 v[56:59], v[112:115], v[116:119], v[56:59]\n" \
    "v_cvt_pk_bf16_f32 v122, v12, v13\n" \
    "v_cvt_pk_bf16_f32 v123, v14, v15\n" \
    "v_mfma_f32_16x16x32_bf16 v[8:11], v[96:99], v[68:71], 0\n" \
    "v_exp_f32 v0, v0\n" \
    "v_exp_f32 v1, v1\n" \
    "v_mfma_f32_16x16x32_bf16 v[12:15], v[100:103], v[68:71], 0\n" \
    "ds_read_b128 v[80:83], v124 offset:8192\n" \
    "v_exp_f32 v2, v2\n" \
    "v_exp_f32 v3, v3\n" \
    "v_mfma_f32_16x16x32_bf16 v[40:43], v[88:91], v[120:123], v[40:43]\n" \
    "v_cvt_pk_bf16_f32 v116, v0, v1\n" \
    "v_exp_f32 v4, v4\n" \
    "v_exp_f32 v5, v5\n" \
    "v_mfma_f32_16x16x32_bf16 v[44:47], v[92:95], v[120:123], v[44:47]\n" \
    "v_cvt_pk_bf16_f32 v117, v2, v3\n" \
    "v_exp_f32 v6, v6\n" \
    "v_exp_f32 v7, v7\n" \
    "v_mfma_f32_16x16x32_bf16 v[60:63], v[112:115], v[120:123], v[60:63]\n" \
    "v_cvt_pk_bf16_f32 v118, v4, v5\n" \
    "v_cvt_pk_bf16_f32 v119, v6, v7\n" \
    "s_waitcnt lgkmcnt(1)\n" \
    "v_mfma_f32_16x16x32_bf16 v[0:3], v[96:99], v[72:75], 0\n" \
    "v_exp_f32 v8, v8\n" \
    "v_exp_f32 v9, v9\n" \
    "v_mfma_f32_16x16x32_bf16 v[4:7], v[100:103], v[72:75], 0\n" \
    "ds_read_b128 v[84:87], v124 offset:9216\n" \
    "v_exp_f32 v10, v10\n" \
    "v_exp_f32 v11, v11\n" \
    "v_mfma_f32_16x16x32_bf16 v[16:19], v[104:107], v[116:119], v[16:19]\n" \
    "v_cvt_pk_bf16_f32 v120, v8, v9\n" \
    "v_exp_f32 v12, v12\n" \
    "v_exp_f32 v13, v13\n" \
    "v_mfma_f32_16x16x32_bf16 v[20:23], v[108:111], v[116:119], v[20:23]\n" \
    "v_cvt_pk_bf16_f32 v121, v10, v11\n" \
    "v_exp_f32 v14, v14\n" \
    "v_exp_f32 v15, v15\n" \
    "v_mfma_f32_16x16x32_bf16 v[48:51], v[112:115], v[116:119], v[48:51]\n" \
    "v_cvt_pk_bf16_f32 v122, v12, v13\n" \
    "v_cvt_pk_bf16_f32 v123, v14, v15\n" \
    "v_mfma_f32_16x16x32_bf16 v[8:11], v[96:99], v[76:79], 0\n" \
    "v_exp_f32 v0, v0\n" \
    "v_exp_f32 v1, v1\n" \
    "v_mfma_f32_16x16x32_bf16 v[12:15], v[100:103], v[76:79], 0\n" \
    "ds_read_b128 v[88:91], v124 offset:10240\n" \
    "v_exp_f32 v2, v2\n" \
    "v_exp_f32 v3, v3\n" \
    "v_mfma_f32_16x16x32_bf16 v[24:27], v[104:107], v[120:123], v[24:27]\n" \
    "v_cvt_pk_bf16_f32 v116, v0, v1\n" \
    "v_exp_f32 v4, v4\n" \
    "v_exp_f32 v5, v5\n" \
    "v_mfma_f32_16x16x32_bf16 v[28:31], v[108:111], v[120:123], v[28:31]\n" \
    "v_cvt_pk_bf16_f32 v117, v2, v3\n" \
    "v_exp_f32 v6, v6\n" \
    "v_exp_f32 v7, v7\n" \
    "v_mfma_f32_16x16x32_bf16 v[52:55], v[112:115], v[120:123], v[52:55]\n" \
    "v_cvt_pk_bf16_f32 v118, v4, v5\n" \
    "v_cvt_pk_bf16_f32 v119, v6, v7\n" \
    "s_waitcnt lgkmcnt(1)\n" \
    "v_mfma_f32_16x16x32_bf16 v[0:3], v[80:83], v[64:67], 0\n" \
    "v_exp_f32 v8, v8\n" \
    "v_exp_f32 v9, v9\n" \
    "v_mfma_f32_16x16x32_bf16 v[4:7], v[84:87], v[64:67], 0\n" \
    "ds_read_b128 v[92:95], v124 offset:11264\n" \
    "v_exp_f32 v10, v10\n" \
    "v_exp_f32 v11, v11\n" \
    "v_mfma_f32_16x16x32_bf16 v[32:35], v[104:107], v[116:119], v[32:35]\n" \
    "v_cvt_pk_bf16_f32 v120, v8, v9\n" \
    "v_exp_f32 v12, v12\n" \
    "v_exp_f32 v13, v13\n" \
    "v_mfma_f32_16x16x32_bf16 v[36:39], v[108:111], v[116:119], v[36:39]\n" \
    "v_cvt_pk_bf16_f32 v121, v10, v11\n" \
    "v_exp_f32 v14, v14\n" \
    "v_exp_f32 v15, v15\n" \
    "v_mfma_f32_16x16x32_bf16 v[56:59], v[112:115], v[116:119], v[56:59]\n" \
    "v_cvt_pk_bf16_f32 v122, v12, v13\n" \
    "v_cvt_pk_bf16_f32 v123, v14, v15\n" \
    "v_mfma_f32_16x16x32_bf16 v[8:11], v[80:83], v[68:71], 0\n" \
    "v_exp_f32 v0, v0\n" \
    "v_exp_f32 v1, v1\n" \
    "v_mfma_f32_16x16x32_bf16 v[12:15], v[84:87], v[68:71], 0\n" \
    "ds_read_b128 v[96:99], v124 offset:12288\n" \
    "v_exp_f32 v2, v2\n" \
    "v_exp_f32 v3, v3\n" \
    "v_mfma_f32_16x16x32_bf16 v[40:43], v[104:107], v[120:123], v[40:43]\n" \
    "v_cvt_pk_bf16_f32 v116, v0, v1\n" \
    "v_exp_f32 v4, v4\n" \
    "v_exp_f32 v5, v5\n" \
    "v_mfma_f32_16x16x32_bf16 v[44:47], v[108:111], v[120:123], v[44:47]\n" \
    "v_cvt_pk_bf16_f32 v117, v2, v3\n" \
    "v_exp_f32 v6, v6\n" \
    "v_exp_f32 v7, v7\n" \
    "v_mfma_f32_16x16x32_bf16 v[60:63], v[112:115], v[120:123], v[60:63]\n" \
    "v_cvt_pk_bf16_f32 v118, v4, v5\n" \
    "v_cvt_pk_bf16_f32 v119, v6, v7\n" \
    "s_waitcnt lgkmcnt(1)\n" \
    "v_mfma_f32_16x16x32_bf16 v[0:3], v[80:83], v[72:75], 0\n" \
    "v_exp_f32 v8, v8\n" \
    "v_exp_f32 v9, v9\n" \
    "v_mfma_f32_16x16x32_bf16 v[4:7], v[84:87], v[72:75], 0\n" \
    "ds_read_b128 v[100:103], v124 offset:13312\n" \
    "v_exp_f32 v10, v10\n" \
    "v_exp_f32 v11, v11\n" \
    "v_mfma_f32_16x16x32_bf16 v[16:19], v[88:91], v[116:119], v[16:19]\n" \
    "v_cvt_pk_bf16_f32 v120, v8, v9\n" \
    "v_exp_f32 v12, v12\n" \
    "v_exp_f32 v13, v13\n" \
    "v_mfma_f32_16x16x32_bf16 v[20:23], v[92:95], v[116:119], v[20:23]\n" \
    "v_cvt_pk_bf16_f32 v121, v10, v11\n" \
    "v_exp_f32 v14, v14\n" \
    "v_exp_f32 v15, v15\n" \
    "v_mfma_f32_16x16x32_bf16 v[48:51], v[112:115], v[116:119], v[48:51]\n" \
    "v_cvt_pk_bf16_f32 v122, v12, v13\n" \
    "v_cvt_pk_bf16_f32 v123, v14, v15\n" \
    "v_mfma_f32_16x16x32_bf16 v[8:11], v[80:83], v[76:79], 0\n" \
    "v_exp_f32 v0, v0\n" \
    "v_exp_f32 v1, v1\n" \
    "v_mfma_f32_16x16x32_bf16 v[12:15], v[84:87], v[76:79], 0\n" \
    "ds_read_b128 v[104:107], v124 offset:14336\n" \
    "v_exp_f32 v2, v2\n" \
    "v_exp_f32 v3, v3\n" \
    "v_mfma_f32_16x16x32_bf16 v[24:27], v[88:91], v[120:123], v[24:27]\n" \
    "v_cvt_pk_bf16_f32 v116, v0, v1\n" \
    "v_exp_f32 v4, v4\n" \
    "v_exp_f32 v5, v5\n" \
    "v_mfma_f32_16x16x32_bf16 v[28:31], v[92:95], v[120:123], v[28:31]\n" \
    "v_cvt_pk_bf16_f32 v117, v2, v3\n" \
    "v_exp_f32 v6, v6\n" \
    "v_exp_f32 v7, v7\n" \
    "v_mfma_f32_16x16x32_bf16 v[52:55], v[112:115], v[120:123], v[52:55]\n" \
    "v_cvt_pk_bf16_f32 v118, v4, v5\n" \
    "v_cvt_pk_bf16_f32 v119, v6, v7\n" \
    "s_waitcnt lgkmcnt(1)\n" \
    "v_mfma_f32_16x16x32_bf16 v[0:3], v[96:99], v[64:67], 0\n" \
    "v_exp_f32 v8, v8\n" \
    "v_exp_f32 v9, v9\n" \
    "v_mfma_f32_16x16x32_bf16 v[4:7], v[100:103], v[64:67], 0\n" \
    "ds_read_b128 v[108:111], v124 offset:15360\n" \
    "v_exp_f32 v10, v10\n" \
    "v_exp_f32 v11, v11\n" \
    "v_mfma_f32_16x16x32_bf16 v[32:35], v[88:91], v[116:119], v[32:35]\n" \
    "v_cvt_pk_bf16_f32 v120, v8, v9\n" \
    "v_exp_f32 v12, v12\n" \
    "v_exp_f32 v13, v13\n" \
    "v_mfma_f32_16x16x32_bf16 v[36:39], v[92:95], v[116:119], v[36:39]\n" \
    "v_cvt_pk_bf16_f32 v121, v10, v11\n" \
    "v_exp_f32 v14, v14\n" \
    "v_exp_f32 v15, v15\n" \
    "v_mfma_f32_16x16x32_bf16 v[56:59], v[112:115], v[116:119], v[56:59]\n" \
    "v_cvt_pk_bf16_f32 v122, v12, v13\n" \
    "v_cvt_pk_bf16_f32 v123, v14, v15\n" \
    "s_waitcnt vmcnt(4)\n" \
    "s_barrier\n" \
    "v_mfma_f32_16x16x32_bf16 v[8:11], v[96:99], v[68:71], 0\n" \
    "v_exp_f32 v0, v0\n" \
    "v_exp_f32 v1, v1\n" \
    "v_mfma_f32_16x16x32_bf16 v[12:15], v[100:103], v[68:71], 0\n" \
    "ds_read_b128 v[80:83], v125 offset:0\n" \
    "v_exp_f32 v2, v2\n" \
    "v_exp_f32 v3, v3\n" \
    "v_mfma_f32_16x16x32_bf16 v[40:43], v[88:91], v[120:123], v[40:43]\n" \
    "v_cvt_pk_bf16_f32 v116, v0, v1\n" \
    "v_exp_f32 v4, v4\n" \
    "v_exp_f32 v5, v5\n" \
    "v_mfma_f32_16x16x32_bf16 v[44:47], v[92:95], v[120:123], v[44:47]\n" \
    "s_mov_b32 m0, s20\n" \
    "s_add_u32 s20, s20, 0x400\n" \
    "buffer_load_dwordx4 v126, %[srd], s21 offen lds\n" \
    "s_add_u32 s21, s21, 0x400\n" \
    "v_cvt_pk_bf16_f32 v117, v2, v3\n" \
    "v_exp_f32 v6, v6\n" \
    "v_exp_f32 v7, v7\n" \
    "v_mfma_f32_16x16x32_bf16 v[60:63], v[112:115], v[120:123], v[60:63]\n" \
    "v_cvt_pk_bf16_f32 v118, v4, v5\n" \
    "v_cvt_pk_bf16_f32 v119, v6, v7\n" \
    "s_waitcnt lgkmcnt(1)\n" \
    "v_mfma_f32_16x16x32_bf16 v[0:3], v[96:99], v[72:75], 0\n" \
    "v_exp_f32 v8, v8\n" \
    "v_exp_f32 v9, v9\n" \
    "v_mfma_f32_16x16x32_bf16 v[4:7], v[100:103], v[72:75], 0\n" \
    "ds_read_b128 v[84:87], v125 offset:1024\n" \
    "v_exp_f32 v10, v10\n" \
    "v_exp_f32 v11, v11\n" \
    "v_mfma_f32_16x16x32_bf16 v[16:19], v[104:107], v[116:119], v[16:19]\n" \
    "v_cvt_pk_bf16_f32 v120, v8, v9\n" \
    "v_exp_f32 v12, v12\n" \
    "v_exp_f32 v13, v13\n" \
    "v_mfma_f32_16x16x32_bf16 v[20:23], v[108:111], v[116:119], v[20:23]\n" \
    "v_cvt_pk_bf16_f32 v121, v10, v11\n" \
    "v_exp_f32 v14, v14\n" \
    "v_exp_f32 v15, v15\n" \
    "v_mfma_f32_16x16x32_bf16 v[48:51], v[112:115], v[116:119], v[48:51]\n" \
    "v_cvt_pk_bf16_f32 v122, v12, v13\n" \
    "v_cvt_pk_bf16_f32 v123, v14, v15\n" \
    "v_mfma_f32_16x16x32_bf16 v[8:11], v[96:99], v[76:79], 0\n" \
    "v_exp_f32 v0, v0\n" \
    "v_exp_f32 v1, v1\n" \
    "v_mfma_f32_16x16x32_bf16 v[12:15], v[100:103], v[76:79], 0\n" \
    "ds_read_b128 v[88:91], v125 offset:2048\n" \
    "v_exp_f32 v2, v2\n" \
    "v_exp_f32 v3, v3\n" \
    "v_mfma_f32_16x16x32_bf16 v[24:27], v[104:107], v[120:123], v[24:27]\n" \
    "v_cvt_pk_bf16_f32 v116, v0, v1\n" \
    "v_exp_f32 v4, v4\n" \
    "v_exp_f32 v5, v5\n" \
    "v_mfma_f32_16x16x32_bf16 v[28:31], v[108:111], v[120:123], v[28:31]\n" \
    "s_mov_b32 m0, s20\n" \
    "s_add_u32 s20, s20, 0x400\n" \
    "buffer_load_dwordx4 v126, %[srd], s21 offen lds\n" \
    "s_add_u32 s21, s21, 0x400\n" \
    "v_cvt_pk_bf16_f32 v117, v2, v3\n" \
    "v_exp_f32 v6, v6\n" \
    "v_exp_f32 v7, v7\n" \
    "v_mfma_f32_16x16x32_bf16 v[52:55], v[112:115], v[120:123], v[52:55]\n" \
    "v_cvt_pk_bf16_f32 v118, v4, v5\n" \
    "v_cvt_pk_bf16_f32 v119, v6, v7\n" \
    "s_waitcnt lgkmcnt(1)\n" \
    "v_mfma_f32_16x16x32_bf16 v[0:3], v[80:83], v[64:67], 0\n" \
    "v_exp_f32 v8, v8\n" \
    "v_exp_f32 v9, v9\n" \
    "v_mfma_f32_16x16x32_bf16 v[4:7], v[84:87], v[64:67], 0\n" \
    "ds_read_b128 v[92:95], v125 offset:3072\n" \
    "v_exp_f32 v10, v10\n" \
    "v_exp_f32 v11, v11\n" \
    "v_mfma_f32_16x16x32_bf16 v[32:35], v[104:107], v[116:119], v[32:35]\n" \
    "v_cvt_pk_bf16_f32 v120, v8, v9\n" \
    "v_exp_f32 v12, v12\n" \
    "v_exp_f32 v13, v13\n" \
    "v_mfma_f32_16x16x32_bf16 v[36:39], v[108:111], v[116:119], v[36:39]\n" \
    "v_cvt_pk_bf16_f32 v121, v10, v11\n" \
    "v_exp_f32 v14, v14\n" \
    "v_exp_f32 v15, v15\n" \
    "v_mfma_f32_16x16x32_bf16 v[56:59], v[112:115], v[116:119], v[56:59]\n" \
    "v_cvt_pk_bf16_f32 v122, v12, v13\n" \
    "v_cvt_pk_bf16_f32 v123, v14, v15\n" \
    "v_mov_b32 v124, v125\n" \
    "s_add_u32 s24, s24, 0x4000\n" \
    "s_and_b32 s24, s24, 0xffff\n" \
    "v_add_u32 v125, s24, v127\n" \
    "s_sub_u32 s23, s23, 1\n" \
    "s_cmp_lg_u32 s23, 0\n" \
    "s_cbranch_scc1 ATT_LOOP_%=\n" \
    "v_mfma_f32_16x16x32_bf16 v[40:43], v[104:107], v[120:123], v[40:43]\n" \
    "v_mfma_f32_16x16x32_bf16 v[44:47], v[108:111], v[120:123], v[44:47]\n" \
    "v_mfma_f32_16x16x32_bf16 v[60:63], v[112:115], v[120:123], v[60:63]\n" \
    "s_waitcnt vmcnt(0) lgkmcnt(0)\n" \
    "s_nop 15\n"

constexpr int Q_NS = 3, Q_SLOT = 24576;
__device__ __forceinline__ void attention(int b, const char* x, const char* wq, char* ws, lds_ptr lds, int w, int lane) {
    const int xc = b & 7, t = b >> 3, st128 = (xc >> 1) * 4 + (t & 3), hp = (xc & 1) * 8 + (t >> 2);
    const int head = 2 * hp + (w & 1), s0 = st128 * 128 + (w >> 1) * 64, g = lane >> 4, c = lane & 15;
    const u32x4 srd = make_srd(ws + WS_KV, S * 128);
    const u32 voff = w * 4096 + lane * 16, ldst = (u32)(uintptr_t)lds + w * 4096;
    auto dma = [&](int st, int p0, int p1) {
#pragma unroll
        for (int p = p0; p < p1; ++p) dma16(ldst + ((st + 2) & 3) * KV_STAGE + p * 1024, voff, srd, st * KV_STAGE + p * 1024);
    };
    __builtin_amdgcn_s_setprio(2);
    float gate[4];
#pragma unroll
    for (int i = 0; i < 4; ++i) asm volatile("global_load_dword %0, %1, off" : "=v"(gate[i]) : "v"(ws + WS_G + (s0 + i * 16 + c) * 4) : "memory");
    f32x4 kb2;
    asm volatile("global_load_dwordx4 %0, %1, off" : "=v"(kb2) : "v"(ws + WS_BND + lane * 16) : "memory");
    u32x4 q[4];
    {
        OpTr<64, D * 2, 2, true, 2> a; a.init(wq, D * D * 2, hp * 64, 0, lds, w, (w & 1) * 2, lane);
        OpRow bb; bb.init(x, S * D * 2, st128 * 128, lds + a.BYTES, w, (w >> 1) * 64, lane);
        Engine<Q_NS, 2, 4, 2> e;
        static_assert((D / 64 - 1) % Q_NS == 0 && Q_SLOT <= 2 * KV_STAGE, "the last Q slot lies below the first two K/V stages");
        gemm_run<Q_SLOT, D / 64, 2>(e, a, bb, [] {}, [](int) {}, [&](auto p) { if constexpr (p.v == Rounds<Q_NS, D / 64>::TAIL - 2) { dma(0, 0, 4); dma(1, 0, 4); } });
        wait_bar<4>();
        dma(2, 0, 2);
#pragma unroll
        for (int i = 0; i < 4; ++i) q[i] = pack8(e.acc[0][i] * QSCALE, e.acc[1][i] * QSCALE);
    }
    PIN(kb2);
    const float k2 = wave_max(fmaxf(fmaxf(kb2[0], kb2[1]), fmaxf(kb2[2], kb2[3])));
    bool ok = true;
#pragma unroll
    for (int i = 0; i < 4; ++i) {
        float n = 0.f;
#pragma unroll
        for (int m = 0; m < 4; ++m) { const float a = bf_lo(q[i][m]), h = bf_hi(q[i][m]); n += a * a + h * h; }
        ok = ok && group_sum(n) * k2 * 1.02f <= SAFE_LOG2 * SAFE_LOG2;
        PIN(gate[i]);
    }
    const bool safe = __all(ok);
    __builtin_amdgcn_s_setprio(0);
    f32x4 o[8];
#pragma unroll
    for (int i = 0; i < 8; ++i) o[i] = f32x4{};
    float lsum[4];
    const lds_ptr base = lds + lane * 16;
    const f32x4 z = {};
    if (safe) {
        const u32 one = c == 0 ? 0x3f803f80u : 0u;
        const u32x4 ones = {one, one, one, one};
        const lds_ptr first = base + 2 * KV_STAGE;
        u32x4 ka = lds_rd128(first), kb = lds_rd128(first + 1024), v0 = lds_rd128(first + 2048), v1 = lds_rd128(first + 3072);
        f32x4 sa = M16(ka, q[0], z), sb = M16(kb, q[0], z);
        f32x16 oa = {}, ob = {}, l = {};
        u32x4 vz0 = {}, vz1 = {}, pz = {};
        u32 cur = (u32)(uintptr_t)first, nxt = cur + KV_STAGE;
        asm volatile("s_nop 7\n" ATT_LOOP_ASM
                     : "+{v[0:3]}"(sa), "+{v[4:7]}"(sb), "+{v[16:31]}"(oa), "+{v[32:47]}"(ob), "+{v[48:63]}"(l), "+{v[80:83]}"(ka), "+{v[84:87]}"(kb),
                       "+{v[88:91]}"(v0), "+{v[92:95]}"(v1), "+{v[104:107]}"(vz0), "+{v[108:111]}"(vz1), "+{v[120:123]}"(pz), "+{v124}"(cur), "+{v125}"(nxt)
                     : "{v[64:67]}"(q[0]), "{v[68:71]}"(q[1]), "{v[72:75]}"(q[2]), "{v[76:79]}"(q[3]), "{v[112:115]}"(ones), "{v126}"(voff),
                       "{v127}"((u32)(uintptr_t)base), [srd] "s"(srd), [ldst] "s"(ldst)
                     : "v8", "v9", "v10", "v11", "v12", "v13", "v14", "v15", "v96", "v97", "v98", "v99", "v100", "v101", "v102", "v103",
                       "v116", "v117", "v118", "v119", "s20", "s21", "s22", "s23", "s24", "scc", "memory");
#pragma unroll
        for (int i = 0; i < 4; ++i) {
            lsum[i] = __shfl(l[4 * i], c);
            o[i] = f32x4{oa[4 * i], oa[4 * i + 1], oa[4 * i + 2], oa[4 * i + 3]};
            o[4 + i] = f32x4{ob[4 * i], ob[4 * i + 1], ob[4 * i + 2], ob[4 * i + 3]};
        }
    } else {
        float mx[4];
#pragma unroll
        for (int i = 0; i < 4; ++i) { mx[i] = -__builtin_inff(); lsum[i] = 0.f; }
        dma(2, 2, 4);
        for (int st = 0; st < KV_NSTAGE; ++st) {
            wait_bar<4>();
            dma(st + 3, 0, 4);
#pragma unroll 1
            for (int kb = 0; kb < 4; ++kb) {
                const lds_ptr p = base + ((st + 2) & 3) * KV_STAGE + kb * 4096;
                const u32x4 k0 = lds_rd128(p), k1 = lds_rd128(p + 1024), v0 = lds_rd128(p + 2048), v1 = lds_rd128(p + 3072);
#pragma unroll
                for (int i = 0; i < 4; ++i) {
                    f32x4 sa = M16(k0, q[i], z), sb = M16(k1, q[i], z);
                    float m = mx[i];
#pragma unroll
                    for (int r = 0; r < 4; ++r) m = fmaxf(m, fmaxf(sa[r], sb[r]));
                    m = fmaxf(m, __shfl_xor(m, 16)); m = fmaxf(m, __shfl_xor(m, 32));
                    const float alpha = EX2(mx[i] - m);
                    mx[i] = m;
                    float t = 0.f;
#pragma unroll
                    for (int r = 0; r < 4; ++r) { sa[r] = EX2(sa[r] - m); sb[r] = EX2(sb[r] - m); t += sa[r] + sb[r]; }
                    lsum[i] = lsum[i] * alpha + t;
                    const u32x4 pp = pack8(sa, sb);
                    o[2 * i] = M16(v0, pp, o[2 * i] * alpha);
                    o[2 * i + 1] = M16(v1, pp, o[2 * i + 1] * alpha);
                }
            }
        }
        asm volatile("s_waitcnt vmcnt(0)" ::: "memory");
#pragma unroll
        for (int i = 0; i < 4; ++i) lsum[i] = group_sum(lsum[i]);
    }
#pragma unroll
    for (int i = 0; i < 4; ++i) {
        const float r = gate[i] / lsum[i];
        store_wt(ws + WS_OF, S * D * 2, (u32)((head * 4 + g) * S + s0 + i * 16 + c) * 16u, pack8(o[2 * i] * r, o[2 * i + 1] * r));
    }
    publish(ws, CNT_O + st128);
}

__device__ __forceinline__ f32x2 gelu_tanh(f32x2 x) {
    constexpr float C0 = -2.f * 0.7978845608028654f * 1.4426950408889634f, C1 = C0 * 0.044715f;
    const f32x2 y = x * __builtin_elementwise_fma(x * x, f32x2{C1, C1}, f32x2{C0, C0});
    const f32x2 d = f32x2{EX2(y[0]), EX2(y[1])} + 1.f;
    return x * f32x2{__builtin_amdgcn_rcpf(d[0]), __builtin_amdgcn_rcpf(d[1])};
}

__device__ __forceinline__ void gemm1_tile(int b, const void* w1, const char* b1, char* ws, lds_ptr lds, int w, int lane) {
    const int x = b & 7, t = b >> 3;
    const int dt = (x & 1) * 4 + (t & 3), d0 = dt * 128, f0 = ((x >> 1) * 7 + (t >> 2)) * 128;
    const int g = lane >> 4, c = lane & 15;
    u32x2 bias[8];
#pragma unroll
    for (int j = 0; j < 8; ++j) bias[j] = *(const u32x2*)(b1 + (f0 + j * 16 + g * 4) * 2);
    OpReg<2, D, false> a; a.init(ws + WS_HF, S * D * 2, d0 + w * 32, 0, lane);
    OpTr<128, F * 2, 1, false, 8> bb; bb.init(w1, S * F * 2, f0, 0, lds, w, 0, lane);
    Engine<G1_NS, 2, 8> e;
    const u32 scratch = (u32)(uintptr_t)lds + G1_NS * 8192 + w * 256;
    gemm_run<8192, S / 32, 0>(e, a, bb, [] {}, [&](int r) { if (r == 1) touch<S * F * 2 - W1_PRE, MID_G1, 256>((const char*)w1 + W1_PRE, b, scratch); }, [](auto) {});
    auto& acc = e.acc;
#pragma unroll
    for (int jp = 0; jp < 4; ++jp)
#pragma unroll
        for (int i = 0; i < 2; ++i) {
            f32x4 u[2];
#pragma unroll
            for (int h = 0; h < 2; ++h) {
                const u32x2 bv = bias[2 * jp + h];
                const f32x4 v = acc[i][2 * jp + h] + f32x4{bf_lo(bv[0]), bf_hi(bv[0]), bf_lo(bv[1]), bf_hi(bv[1])};
                const f32x2 lo = gelu_tanh(f32x2{v[0], v[1]}), hi = gelu_tanh(f32x2{v[2], v[3]});
                u[h] = f32x4{lo[0], lo[1], hi[0], hi[1]};
            }
            store_wt(ws + WS_UF, F * D * 2, (u32)(((f0 / 32 + jp) * 4 + g) * D + d0 + w * 32 + i * 16 + c) * 16u, pack8(u[0], u[1]));
        }
    publish(ws, CNT_U + dt * 2 + (x >> 2));
}

constexpr int P1_NS = 8, P1_BASE = 8192, P2_NS = 6, P2_SLOT = 12288;

__device__ __forceinline__ void post_tile(int b, const char* w2, const char* b2, const char* wo, char* ws, char* out, lds_ptr lds, int w, int lane) {
    const int x = b & 7, t = b >> 3, h = t & 1;
    const int s0 = ((x >> 1) * 8 + (t & 7)) * 64, dt = (x & 1) * 4 + (t >> 3), d0 = dt * 128;
    const int g = lane >> 4, c = lane & 15;
    constexpr int N1 = F / 64, N2 = D / 32;
    static_assert(N1 % P1_NS == 0 && P1_BASE + (P1_NS - 1) * 8192 >= (P2_NS - 1) * P2_SLOT, "the last w2 slot lies in the last wo slot");
    float omg[4], bs[4];
#pragma unroll
    for (int j = 0; j < 4; ++j) {
        const int s = s0 + j * 16 + c;
        asm volatile("global_load_dword %0, %1, off" : "=v"(omg[j]) : "v"(ws + WS_G + s * 4) : "memory");
        asm volatile("global_load_ushort %0, %1, off" : "=v"(bs[j]) : "v"(b2 + s * 2) : "memory");
    }
    OpTr<128, D * 2, 1, false, 2> a2; a2.init(wo, D * D * 2, d0, 0, lds, w, w * 2, lane);
    OpFrag<64, S> b2o; b2o.init(ws + WS_OF, S * D * 2, s0, lds + 8192, w, 0, lane);
    Engine<P2_NS, 2, 4> e;
    auto& acc = e.acc;
    u32 seen = 0;
    {
        OpReg<2, D, true> a1; a1.init(ws + WS_UF, F * D * 2, d0 + w * 32, h * N1, lane);
        OpTr<128, S * 2, 1, false, 8> b1; b1.init(w2, F * S * 2, s0 & ~127, h * N1, lds + P1_BASE, w, 4 * h, lane);
        Engine<P1_NS, 2, 8> e1;
        constexpr int TAIL = Rounds<P1_NS, N1>::TAIL;
        gemm_run<8192, N1, 2>(e1, a1, b1,
            [&] { if (w == 0) await(ws, CNT_U + dt * 2 + h, F / 256); __builtin_amdgcn_s_barrier(); },
            [&](int r) { if (r == Rounds<P1_NS, N1>::N - 1) sample(seen, ws, CNT_O + (s0 >> 7)); },
            [&](auto p) { if constexpr (p.v == TAIL - 2) { confirm(seen, ws, CNT_O + (s0 >> 7), 16); gemm_fill<P2_SLOT>(a2, b2o, 0, P2_NS - 1); } });
#pragma unroll
        for (int j = 0; j < 4; ++j) {
            PIN(omg[j]); PIN(bs[j]);
            omg[j] = bf_round(1.f - omg[j]); bs[j] = __uint_as_float(__float_as_uint(bs[j]) << 16);
#pragma unroll
            for (int i = 0; i < 2; ++i) {
                store_wt(ws + WS_X, 256u << 15, (u32)(((b * 4 + w) * 8 + i * 4 + j) * 64 + lane) * 16u, __builtin_bit_cast(u32x4, e1.acc[i][4 + j]));
                acc[i][j] = (e1.acc[i][j] + bs[j]) * omg[j];
            }
        }
    }
    constexpr int PPW = 3, XW = 8;
    wait_bar<(P2_NS - 2) * PPW + XW>();
    gemm_fill<P2_SLOT>(a2, b2o, P2_NS - 1, P2_NS);
    gemm_first(e, a2, b2o);
    gemm_steps<0, P2_NS, MANY, P2_SLOT, 2, XW>(e, a2, b2o, P2_NS, [&](auto p) { if constexpr (p.v == P2_NS - 2) signal(ws, CNT_X + b); });
    for (int r = 1; r < 3; ++r) gemm_steps<0, P2_NS, MANY, P2_SLOT, 2, 0>(e, a2, b2o, (r + 1) * P2_NS, [](auto) {});
    seen = 0;
    u32x4 got[8];
    const u32x4 xsrd = make_srd(ws + WS_X, 256u << 15);
    gemm_steps<0, N2 - 3 * P2_NS, N2 - 3 * P2_NS, P2_SLOT, 2, 0>(e, a2, b2o, 4 * P2_NS, [&](auto p) {
        if constexpr (p.v == 0) sample(seen, ws, CNT_X + (b ^ 8));
        if constexpr (p.v == P2_NS - 2) {
            confirm(seen, ws, CNT_X + (b ^ 8), 1);
#pragma unroll
            for (int i = 0; i < 8; ++i) {
                got[i] = u32x4{};
                load_coh(got[i], lane * 16, xsrd, (u32)(((b ^ 8) * 4 + w) * 8 + i) * 1024u);
            }
        }
    });
    asm volatile("s_waitcnt vmcnt(0)" ::: "memory");
#pragma unroll
    for (int i = 0; i < 8; ++i) { PIN(got[i]); acc[i >> 2][i & 3] += __builtin_bit_cast(f32x4, got[i]) * omg[i & 3]; }
#pragma unroll
    for (int j = 0; j < 4; ++j) {
        const auto r0 = __builtin_amdgcn_permlane16_swap(cvtpk(acc[0][j][0], acc[0][j][1]), cvtpk(acc[1][j][0], acc[1][j][1]), false, false);
        const auto r1 = __builtin_amdgcn_permlane16_swap(cvtpk(acc[0][j][2], acc[0][j][3]), cvtpk(acc[1][j][2], acc[1][j][3]), false, false);
        const int s = s0 + j * 16 + c, d = d0 + w * 32 + (g & 1) * 16 + (g >> 1) * 8;
        *(u32x4*)(out + ((size_t)s * D + d) * 2) = u32x4{r0[0], r1[0], r0[1], r1[1]};
    }
}

constexpr int MAIN_LDS = Q_NS * Q_SLOT;
static_assert(MAIN_LDS >= P2_NS * P2_SLOT && MAIN_LDS >= P1_BASE + P1_NS * 8192 && MAIN_LDS >= 4 * KV_STAGE && 2 * MAIN_LDS <= 160 * 1024, "two workgroups per CU");

__global__ void __launch_bounds__(256, 2) k_main(const char* __restrict__ x, const char* __restrict__ wq, const char* __restrict__ w1, const char* __restrict__ b1, const char* __restrict__ w2, const char* __restrict__ b2,
                                                 const char* __restrict__ wo, char* __restrict__ ws, char* __restrict__ out) {
    extern __shared__ __attribute__((aligned(16))) char smem[];
    const int lane = threadIdx.x & 63, w = __builtin_amdgcn_readfirstlane(threadIdx.x >> 6), b = blockIdx.x;
    asm volatile("main_code_begin:");
    if (b < MID_ATT) {
        attention(b, x, wq, ws, (lds_ptr)smem, w, lane);
    } else {
        const int gb = b - MID_ATT;
        __builtin_amdgcn_s_setprio(1);
        if (gb < MID_G1) gemm1_tile(gb, w1, b1, ws, (lds_ptr)smem, w, lane);
        else {
            const u32 scratch = (u32)(uintptr_t)(lds_ptr)smem + w * 256;
            const int part = (gb - MID_G1) >> 3;
            constexpr u32 WS_HALF = (WS_X + (256u << 15) - WS_OF) / 2;
            if (part == 0) { touch_pages(w1, S * F * 2, scratch); touch_pages(wo, D * D * 2, scratch); }
            else if (part == 1) { touch_pages(w2, S * F * 2, scratch); touch_pages(out, S * D * 2, scratch); }
            else touch_pages(ws + WS_OF + (part - 2) * WS_HALF, WS_HALF, scratch);
            for (int i = 0; i < 5; ++i) __builtin_amdgcn_s_sleep(64);
            for (int i = 0; i < 4; ++i) {
                touch<F * S * 2, MID_PF * 4, 256>(w2, (gb - MID_G1) * 4 + i, scratch);
                asm volatile("s_waitcnt vmcnt(0)" ::: "memory");
            }
            touch<D * D * 2, MID_PF, 256>(wo, gb - MID_G1, scratch);
            wait_bar<0>();
        }
        __builtin_amdgcn_s_setprio(0);
        post_tile(gb, w2, b2, wo, ws, out, (lds_ptr)smem, w, lane);
    }
    asm volatile("main_code_end:");
}

extern "C" void kernel_launch(void* const* d_in, const int* in_sizes, int n_in, void* d_out, int out_size, void* d_ws, size_t ws_size,
                              hipStream_t stream) {
    const char* x = (const char*)d_in[0];
    const char *wq = (const char*)d_in[1], *wk = (const char*)d_in[2], *wv = (const char*)d_in[3], *wo = (const char*)d_in[4];
    const char *w1 = (const char*)d_in[6], *b1 = (const char*)d_in[7], *w2 = (const char*)d_in[8], *b2 = (const char*)d_in[9];
    const char* gw = (const char*)d_in[10];
    char* ws = (char*)d_ws;
    (void)hipFuncSetAttribute((const void*)k_pre, hipFuncAttributeMaxDynamicSharedMemorySize, PRE_LDS);
    (void)hipFuncSetAttribute((const void*)k_main, hipFuncAttributeMaxDynamicSharedMemorySize, MAIN_LDS);
    k_pre<<<PRE_KV + PRE_LN, 512, PRE_LDS, stream>>>(x, wq, wk, wv, gw, w1, ws);
    k_main<<<MID_ATT + MID_G1 + MID_PF, 256, MAIN_LDS, stream>>>(x, wq, w1, b1, w2, b2, wo, ws, (char*)d_out);
}
```

```cpp
#include <hip/hip_runtime.h>
#include <stdint.h>

typedef unsigned u32;
typedef u32 u32x4 __attribute__((ext_vector_type(4)));
typedef u32 u32x2 __attribute__((ext_vector_type(2)));
typedef float f32x2 __attribute__((ext_vector_type(2)));
typedef float f32x4 __attribute__((ext_vector_type(4)));
typedef float f32x16 __attribute__((ext_vector_type(16)));
typedef __bf16 bf16x2 __attribute__((ext_vector_type(2)));
typedef __bf16 bf16x8 __attribute__((ext_vector_type(8)));
typedef short s16x4 __attribute__((ext_vector_type(4)));
#define LDS __attribute__((address_space(3)))
typedef LDS char* lds_ptr;

constexpr int S = 2048, D = 1024, F = 3584;
constexpr u32 WS_HF = 0, WS_OF = 8u << 20, WS_UF = 12u << 20, WS_KV = 20u << 20;
constexpr u32 WS_G = WS_KV + (256u << 10), WS_BND = WS_G + (8u << 10), WS_CNT = WS_BND + (4u << 10);
constexpr u32 WS_X = 24u << 20;
constexpr int CNT_U = 0, CNT_O = 16, CNT_X = 32, CNT_N = 288, CNT_STRIDE = 256;
constexpr float QSCALE = 0.17677669529663687f * 1.4426950408889634f;
constexpr float SAFE_LOG2 = 60.f;
constexpr float SAFE_V = 1.0e12f;

#define SB() __builtin_amdgcn_sched_barrier(0)
#define PIN(x) asm volatile("" : "+v"(x))
#define M16(a, b, c) __builtin_amdgcn_mfma_f32_16x16x32_bf16(__builtin_bit_cast(bf16x8, a), __builtin_bit_cast(bf16x8, b), c, 0, 0, 0)
__device__ __forceinline__ void mma16(f32x4& c, const u32x4& a, const u32x4& b) { asm volatile("v_mfma_f32_16x16x32_bf16 %0, %1, %2, %0" : "+v"(c) : "v"(a), "v"(b)); }
__device__ __forceinline__ void mfma_settle() { asm volatile("s_nop 7\n\ts_nop 1" ::: "memory"); SB(); }
#define EX2(x) __builtin_amdgcn_exp2f(x)

__device__ __forceinline__ u32 cvtpk(float lo, float hi) { f32x2 v = {lo, hi}; return __builtin_bit_cast(u32, __builtin_convertvector(v, bf16x2)); }
__device__ __forceinline__ float bf_lo(u32 v) { return __uint_as_float(v << 16); }
__device__ __forceinline__ float bf_hi(u32 v) { return __uint_as_float(v & 0xffff0000u); }
__device__ __forceinline__ float bf_round(float x) { return bf_lo(cvtpk(x, x)); }
__device__ __forceinline__ u32x4 pack8(const f32x4& a, const f32x4& b) { return u32x4{cvtpk(a[0], a[1]), cvtpk(a[2], a[3]), cvtpk(b[0], b[1]), cvtpk(b[2], b[3])}; }

__device__ __forceinline__ u32x4 make_srd(const void* p, u32 bytes) {
    const uint64_t a = (uint64_t)p;
    return u32x4{(u32)a, (u32)(a >> 32) & 0xffffu, bytes, 0x00020000u};
}
__device__ __forceinline__ void dma16(u32 lds, u32 voff, const u32x4& srd, u32 soff) {
    asm volatile("s_mov_b32 m0, %0\n\ts_nop 0\n\tbuffer_load_dwordx4 %1, %2, %3 offen lds" ::"s"(lds), "v"(voff), "s"(srd), "s"(soff) : "memory");
}
__device__ __forceinline__ void dma16_coh(u32 lds, u32 voff, const u32x4& srd, u32 soff) {
    asm volatile("s_mov_b32 m0, %0\n\ts_nop 0\n\tbuffer_load_dwordx4 %1, %2, %3 offen sc1 lds" ::"s"(lds), "v"(voff), "s"(srd), "s"(soff) : "memory");
}
template <int N> __device__ __forceinline__ void wait_bar() {
    __builtin_amdgcn_s_waitcnt(0xc07f);
    asm volatile("s_waitcnt vmcnt(%0)\n\ts_barrier" ::"n"(N) : "memory");
}
__device__ __forceinline__ u32x4 lds_rd128(lds_ptr p) { return *(LDS const u32x4*)p; }
__device__ __forceinline__ u32x2 lds_rdtr(lds_ptr p) { return __builtin_bit_cast(u32x2, __builtin_amdgcn_ds_read_tr16_b64_v4i16((LDS s16x4*)p)); }

typedef __attribute__((address_space(1))) u32 gu32;
__device__ __forceinline__ void store_wt(char* base, u32 bytes, u32 off, const u32x4& v) {
    __builtin_amdgcn_raw_buffer_store_b128(v, __builtin_amdgcn_make_buffer_rsrc(base, 0, bytes, 0x00020000), off, 0, 16);
}
__device__ __forceinline__ void load_coh(u32x4& f, u32 voff, const u32x4& srd, u32 soff) {
    asm volatile("buffer_load_dwordx4 %0, %1, %2, %3 offen sc1" : "+v"(f) : "v"(voff), "s"(srd), "s"(soff) : "memory");
}
__device__ __forceinline__ void signal(char* ws, int cnt) {
    if (threadIdx.x == 0) __hip_atomic_fetch_add((gu32*)(ws + WS_CNT + cnt * CNT_STRIDE), 1u, __ATOMIC_RELAXED, __HIP_MEMORY_SCOPE_AGENT);
}
__device__ __forceinline__ void publish(char* ws, int cnt) {
    asm volatile("s_waitcnt vmcnt(0)\n\ts_barrier" ::: "memory");
    signal(ws, cnt);
}
__device__ __forceinline__ void await(char* ws, int cnt, u32 target) {
    gu32* p = (gu32*)(ws + WS_CNT + cnt * CNT_STRIDE);
    for (int spins = 0; spins < (1 << 20); ++spins) {
        if (__builtin_amdgcn_readfirstlane(__hip_atomic_load(p, __ATOMIC_RELAXED, __HIP_MEMORY_SCOPE_AGENT)) >= target) break;
        __builtin_amdgcn_s_sleep(1);
    }
    asm volatile("" ::: "memory");
}

__device__ __forceinline__ void sample(u32& seen, char* ws, int cnt) {
    asm volatile("global_load_dword %0, %1, off sc1" : "+v"(seen) : "v"(ws + WS_CNT + cnt * CNT_STRIDE) : "memory");
}
__device__ __forceinline__ void confirm(u32& seen, char* ws, int cnt, u32 target) {
    PIN(seen);
    if (__builtin_amdgcn_readfirstlane(seen) < target) await(ws, cnt, target);
}


template <int R, int NR> struct OpFrag {
    static constexpr int BYTES = 4 * R * 16, PPW = BYTES / 4096, RPF = 1;
    static constexpr bool REG = false;
    u32x4 srd; u32 voff, ldst; lds_ptr rd;
    __device__ __forceinline__ void init(const void* base, u32 bytes, int row0, lds_ptr img, int w, int wrow, int lane) {
        srd = make_srd(base, bytes);
        voff = (u32)(row0 + lane + w * NR) * 16u;
        ldst = (u32)(uintptr_t)img + w * (R * 16);
        rd = img + ((lane >> 4) * R + wrow + (lane & 15)) * 16;
    }
    __device__ __forceinline__ void issue(int slot, u32 pos, int i) const { dma16_coh(ldst + pos + i * 1024, voff, srd, (u32)slot * (4u * NR * 16u) + i * 1024u); }
    __device__ __forceinline__ void read(u32x4& f, int pos, int i, int) const { f = lds_rd128(rd + pos + i * 256); }
};

template <int C, int RS, int KS, bool NAT, int NF> struct OpTr {
    static constexpr int BYTES = KS * 32 * C * 2, PPW = BYTES / 4096, RPF = 2;
    static constexpr bool REG = false;
    u32x4 srd; u32 voff, ldst; lds_ptr rd[NF];
    __device__ __forceinline__ void init(const void* base, u32 bytes, int col0, int k0, lds_ptr img, int w, int wch, int lane) {
        srd = make_srd(base, bytes);
        ldst = (u32)(uintptr_t)img + w * 1024;
        const int g = lane >> 4, q = (lane >> 2) & 3, p = lane & 3;
        if constexpr (C == 128) {
            const int f = NAT ? (g | (((w >> 1) & 1) << 2)) : (4 * (w & 1) + g);
            voff = (u32)(32 * k0 + 4 * w + g) * RS + col0 * 2 + ((((lane & 15) >> 1) ^ f) * 32) + (lane & 1) * 16;
            const int row = NAT ? 8 * g + q : 4 * g + q, fr = NAT ? (q | ((g & 1) << 2)) : (row & 7);
#pragma unroll
            for (int i = 0; i < NF; ++i) rd[i] = img + row * 256 + (((wch ^ i) ^ fr) * 32) + p * 8;
        } else {
            static_assert(C == 128 || C == 64, "unsupported transposed operand");
            const int f = NAT ? ((g & 1) | ((w & 1) << 1)) : g;
            voff = (u32)(32 * k0 + 8 * w + (lane >> 3)) * RS + col0 * 2 + ((((lane & 7) >> 1) ^ f) * 32) + (lane & 1) * 16;
            const int row = NAT ? 8 * g + q : 4 * g + q, fr = NAT ? ((q >> 1) | ((g & 1) << 1)) : ((row >> 1) & 3);
#pragma unroll
            for (int i = 0; i < NF; ++i) rd[i] = img + row * 128 + (((wch ^ i) ^ fr) * 32) + p * 8;
        }
    }
    __device__ __forceinline__ void issue(int slot, u32 pos, int i) const { dma16(ldst + pos + i * 4096, voff, srd, (u32)(slot * KS * 32 + i * (2048 / C)) * RS); }
    __device__ __forceinline__ void read(u32x4& f, int pos, int i, int t) const {
        const u32x2 v = lds_rdtr(rd[i % NF] + pos + i / NF * (32 * C * 2) + t * ((NAT ? 4 : 16) * C * 2));
        f[2 * t] = v[0]; f[2 * t + 1] = v[1];
    }
};

struct OpRow {
    static constexpr int BYTES = 128 * 128, PPW = 4, RPF = 1;
    static constexpr bool REG = false;
    u32x4 srd; u32 voff, ldst; lds_ptr rd[2];
    __device__ __forceinline__ void init(const void* base, u32 bytes, int row0, lds_ptr img, int w, int wrow, int lane) {
        srd = make_srd(base, bytes);
        ldst = (u32)(uintptr_t)img + w * 1024;
        const int g = lane >> 4, c = lane & 15;
        voff = (u32)(row0 + 8 * w + (lane >> 3)) * (D * 2) + (((lane & 7) ^ (4 * (w & 1) + g)) * 16);
#pragma unroll
        for (int ks = 0; ks < 2; ++ks) rd[ks] = img + (wrow + c) * 128 + (((4 * ks + g) ^ (c >> 1)) * 16);
    }
    __device__ __forceinline__ void issue(int slot, u32 pos, int i) const { dma16(ldst + pos + i * 4096, voff, srd, (u32)slot * 128u + i * (32u * D * 2u)); }
    __device__ __forceinline__ void read(u32x4& f, int pos, int i, int) const { f = lds_rd128(rd[i >> 2] + pos + (i & 3) * 2048); }
};

template <int NF, int NR, bool COH> struct OpReg {
    static constexpr int PPW = NF, RPF = 0;
    static constexpr bool REG = true;
    u32x4 srd; u32 voff;
    __device__ __forceinline__ void init(const void* base, u32 bytes, int row0, int k0, int lane) {
        srd = make_srd(base, bytes);
        voff = (u32)(row0 + (lane & 15) + ((lane >> 4) + 4 * k0) * NR) * 16u;
    }
    __device__ __forceinline__ void load(u32x4& f, int slot, int i) const {
        const u32 soff = (u32)slot * (4u * NR * 16u);
        if (COH) asm volatile("buffer_load_dwordx4 %0, %1, %2, %3 offen offset:%4 sc1" : "+v"(f) : "v"(voff), "s"(srd), "s"(soff), "n"(i * 256) : "memory");
        else asm volatile("buffer_load_dwordx4 %0, %1, %2, %3 offen offset:%4" : "+v"(f) : "v"(voff), "s"(srd), "s"(soff), "n"(i * 256) : "memory");
    }
};

template <int NA, int NB> struct Frags { u32x4 a[NA], b[NB]; };
template <int NS, int NA, int NB, int KS = 1> struct Engine { f32x4 acc[NA][NB]; Frags<NA * KS, NB * KS> f[2]; u32x4 ring[NS][NA]; };

template <int KS, int SWAP, bool READ, bool ISSUE, bool RISSUE, int NA, int NB, class A, class B>
__device__ __forceinline__ void gemm_step(f32x4 (&acc)[NA][NB], const Frags<NA * KS, NB * KS>& cur, Frags<NA * KS, NB * KS>& nxt, u32x4 (&ring)[NA],
                                          u32x4 (&prev)[NA], const A& a, const B& b, int rpos, int islot, u32 ipos) {
    constexpr int NM = NA * NB * KS, NRA = NA * A::RPF, NRK = NRA + NB * B::RPF, NRD = NRK * KS, RPM = (2 * NRD + NM - 1) / NM;
    constexpr int ND = A::PPW + B::PPW, DSTEP = NM / ND, DOFF = 1 % DSTEP;
    if constexpr (A::REG) {
#pragma unroll
        for (int i = 0; i < NA; ++i) PIN(ring[i]);
    }
#pragma unroll
    for (int m = 0; m < NM; ++m) {
        const int i = m % NA, j = m / NA % NB, ks = m / (NA * NB);
        const u32x4 av = A::REG ? ring[i] : cur.a[ks * NA + i];
        if (i >= SWAP) mma16(acc[i][j], cur.b[ks * NB + j], av);
        else mma16(acc[i][j], av, cur.b[ks * NB + j]);
        if constexpr (READ) {
#pragma unroll
            for (int rr = m * RPM; rr < (m + 1) * RPM && rr < NRD; ++rr) {
                const int k = rr / NRK, r = rr % NRK;
                if (r < B::RPF) b.read(nxt.b[k * NB], rpos, k * NB, r);
                else if (r >= B::RPF + NRA) b.read(nxt.b[k * NB + (r - NRA) / B::RPF], rpos, k * NB + (r - NRA) / B::RPF, (r - NRA) % B::RPF);
                else if constexpr (!A::REG) a.read(nxt.a[k * NA + (r - B::RPF) / A::RPF], rpos, k * NA + (r - B::RPF) / A::RPF, (r - B::RPF) % A::RPF);
            }
        }
        if (m % DSTEP == DOFF && m / DSTEP < ND) {
            const int d = m / DSTEP;
            if (d < B::PPW) { if constexpr (ISSUE) b.issue(islot, ipos, d); }
            else if constexpr (A::REG) { if constexpr (RISSUE) a.load(prev[d - B::PPW], islot - 1, d - B::PPW); }
            else if constexpr (ISSUE) a.issue(islot, ipos, d - B::PPW);
        }
        SB();
    }
}

template <int V> struct IC { static constexpr int v = V; };
template <int NS, int PA, int PB, bool REG> constexpr int younger(int rem) {
    if (rem == 2) return 0;
    int n = 0;
    for (int k = 1; k <= NS - 2; ++k) n += (rem + k > NS ? PB + (REG ? 0 : PA) : 0) + (REG && rem + k > NS - 1 ? PA : 0);
    return n;
}
template <int P, int COUNT, int REM, int SLOTB, int SWAP, int XW, int NS, int NA, int NB, int KS, class A, class B, class Hook>
__device__ __forceinline__ void gemm_steps(Engine<NS, NA, NB, KS>& e, const A& a, const B& b, int islot, const Hook& hook) {
    constexpr bool READ = REM > 1;
    if constexpr (READ) wait_bar<younger<NS, A::PPW, B::PPW, A::REG>(REM) + (P < NS - 2 ? XW : 0)>();
    hook(IC<P>{});
    gemm_step<KS, SWAP, READ, (REM > NS), (A::REG && REM > NS - 1)>(e.acc, e.f[P & 1], e.f[(P & 1) ^ 1], e.ring[P % NS], e.ring[(P + NS - 1) % NS], a, b,
                                                                   (P + 1) % NS * SLOTB, islot + P, P % NS * SLOTB);
    if constexpr (COUNT > 1) gemm_steps<P + 1, COUNT - 1, REM - 1, SLOTB, SWAP, XW>(e, a, b, islot, hook);
    else if constexpr (REM == 1) mfma_settle();
}

template <int SLOTB, class A, class B> __device__ __forceinline__ void gemm_fill(const A& a, const B& b, int p0, int p1) {
#pragma unroll
    for (int p = p0; p < p1; ++p) {
#pragma unroll
        for (int i = 0; i < B::PPW; ++i) b.issue(p, p * SLOTB, i);
        if constexpr (!A::REG) {
#pragma unroll
            for (int i = 0; i < A::PPW; ++i) a.issue(p, p * SLOTB, i);
        }
    }
}
template <int NS, int NA, int NB, int KS, class A, class B> __device__ __forceinline__ void gemm_first(Engine<NS, NA, NB, KS>& e, const A& a, const B& b) {
    if constexpr (!A::REG) {
#pragma unroll
        for (int i = 0; i < KS * NA * A::RPF; ++i) a.read(e.f[0].a[i / A::RPF], 0, i / A::RPF, i % A::RPF);
    }
#pragma unroll
    for (int i = 0; i < KS * NB * B::RPF; ++i) b.read(e.f[0].b[i / B::RPF], 0, i / B::RPF, i % B::RPF);
    SB();
}

constexpr int MANY = 1 << 20;
template <int NS, int NSLOTS> struct Rounds { static constexpr int RL = NS % 2 ? 2 * NS : NS, N = (NSLOTS - NS) / RL, TAIL = NSLOTS - N * RL; };
template <int SLOTB, int NSLOTS, int SWAP, int NS, int NA, int NB, int KS, class A, class B, class Gate, class Round, class Tail>
__device__ __forceinline__ void gemm_run(Engine<NS, NA, NB, KS>& e, const A& a, const B& b, const Gate& gate, const Round& round, const Tail& tail) {
    using R = Rounds<NS, NSLOTS>;
#pragma unroll
    for (int i = 0; i < NA; ++i)
#pragma unroll
        for (int j = 0; j < NB; ++j) e.acc[i][j] = f32x4{};
    gemm_fill<SLOTB>(a, b, 0, NS);
    if constexpr (A::REG) {
        gate();
#pragma unroll
        for (int p = 0; p < NS; ++p)
#pragma unroll
            for (int i = 0; i < NA; ++i) { e.ring[p][i] = u32x4{}; if (p < NS - 1) a.load(e.ring[p][i], p, i); }
        wait_bar<NA>();
    } else {
        wait_bar<(NS - 1) * (A::PPW + B::PPW)>();
    }
    gemm_first(e, a, b);
    for (int r = 0; r < R::N; ++r) { round(r); gemm_steps<0, R::RL, MANY, SLOTB, SWAP, 0>(e, a, b, r * R::RL + NS, [](auto) {}); }
    gemm_steps<0, R::TAIL, R::TAIL, SLOTB, SWAP, 0>(e, a, b, R::N * R::RL + NS, tail);
}

constexpr int PRE_KV = 128, PRE_LN = 128, PRE_LDS = 65536 + 1024 + 2048, W1_PRE = S / 2 * F * 2;

template <int CTRL> __device__ __forceinline__ float dpp_add(float v) {
    return v + __builtin_bit_cast(float, __builtin_amdgcn_update_dpp(0, __builtin_bit_cast(int, v), CTRL, 0xf, 0xf, true));
}
__device__ __forceinline__ float group_sum(float v) {
    const auto a = __builtin_amdgcn_permlane16_swap(__float_as_uint(v), __float_as_uint(v), false, false);
    v = __uint_as_float(a[0]) + __uint_as_float(a[1]);
    const auto b = __builtin_amdgcn_permlane32_swap(__float_as_uint(v), __float_as_uint(v), false, false);
    return __uint_as_float(b[0]) + __uint_as_float(b[1]);
}

__device__ __forceinline__ float row_sum(float v) { return dpp_add<0x140>(dpp_add<0x141>(dpp_add<0x4e>(dpp_add<0xb1>(v)))); }
__device__ __forceinline__ float fold32(float a, float b) {
    const auto r = __builtin_amdgcn_permlane32_swap(__float_as_uint(a), __float_as_uint(b), false, false);
    return __uint_as_float(r[0]) + __uint_as_float(r[1]);
}
__device__ __forceinline__ float fold16(float a, float b) {
    const auto r = __builtin_amdgcn_permlane16_swap(__float_as_uint(a), __float_as_uint(b), false, false);
    return __uint_as_float(r[0]) + __uint_as_float(r[1]);
}
template <int CTRL> __device__ __forceinline__ float dpp_max(float v) {
    return fmaxf(v, __builtin_bit_cast(float, __builtin_amdgcn_update_dpp(0, __builtin_bit_cast(int, v), CTRL, 0xf, 0xf, true)));
}
__device__ __forceinline__ float wave_max(float v) {
    v = dpp_max<0xb1>(v); v = dpp_max<0x4e>(v); v = dpp_max<0x141>(v); v = dpp_max<0x140>(v);
    const auto a = __builtin_amdgcn_permlane16_swap(__float_as_uint(v), __float_as_uint(v), false, false);
    v = fmaxf(__uint_as_float(a[0]), __uint_as_float(a[1]));
    const auto b = __builtin_amdgcn_permlane32_swap(__float_as_uint(v), __float_as_uint(v), false, false);
    return fmaxf(__uint_as_float(b[0]), __uint_as_float(b[1]));
}

template <u32 BYTES, int NB, int NTHR> __device__ __forceinline__ void touch(const char* p, int b, u32 scratch) {
    constexpr u32 LINES = BYTES / 128, PER = (LINES + NB - 1) / NB, N = (PER + NTHR - 1) / NTHR;
    const u32x4 srd = make_srd(p, BYTES);
#pragma unroll
    for (u32 i = 0; i < N; ++i) {
        const u32 l = i * NTHR + threadIdx.x, off = l < PER ? (b * PER + l) * 128u : 0x80000000u;
        asm volatile("s_mov_b32 m0, %0\n\ts_nop 0\n\tbuffer_load_dword %1, %2, 0 offen lds" ::"s"(scratch), "v"(off), "s"(srd) : "memory");
    }
}

#define CODE_ADDR(v, mark) asm volatile("s_getpc_b64 s[40:41]\n\ts_add_u32 s40, s40, " mark "@rel32@lo+4\n\ts_addc_u32 s41, s41, " mark "@rel32@hi+12" : "={s[40:41]}"(v) :: "scc")
__device__ __forceinline__ void touch_code(uint64_t a, uint64_t e, u32 scratch) {
    const u32x4 srd = make_srd((const void*)a, e > a ? (u32)(e - a) & ~3u : 0u);
    asm volatile("s_mov_b32 m0, %0\n\ts_nop 0\n\tbuffer_load_dword %1, %2, 0 offen lds" ::"s"(scratch), "v"(threadIdx.x * 128u), "s"(srd) : "memory");
}

template <bool V> __device__ __forceinline__ void kv_block(int t, const char* x, const char* wm, char* ws, lds_ptr lds, int w8, int lane) {
    const int g = lane >> 4, c = lane & 15, q = (lane >> 2) & 3, p = lane & 3;
    const lds_ptr img = lds + w8 * 8192;
    const u32x4 srd = make_srd(wm, D * 64);
    const u32 voff = (u32)(lane >> 2) * 64 + ((((lane >> 1) & 1) ^ (lane >> 5)) * 32) + (lane & 1) * 16;
    u32x4 xf[2][4];
#pragma unroll
    for (int ks = 0; ks < 4; ++ks)
#pragma unroll
        for (int j = 0; j < 2; ++j) xf[j][ks] = *(const u32x4*)(x + (size_t)(t * 32 + j * 16 + c) * (D * 2) + (w8 * 128 + ks * 32 + g * 8) * 2);
#pragma unroll
    for (int i = 0; i < 8; ++i) dma16((u32)(uintptr_t)img + i * 1024, voff, srd, w8 * 8192 + i * 1024);
    asm volatile("s_waitcnt vmcnt(0)" ::: "memory");
    f32x4 acc[4];
#pragma unroll
    for (int i = 0; i < 4; ++i) acc[i] = f32x4{};
#pragma unroll
    for (int ks = 0; ks < 4; ++ks)
#pragma unroll
        for (int i = 0; i < 2; ++i) {
            const lds_ptr rd = img + (ks * 32 + 8 * g + q) * 64 + ((i ^ (g & 1)) * 32) + p * 8;
            const u32x2 lo = lds_rdtr(rd), hi = lds_rdtr(rd + 256);
            const u32x4 wf = {lo[0], lo[1], hi[0], hi[1]};
#pragma unroll
            for (int j = 0; j < 2; ++j) acc[2 * i + j] = V ? M16(xf[j][ks], wf, acc[2 * i + j]) : M16(wf, xf[j][ks], acc[2 * i + j]);
        }
#pragma unroll
    for (int i = 0; i < 4; ++i) *(LDS f32x4*)(img + i * 1024 + lane * 16) = acc[i];
    wait_bar<0>();
    if (w8 >= 2) return;
    const int ta = V ? 2 * w8 : w8, tb = V ? 2 * w8 + 1 : w8 + 2, fr = t * 4 + 2 * V + w8;
    f32x4 a = {}, b = {};
#pragma unroll
    for (int v = 0; v < 8; ++v) { a += *(LDS const f32x4*)(lds + v * 8192 + ta * 1024 + lane * 16); b += *(LDS const f32x4*)(lds + v * 8192 + tb * 1024 + lane * 16); }
    store_wt(ws + WS_KV, S * 128, fr * 1024 + lane * 16, pack8(a, b));
    float bnd = 0.f;
    if (!V) {
#pragma unroll
        for (int r = 0; r < 4; ++r) bnd += a[r] * a[r] + b[r] * b[r];
        bnd = group_sum(bnd);
        bnd = bnd == bnd ? bnd : __builtin_inff();
    } else {
#pragma unroll
        for (int r = 0; r < 4; ++r)
            if (!(fabsf(a[r]) <= SAFE_V && fabsf(b[r]) <= SAFE_V)) bnd = __builtin_inff();
    }
    bnd = wave_max(bnd);
    if (lane == 0) ((float*)(ws + WS_BND))[fr] = bnd;
}

__device__ __forceinline__ void ln_rows(int r, const char* x, const char* gw, const char* wq, const char* w1, char* ws, lds_ptr lds, u32 scratch, int w8, int lane) {
    const int u = w8 >> 2, cq = w8 & 3, chunk = 2 * r + u, row0 = 32 * (chunk >> 2) + 4 * (chunk & 3);
    u32x2 xv[8];
    float sh[8];
#pragma unroll
    for (int e = 0; e < 8; ++e) {
        const char* row = x + (size_t)(row0 + (e & 3) + 16 * (e >> 2)) * (D * 2);
        xv[e] = *(const u32x2*)(row + cq * 512 + lane * 8);
        sh[e] = bf_lo(*(const u32*)row);
    }
    u32x2 gv = *(const u32x2*)(gw + cq * 512 + lane * 8);
    PIN(gv);
#pragma unroll
    for (int e = 0; e < 8; ++e) PIN(xv[e]);
    if (r < 16) {
        uint64_t a, e;
        CODE_ADDR(a, "main_code_begin"); CODE_ADDR(e, "main_code_end");
        touch_code(a, e, scratch);
    }
    touch<D * D * 2, PRE_LN, 512>(wq, r, scratch);
    touch<W1_PRE, PRE_LN, 512>(w1, r, scratch);
    float part[3][8];
#pragma unroll
    for (int e = 0; e < 8; ++e) {
        const float a0 = bf_lo(xv[e][0]), a1 = bf_hi(xv[e][0]), a2 = bf_lo(xv[e][1]), a3 = bf_hi(xv[e][1]);
        const float d0 = a0 - sh[e], d1 = a1 - sh[e], d2 = a2 - sh[e], d3 = a3 - sh[e];
        part[0][e] = (d0 + d1) + (d2 + d3);
        part[1][e] = (d0 * d0 + d1 * d1) + (d2 * d2 + d3 * d3);
        part[2][e] = (a0 * bf_lo(gv[0]) + a1 * bf_hi(gv[0])) + (a2 * bf_lo(gv[1]) + a3 * bf_hi(gv[1]));
    }
    LDS float* red = (LDS float*)(lds + 65536);
#pragma unroll
    for (int k = 0; k < 3; ++k)
#pragma unroll
        for (int h = 0; h < 2; ++h) {
            const float v = row_sum(fold16(fold32(part[k][4 * h], part[k][4 * h + 2]), fold32(part[k][4 * h + 1], part[k][4 * h + 3])));
            if ((lane & 15) == 0) red[w8 * 24 + k * 8 + 4 * h + (lane >> 4)] = v;
        }
    wait_bar<63>();
    const int e8 = lane & 7;
    float st[3], mysh = 0.f;
#pragma unroll
    for (int k = 0; k < 3; ++k) st[k] = (red[(4 * u) * 24 + k * 8 + e8] + red[(4 * u + 1) * 24 + k * 8 + e8]) + (red[(4 * u + 2) * 24 + k * 8 + e8] + red[(4 * u + 3) * 24 + k * 8 + e8]);
#pragma unroll
    for (int e = 0; e < 8; ++e) mysh = e8 == e ? sh[e] : mysh;
    const float dm = st[0] * (1.f / D), var = fmaxf(st[1] * (1.f / D) - dm * dm, 0.f);
    const float mean = bf_round(mysh + dm), rs = 1.f / bf_round(sqrtf(bf_round(bf_round(var) + 1e-5f)));
    if (cq == 0 && lane < 8) ((float*)(ws + WS_G))[row0 + (lane & 3) + 16 * (lane >> 2)] = bf_round(1.f / (1.f + __expf(-bf_round(st[2]))));
    float h[4][8];
#pragma unroll
    for (int e = 0; e < 8; ++e) {
        const float m = __uint_as_float(__builtin_amdgcn_readlane(__float_as_uint(mean), e)), q = __uint_as_float(__builtin_amdgcn_readlane(__float_as_uint(rs), e));
        h[0][e] = (bf_lo(xv[e][0]) - m) * q; h[1][e] = (bf_hi(xv[e][0]) - m) * q; h[2][e] = (bf_lo(xv[e][1]) - m) * q; h[3][e] = (bf_hi(xv[e][1]) - m) * q;
    }
    const lds_ptr tb = lds + w8 * 4096;
#pragma unroll
    for (int m = 0; m < 4; ++m)
        *(LDS u32x4*)(tb + lane * 64 + ((m ^ ((lane >> 1) & 3)) * 16)) = u32x4{cvtpk(h[m][0], h[m][1]), cvtpk(h[m][2], h[m][3]), cvtpk(h[m][4], h[m][5]), cvtpk(h[m][6], h[m][7])};
    const u32 dst = chunk * (D * 16) + cq * 4096 + lane * 16;
#pragma unroll
    for (int i = 0; i < 4; ++i) store_wt(ws + WS_HF, S * D * 2, dst + i * 1024, lds_rd128(tb + (i * 16 + (lane >> 2)) * 64 + (((lane & 3) ^ ((lane >> 3) & 3)) * 16)));
}

__global__ void __launch_bounds__(512, 2) k_pre(const char* __restrict__ x, const char* __restrict__ wq, const char* __restrict__ wk, const char* __restrict__ wv,
                                                const char* __restrict__ gw, const char* __restrict__ w1, char* __restrict__ ws) {
    extern __shared__ __attribute__((aligned(16))) char smem[];
    const int lane = threadIdx.x & 63, w8 = __builtin_amdgcn_readfirstlane(threadIdx.x >> 6), b = blockIdx.x;
    const u32 scratch = (u32)(uintptr_t)(lds_ptr)smem + 65536 + 1024 + w8 * 256;
    asm volatile("pre_code_begin:");
    if (w8 == 0) {
        uint64_t a, e;
        CODE_ADDR(a, "pre_code_begin"); CODE_ADDR(e, "pre_code_end");
        touch_code(a, e, scratch);
    }
    const int t = (b & 7) + 8 * ((b >> 4) & 7), half = (b >> 3) & 1;
    if (b >= PRE_KV) {
        ln_rows(2 * t + half, x, gw, wq, w1, ws, (lds_ptr)smem, scratch, w8, lane);
        if (b == PRE_KV && threadIdx.x < CNT_N) *(u32*)(ws + WS_CNT + threadIdx.x * CNT_STRIDE) = 0u;
        asm volatile("s_waitcnt vmcnt(0)" ::: "memory");
    } else if (half) kv_block<true>(t, x, wv, ws, (lds_ptr)smem, w8, lane);
    else kv_block<false>(t, x, wk, ws, (lds_ptr)smem, w8, lane);
    asm volatile("pre_code_end:");
}

constexpr int MID_ATT = 256, MID_G1 = 224, MID_PF = 32, G1_NS = 6;
constexpr int KV_STAGE = 16384, KV_NSTAGE = 16;

#define ATT_LOOP_ASM \
    "s_mov_b32 s23, 16\n" \
    "s_mov_b32 s24, 0xc000\n" \
    "s_mov_b32 s22, 0\n" \
    "s_add_u32 s20, %[ldst], 0x800\n" \
    "s_mov_b32 s21, 0x8800\n" \
    "ATT_LOOP_%=:\n" \
    "v_mfma_f32_16x16x32_bf16 v[8:11], v[80:83], v[68:71], 0\n" \
    "v_exp_f32 v0, v0\n" \
    "v_exp_f32 v1, v1\n" \
    "v_mfma_f32_16x16x32_bf16 v[12:15], v[84:87], v[68:71], 0\n" \
    "ds_read_b128 v[96:99], v124 offset:4096\n" \
    "v_exp_f32 v2, v2\n" \
    "v_exp_f32 v3, v3\n" \
    "v_mfma_f32_16x16x32_bf16 v[40:43], v[104:107], v[120:123], v[40:43]\n" \
    "v_cvt_pk_bf16_f32 v116, v0, v1\n" \
    "v_exp_f32 v4, v4\n" \
    "v_exp_f32 v5, v5\n" \
    "v_mfma_f32_16x16x32_bf16 v[44:47], v[108:111], v[120:123], v[44:47]\n" \
    "s_mov_b32 m0, s20\n" \
    "s_add_u32 s20, s20, 0x400\n" \
    "buffer_load_dwordx4 v126, %[srd], s21 offen lds\n" \
    "s_add_u32 s21, s21, 0x400\n" \
    "v_cvt_pk_bf16_f32 v117, v2, v3\n" \
    "v_exp_f32 v6, v6\n" \
    "v_exp_f32 v7, v7\n" \
    "v_mfma_f32_16x16x32_bf16 v[60:63], v[112:115], v[120:123], v[60:63]\n" \
    "v_cvt_pk_bf16_f32 v118, v4, v5\n" \
    "v_cvt_pk_bf16_f32 v119, v6, v7\n" \
    "s_waitcnt lgkmcnt(1)\n" \
    "v_mfma_f32_16x16x32_bf16 v[0:3], v[80:83], v[72:75], 0\n" \
    "v_exp_f32 v8, v8\n" \
    "v_exp_f32 v9, v9\n" \
    "v_mfma_f32_16x16x32_bf16 v[4:7], v[84:87], v[72:75], 0\n" \
    "ds_read_b128 v[100:103], v124 offset:5120\n" \
    "v_exp_f32 v10, v10\n" \
    "v_exp_f32 v11, v11\n" \
    "v_mfma_f32_16x16x32_bf16 v[16:19], v[88:91], v[116:119], v[16:19]\n" \
    "v_cvt_pk_bf16_f32 v120, v8, v9\n" \
    "v_exp_f32 v12, v12\n" \
    "v_exp_f32 v13, v13\n" \
    "v_mfma_f32_16x16x32_bf16 v[20:23], v[92:95], v[116:119], v[20:23]\n" \
    "v_cvt_pk_bf16_f32 v121, v10, v11\n" \
    "v_exp_f32 v14, v14\n" \
    "v_exp_f32 v15, v15\n" \
    "v_mfma_f32_16x16x32_bf16 v[48:51], v[112:115], v[116:119], v[48:51]\n" \
    "v_cvt_pk_bf16_f32 v122, v12, v13\n" \
    "v_cvt_pk_bf16_f32 v123, v14, v15\n" \
    "v_mfma_f32_16x16x32_bf16 v[8:11], v[80:83], v[76:79], 0\n" \
    "v_exp_f32 v0, v0\n" \
    "v_exp_f32 v1, v1\n" \
    "v_mfma_f32_16x16x32_bf16 v[12:15], v[84:87], v[76:79], 0\n" \
    "ds_read_b128 v[104:107], v124 offset:6144\n" \
    "v_exp_f32 v2, v2\n" \
    "v_exp_f32 v3, v3\n" \
    "v_mfma_f32_16x16x32_bf16 v[24:27], v[88:91], v[120:123], v[24:27]\n" \
    "v_cvt_pk_bf16_f32 v116, v0, v1\n" \
    "v_exp_f32 v4, v4\n" \
    "v_exp_f32 v5, v5\n" \
    "v_mfma_f32_16x16x32_bf16 v[28:31], v[92:95], v[120:123], v[28:31]\n" \
    "s_mov_b32 m0, s20\n" \
    "s_add_u32 s20, s20, 0x400\n" \
    "buffer_load_dwordx4 v126, %[srd], s21 offen lds\n" \
    "s_add_u32 s21, s21, 0x400\n" \
    "s_add_u32 s22, s22, 0x4000\n" \
    "s_and_b32 s22, s22, 0xffff\n" \
    "s_add_u32 s20, s22, %[ldst]\n" \
    "s_add_u32 s21, s21, 0x3000\n" \
    "v_cvt_pk_bf16_f32 v117, v2, v3\n" \
    "v_exp_f32 v6, v6\n" \
    "v_exp_f32 v7, v7\n" \
    "v_mfma_f32_16x16x32_bf16 v[52:55], v[112:115], v[120:123], v[52:55]\n" \
    "v_cvt_pk_bf16_f32 v118, v4, v5\n" \
    "v_cvt_pk_bf16_f32 v119, v6, v7\n" \
    "s_waitcnt lgkmcnt(1)\n" \
    "v_mfma_f32_16x16x32_bf16 v[0:3], v[96:99], v[64:67], 0\n" \
    "v_exp_f32 v8, v8\n" \
    "v_exp_f32 v9, v9\n" \
    "v_mfma_f32_16x16x32_bf16 v[4:7], v[100:103], v[64:67], 0\n" \
    "ds_read_b128 v[108:111], v124 offset:7168\n" \
    "v_exp_f32 v10, v10\n" \
    "v_exp_f32 v11, v11\n" \
    "v_mfma_f32_16x16x32_bf16 v[32:35], v[88:91], v[116:119], v[32:35]\n" \
    "v_cvt_pk_bf16_f32 v120, v8, v9\n" \
    "v_exp_f32 v12, v12\n" \
    "v_exp_f32 v13, v13\n" \
    "v_mfma_f32_16x16x32_bf16 v[36:39], v[92:95], v[116:119], v[36:39]\n" \
    "v_cvt_pk_bf16_f32 v121, v10, v11\n" \
    "v_exp_f32 v14, v14\n" \
    "v_exp_f32 v15, v15\n" \
    "v_mfma_f32_16x16x32_bf16 v[56:59], v[112:115], v[116:119], v[56:59]\n" \
    "v_cvt_pk_bf16_f32 v122, v12, v13\n" \
    "v_cvt_pk_bf16_f32 v123, v14, v15\n" \
    "v_mfma_f32_16x16x32_bf16 v[8:11], v[96:99], v[68:71], 0\n" \
    "v_exp_f32 v0, v0\n" \
    "v_exp_f32 v1, v1\n" \
    "v_mfma_f32_16x16x32_bf16 v[12:15], v[100:103], v[68:71], 0\n" \
    "ds_read_b128 v[80:83], v124 offset:8192\n" \
    "v_exp_f32 v2, v2\n" \
    "v_exp_f32 v3, v3\n" \
    "v_mfma_f32_16x16x32_bf16 v[40:43], v[88:91], v[120:123], v[40:43]\n" \
    "v_cvt_pk_bf16_f32 v116, v0, v1\n" \
    "v_exp_f32 v4, v4\n" \
    "v_exp_f32 v5, v5\n" \
    "v_mfma_f32_16x16x32_bf16 v[44:47], v[92:95], v[120:123], v[44:47]\n" \
    "v_cvt_pk_bf16_f32 v117, v2, v3\n" \
    "v_exp_f32 v6, v6\n" \
    "v_exp_f32 v7, v7\n" \
    "v_mfma_f32_16x16x32_bf16 v[60:63], v[112:115], v[120:123], v[60:63]\n" \
    "v_cvt_pk_bf16_f32 v118, v4, v5\n" \
    "v_cvt_pk_bf16_f32 v119, v6, v7\n" \
    "s_waitcnt lgkmcnt(1)\n" \
    "v_mfma_f32_16x16x32_bf16 v[0:3], v[96:99], v[72:75], 0\n" \
    "v_exp_f32 v8, v8\n" \
    "v_exp_f32 v9, v9\n" \
    "v_mfma_f32_16x16x32_bf16 v[4:7], v[100:103], v[72:75], 0\n" \
    "ds_read_b128 v[84:87], v124 offset:9216\n" \
    "v_exp_f32 v10, v10\n" \
    "v_exp_f32 v11, v11\n" \
    "v_mfma_f32_16x16x32_bf16 v[16:19], v[104:107], v[116:119], v[16:19]\n" \
    "v_cvt_pk_bf16_f32 v120, v8, v9\n" \
    "v_exp_f32 v12, v12\n" \
    "v_exp_f32 v13, v13\n" \
    "v_mfma_f32_16x16x32_bf16 v[20:23], v[108:111], v[116:119], v[20:23]\n" \
    "v_cvt_pk_bf16_f32 v121, v10, v11\n" \
    "v_exp_f32 v14, v14\n" \
    "v_exp_f32 v15, v15\n" \
    "v_mfma_f32_16x16x32_bf16 v[48:51], v[112:115], v[116:119], v[48:51]\n" \
    "v_cvt_pk_bf16_f32 v122, v12, v13\n" \
    "v_cvt_pk_bf16_f32 v123, v14, v15\n" \
    "v_mfma_f32_16x16x32_bf16 v[8:11], v[96:99], v[76:79], 0\n" \
    "v_exp_f32 v0, v0\n" \
    "v_exp_f32 v1, v1\n" \
    "v_mfma_f32_16x16x32_bf16 v[12:15], v[100:103], v[76:79], 0\n" \
    "ds_read_b128 v[88:91], v124 offset:10240\n" \
    "v_exp_f32 v2, v2\n" \
    "v_exp_f32 v3, v3\n" \
    "v_mfma_f32_16x16x32_bf16 v[24:27], v[104:107], v[120:123], v[24:27]\n" \
    "v_cvt_pk_bf16_f32 v116, v0, v1\n" \
    "v_exp_f32 v4, v4\n" \
    "v_exp_f32 v5, v5\n" \
    "v_mfma_f32_16x16x32_bf16 v[28:31], v[108:111], v[120:123], v[28:31]\n" \
    "v_cvt_pk_bf16_f32 v117, v2, v3\n" \
    "v_exp_f32 v6, v6\n" \
    "v_exp_f32 v7, v7\n" \
    "v_mfma_f32_16x16x32_bf16 v[52:55], v[112:115], v[120:123], v[52:55]\n" \
    "v_cvt_pk_bf16_f32 v118, v4, v5\n" \
    "v_cvt_pk_bf16_f32 v119, v6, v7\n" \
    "s_waitcnt lgkmcnt(1)\n" \
    "v_mfma_f32_16x16x32_bf16 v[0:3], v[80:83], v[64:67], 0\n" \
    "v_exp_f32 v8, v8\n" \
    "v_exp_f32 v9, v9\n" \
    "v_mfma_f32_16x16x32_bf16 v[4:7], v[84:87], v[64:67], 0\n" \
    "ds_read_b128 v[92:95], v124 offset:11264\n" \
    "v_exp_f32 v10, v10\n" \
    "v_exp_f32 v11, v11\n" \
    "v_mfma_f32_16x16x32_bf16 v[32:35], v[104:107], v[116:119], v[32:35]\n" \
    "v_cvt_pk_bf16_f32 v120, v8, v9\n" \
    "v_exp_f32 v12, v12\n" \
    "v_exp_f32 v13, v13\n" \
    "v_mfma_f32_16x16x32_bf16 v[36:39], v[108:111], v[116:119], v[36:39]\n" \
    "v_cvt_pk_bf16_f32 v121, v10, v11\n" \
    "v_exp_f32 v14, v14\n" \
    "v_exp_f32 v15, v15\n" \
    "v_mfma_f32_16x16x32_bf16 v[56:59], v[112:115], v[116:119], v[56:59]\n" \
    "v_cvt_pk_bf16_f32 v122, v12, v13\n" \
    "v_cvt_pk_bf16_f32 v123, v14, v15\n" \
    "v_mfma_f32_16x16x32_bf16 v[8:11], v[80:83], v[68:71], 0\n" \
    "v_exp_f32 v0, v0\n" \
    "v_exp_f32 v1, v1\n" \
    "v_mfma_f32_16x16x32_bf16 v[12:15], v[84:87], v[68:71], 0\n" \
    "ds_read_b128 v[96:99], v124 offset:12288\n" \
    "v_exp_f32 v2, v2\n" \
    "v_exp_f32 v3, v3\n" \
    "v_mfma_f32_16x16x32_bf16 v[40:43], v[104:107], v[120:123], v[40:43]\n" \
    "v_cvt_pk_bf16_f32 v116, v0, v1\n" \
    "v_exp_f32 v4, v4\n" \
    "v_exp_f32 v5, v5\n" \
    "v_mfma_f32_16x16x32_bf16 v[44:47], v[108:111], v[120:123], v[44:47]\n" \
    "v_cvt_pk_bf16_f32 v117, v2, v3\n" \
    "v_exp_f32 v6, v6\n" \
    "v_exp_f32 v7, v7\n" \
    "v_mfma_f32_16x16x32_bf16 v[60:63], v[112:115], v[120:123], v[60:63]\n" \
    "v_cvt_pk_bf16_f32 v118, v4, v5\n" \
    "v_cvt_pk_bf16_f32 v119, v6, v7\n" \
    "s_waitcnt lgkmcnt(1)\n" \
    "v_mfma_f32_16x16x32_bf16 v[0:3], v[80:83], v[72:75], 0\n" \
    "v_exp_f32 v8, v8\n" \
    "v_exp_f32 v9, v9\n" \
    "v_mfma_f32_16x16x32_bf16 v[4:7], v[84:87], v[72:75], 0\n" \
    "ds_read_b128 v[100:103], v124 offset:13312\n" \
    "v_exp_f32 v10, v10\n" \
    "v_exp_f32 v11, v11\n" \
    "v_mfma_f32_16x16x32_bf16 v[16:19], v[88:91], v[116:119], v[16:19]\n" \
    "v_cvt_pk_bf16_f32 v120, v8, v9\n" \
    "v_exp_f32 v12, v12\n" \
    "v_exp_f32 v13, v13\n" \
    "v_mfma_f32_16x16x32_bf16 v[20:23], v[92:95], v[116:119], v[20:23]\n" \
    "v_cvt_pk_bf16_f32 v121, v10, v11\n" \
    "v_exp_f32 v14, v14\n" \
    "v_exp_f32 v15, v15\n" \
    "v_mfma_f32_16x16x32_bf16 v[48:51], v[112:115], v[116:119], v[48:51]\n" \
    "v_cvt_pk_bf16_f32 v122, v12, v13\n" \
    "v_cvt_pk_bf16_f32 v123, v14, v15\n" \
    "v_mfma_f32_16x16x32_bf16 v[8:11], v[80:83], v[76:79], 0\n" \
    "v_exp_f32 v0, v0\n" \
    "v_exp_f32 v1, v1\n" \
    "v_mfma_f32_16x16x32_bf16 v[12:15], v[84:87], v[76:79], 0\n" \
    "ds_read_b128 v[104:107], v124 offset:14336\n" \
    "v_exp_f32 v2, v2\n" \
    "v_exp_f32 v3, v3\n" \
    "v_mfma_f32_16x16x32_bf16 v[24:27], v[88:91], v[120:123], v[24:27]\n" \
    "v_cvt_pk_bf16_f32 v116, v0, v1\n" \
    "v_exp_f32 v4, v4\n" \
    "v_exp_f32 v5, v5\n" \
    "v_mfma_f32_16x16x32_bf16 v[28:31], v[92:95], v[120:123], v[28:31]\n" \
    "v_cvt_pk_bf16_f32 v117, v2, v3\n" \
    "v_exp_f32 v6, v6\n" \
    "v_exp_f32 v7, v7\n" \
    "v_mfma_f32_16x16x32_bf16 v[52:55], v[112:115], v[120:123], v[52:55]\n" \
    "v_cvt_pk_bf16_f32 v118, v4, v5\n" \
    "v_cvt_pk_bf16_f32 v119, v6, v7\n" \
    "s_waitcnt lgkmcnt(1)\n" \
    "v_mfma_f32_16x16x32_bf16 v[0:3], v[96:99], v[64:67], 0\n" \
    "v_exp_f32 v8, v8\n" \
    "v_exp_f32 v9, v9\n" \
    "v_mfma_f32_16x16x32_bf16 v[4:7], v[100:103], v[64:67], 0\n" \
    "ds_read_b128 v[108:111], v124 offset:15360\n" \
    "v_exp_f32 v10, v10\n" \
    "v_exp_f32 v11, v11\n" \
    "v_mfma_f32_16x16x32_bf16 v[32:35], v[88:91], v[116:119], v[32:35]\n" \
    "v_cvt_pk_bf16_f32 v120, v8, v9\n" \
    "v_exp_f32 v12, v12\n" \
    "v_exp_f32 v13, v13\n" \
    "v_mfma_f32_16x16x32_bf16 v[36:39], v[92:95], v[116:119], v[36:39]\n" \
    "v_cvt_pk_bf16_f32 v121, v10, v11\n" \
    "v_exp_f32 v14, v14\n" \
    "v_exp_f32 v15, v15\n" \
    "v_mfma_f32_16x16x32_bf16 v[56:59], v[112:115], v[116:119], v[56:59]\n" \
    "v_cvt_pk_bf16_f32 v122, v12, v13\n" \
    "v_cvt_pk_bf16_f32 v123, v14, v15\n" \
    "s_waitcnt vmcnt(4)\n" \
    "s_barrier\n" \
    "v_mfma_f32_16x16x32_bf16 v[8:11], v[96:99], v[68:71], 0\n" \
    "v_exp_f32 v0, v0\n" \
    "v_exp_f32 v1, v1\n" \
    "v_mfma_f32_16x16x32_bf16 v[12:15], v[100:103], v[68:71], 0\n" \
    "ds_read_b128 v[80:83], v125 offset:0\n" \
    "v_exp_f32 v2, v2\n" \
    "v_exp_f32 v3, v3\n" \
    "v_mfma_f32_16x16x32_bf16 v[40:43], v[88:91], v[120:123], v[40:43]\n" \
    "v_cvt_pk_bf16_f32 v116, v0, v1\n" \
    "v_exp_f32 v4, v4\n" \
    "v_exp_f32 v5, v5\n" \
    "v_mfma_f32_16x16x32_bf16 v[44:47], v[92:95], v[120:123], v[44:47]\n" \
    "s_mov_b32 m0, s20\n" \
    "s_add_u32 s20, s20, 0x400\n" \
    "buffer_load_dwordx4 v126, %[srd], s21 offen lds\n" \
    "s_add_u32 s21, s21, 0x400\n" \
    "v_cvt_pk_bf16_f32 v117, v2, v3\n" \
    "v_exp_f32 v6, v6\n" \
    "v_exp_f32 v7, v7\n" \
    "v_mfma_f32_16x16x32_bf16 v[60:63], v[112:115], v[120:123], v[60:63]\n" \
    "v_cvt_pk_bf16_f32 v118, v4, v5\n" \
    "v_cvt_pk_bf16_f32 v119, v6, v7\n" \
    "s_waitcnt lgkmcnt(1)\n" \
    "v_mfma_f32_16x16x32_bf16 v[0:3], v[96:99], v[72:75], 0\n" \
    "v_exp_f32 v8, v8\n" \
    "v_exp_f32 v9, v9\n" \
    "v_mfma_f32_16x16x32_bf16 v[4:7], v[100:103], v[72:75], 0\n" \
    "ds_read_b128 v[84:87], v125 offset:1024\n" \
    "v_exp_f32 v10, v10\n" \
    "v_exp_f32 v11, v11\n" \
    "v_mfma_f32_16x16x32_bf16 v[16:19], v[104:107], v[116:119], v[16:19]\n" \
    "v_cvt_pk_bf16_f32 v120, v8, v9\n" \
    "v_exp_f32 v12, v12\n" \
    "v_exp_f32 v13, v13\n" \
    "v_mfma_f32_16x16x32_bf16 v[20:23], v[108:111], v[116:119], v[20:23]\n" \
    "v_cvt_pk_bf16_f32 v121, v10, v11\n" \
    "v_exp_f32 v14, v14\n" \
    "v_exp_f32 v15, v15\n" \
    "v_mfma_f32_16x16x32_bf16 v[48:51], v[112:115], v[116:119], v[48:51]\n" \
    "v_cvt_pk_bf16_f32 v122, v12, v13\n" \
    "v_cvt_pk_bf16_f32 v123, v14, v15\n" \
    "v_mfma_f32_16x16x32_bf16 v[8:11], v[96:99], v[76:79], 0\n" \
    "v_exp_f32 v0, v0\n" \
    "v_exp_f32 v1, v1\n" \
    "v_mfma_f32_16x16x32_bf16 v[12:15], v[100:103], v[76:79], 0\n" \
    "ds_read_b128 v[88:91], v125 offset:2048\n" \
    "v_exp_f32 v2, v2\n" \
    "v_exp_f32 v3, v3\n" \
    "v_mfma_f32_16x16x32_bf16 v[24:27], v[104:107], v[120:123], v[24:27]\n" \
    "v_cvt_pk_bf16_f32 v116, v0, v1\n" \
    "v_exp_f32 v4, v4\n" \
    "v_exp_f32 v5, v5\n" \
    "v_mfma_f32_16x16x32_bf16 v[28:31], v[108:111], v[120:123], v[28:31]\n" \
    "s_mov_b32 m0, s20\n" \
    "s_add_u32 s20, s20, 0x400\n" \
    "buffer_load_dwordx4 v126, %[srd], s21 offen lds\n" \
    "s_add_u32 s21, s21, 0x400\n" \
    "v_cvt_pk_bf16_f32 v117, v2, v3\n" \
    "v_exp_f32 v6, v6\n" \
    "v_exp_f32 v7, v7\n" \
    "v_mfma_f32_16x16x32_bf16 v[52:55], v[112:115], v[120:123], v[52:55]\n" \
    "v_cvt_pk_bf16_f32 v118, v4, v5\n" \
    "v_cvt_pk_bf16_f32 v119, v6, v7\n" \
    "s_waitcnt lgkmcnt(1)\n" \
    "v_mfma_f32_16x16x32_bf16 v[0:3], v[80:83], v[64:67], 0\n" \
    "v_exp_f32 v8, v8\n" \
    "v_exp_f32 v9, v9\n" \
    "v_mfma_f32_16x16x32_bf16 v[4:7], v[84:87], v[64:67], 0\n" \
    "ds_read_b128 v[92:95], v125 offset:3072\n" \
    "v_exp_f32 v10, v10\n" \
    "v_exp_f32 v11, v11\n" \
    "v_mfma_f32_16x16x32_bf16 v[32:35], v[104:107], v[116:119], v[32:35]\n" \
    "v_cvt_pk_bf16_f32 v120, v8, v9\n" \
    "v_exp_f32 v12, v12\n" \
    "v_exp_f32 v13, v13\n" \
    "v_mfma_f32_16x16x32_bf16 v[36:39], v[108:111], v[116:119], v[36:39]\n" \
    "v_cvt_pk_bf16_f32 v121, v10, v11\n" \
    "v_exp_f32 v14, v14\n" \
    "v_exp_f32 v15, v15\n" \
    "v_mfma_f32_16x16x32_bf16 v[56:59], v[112:115], v[116:119], v[56:59]\n" \
    "v_cvt_pk_bf16_f32 v122, v12, v13\n" \
    "v_cvt_pk_bf16_f32 v123, v14, v15\n" \
    "v_mov_b32 v124, v125\n" \
    "s_add_u32 s24, s24, 0x4000\n" \
    "s_and_b32 s24, s24, 0xffff\n" \
    "v_add_u32 v125, s24, v127\n" \
    "s_sub_u32 s23, s23, 1\n" \
    "s_cmp_lg_u32 s23, 0\n" \
    "s_cbranch_scc1 ATT_LOOP_%=\n" \
    "v_mfma_f32_16x16x32_bf16 v[40:43], v[104:107], v[120:123], v[40:43]\n" \
    "v_mfma_f32_16x16x32_bf16 v[44:47], v[108:111], v[120:123], v[44:47]\n" \
    "v_mfma_f32_16x16x32_bf16 v[60:63], v[112:115], v[120:123], v[60:63]\n" \
    "s_waitcnt vmcnt(0) lgkmcnt(0)\n" \
    "s_nop 15\n"

constexpr int Q_NS = 3, Q_SLOT = 24576;
__device__ __forceinline__ void attention(int b, const char* x, const char* wq, char* ws, lds_ptr lds, int w, int lane) {
    const int xc = b & 7, t = b >> 3, st128 = (xc >> 1) * 4 + (t & 3), hp = (xc & 1) * 8 + (t >> 2);
    const int head = 2 * hp + (w & 1), s0 = st128 * 128 + (w >> 1) * 64, g = lane >> 4, c = lane & 15;
    const u32x4 srd = make_srd(ws + WS_KV, S * 128);
    const u32 voff = w * 4096 + lane * 16, ldst = (u32)(uintptr_t)lds + w * 4096;
    auto dma = [&](int st, int p0, int p1) {
#pragma unroll
        for (int p = p0; p < p1; ++p) dma16(ldst + ((st + 2) & 3) * KV_STAGE + p * 1024, voff, srd, st * KV_STAGE + p * 1024);
    };
    __builtin_amdgcn_s_setprio(2);
    float gate[4];
#pragma unroll
    for (int i = 0; i < 4; ++i) asm volatile("global_load_dword %0, %1, off" : "=v"(gate[i]) : "v"(ws + WS_G + (s0 + i * 16 + c) * 4) : "memory");
    f32x4 kb2;
    asm volatile("global_load_dwordx4 %0, %1, off" : "=v"(kb2) : "v"(ws + WS_BND + lane * 16) : "memory");
    u32x4 q[4];
    {
        OpTr<64, D * 2, 2, true, 2> a; a.init(wq, D * D * 2, hp * 64, 0, lds, w, (w & 1) * 2, lane);
        OpRow bb; bb.init(x, S * D * 2, st128 * 128, lds + a.BYTES, w, (w >> 1) * 64, lane);
        Engine<Q_NS, 2, 4, 2> e;
        static_assert((D / 64 - 1) % Q_NS == 0 && Q_SLOT <= 2 * KV_STAGE, "the last Q slot lies below the first two K/V stages");
        gemm_run<Q_SLOT, D / 64, 2>(e, a, bb, [] {}, [](int) {}, [&](auto p) { if constexpr (p.v == Rounds<Q_NS, D / 64>::TAIL - 2) { dma(0, 0, 4); dma(1, 0, 4); } });
        wait_bar<4>();
        dma(2, 0, 2);
#pragma unroll
        for (int i = 0; i < 4; ++i) q[i] = pack8(e.acc[0][i] * QSCALE, e.acc[1][i] * QSCALE);
    }
    PIN(kb2);
    const float k2 = wave_max(fmaxf(fmaxf(kb2[0], kb2[1]), fmaxf(kb2[2], kb2[3])));
    bool ok = true;
#pragma unroll
    for (int i = 0; i < 4; ++i) {
        float n = 0.f;
#pragma unroll
        for (int m = 0; m < 4; ++m) { const float a = bf_lo(q[i][m]), h = bf_hi(q[i][m]); n += a * a + h * h; }
        ok = ok && group_sum(n) * k2 * 1.02f <= SAFE_LOG2 * SAFE_LOG2;
        PIN(gate[i]);
    }
    const bool safe = __all(ok);
    __builtin_amdgcn_s_setprio(0);
    f32x4 o[8];
#pragma unroll
    for (int i = 0; i < 8; ++i) o[i] = f32x4{};
    float lsum[4];
    const lds_ptr base = lds + lane * 16;
    const f32x4 z = {};
    if (safe) {
        const u32 one = c == 0 ? 0x3f803f80u : 0u;
        const u32x4 ones = {one, one, one, one};
        const lds_ptr first = base + 2 * KV_STAGE;
        u32x4 ka = lds_rd128(first), kb = lds_rd128(first + 1024), v0 = lds_rd128(first + 2048), v1 = lds_rd128(first + 3072);
        f32x4 sa = M16(ka, q[0], z), sb = M16(kb, q[0], z);
        f32x16 oa = {}, ob = {}, l = {};
        u32x4 vz0 = {}, vz1 = {}, pz = {};
        u32 cur = (u32)(uintptr_t)first, nxt = cur + KV_STAGE;
        asm volatile("s_nop 7\n" ATT_LOOP_ASM
                     : "+{v[0:3]}"(sa), "+{v[4:7]}"(sb), "+{v[16:31]}"(oa), "+{v[32:47]}"(ob), "+{v[48:63]}"(l), "+{v[80:83]}"(ka), "+{v[84:87]}"(kb),
                       "+{v[88:91]}"(v0), "+{v[92:95]}"(v1), "+{v[104:107]}"(vz0), "+{v[108:111]}"(vz1), "+{v[120:123]}"(pz), "+{v124}"(cur), "+{v125}"(nxt)
                     : "{v[64:67]}"(q[0]), "{v[68:71]}"(q[1]), "{v[72:75]}"(q[2]), "{v[76:79]}"(q[3]), "{v[112:115]}"(ones), "{v126}"(voff),
                       "{v127}"((u32)(uintptr_t)base), [srd] "s"(srd), [ldst] "s"(ldst)
                     : "v8", "v9", "v10", "v11", "v12", "v13", "v14", "v15", "v96", "v97", "v98", "v99", "v100", "v101", "v102", "v103",
                       "v116", "v117", "v118", "v119", "s20", "s21", "s22", "s23", "s24", "scc", "memory");
#pragma unroll
        for (int i = 0; i < 4; ++i) {
            lsum[i] = __shfl(l[4 * i], c);
            o[i] = f32x4{oa[4 * i], oa[4 * i + 1], oa[4 * i + 2], oa[4 * i + 3]};
            o[4 + i] = f32x4{ob[4 * i], ob[4 * i + 1], ob[4 * i + 2], ob[4 * i + 3]};
        }
    } else {
        float mx[4];
#pragma unroll
        for (int i = 0; i < 4; ++i) { mx[i] = -__builtin_inff(); lsum[i] = 0.f; }
        dma(2, 2, 4);
        for (int st = 0; st < KV_NSTAGE; ++st) {
            wait_bar<4>();
            dma(st + 3, 0, 4);
#pragma unroll 1
            for (int kb = 0; kb < 4; ++kb) {
                const lds_ptr p = base + ((st + 2) & 3) * KV_STAGE + kb * 4096;
                const u32x4 k0 = lds_rd128(p), k1 = lds_rd128(p + 1024), v0 = lds_rd128(p + 2048), v1 = lds_rd128(p + 3072);
#pragma unroll
                for (int i = 0; i < 4; ++i) {
                    f32x4 sa = M16(k0, q[i], z), sb = M16(k1, q[i], z);
                    float m = mx[i];
#pragma unroll
                    for (int r = 0; r < 4; ++r) m = fmaxf(m, fmaxf(sa[r], sb[r]));
                    m = fmaxf(m, __shfl_xor(m, 16)); m = fmaxf(m, __shfl_xor(m, 32));
                    const float alpha = EX2(mx[i] - m);
                    mx[i] = m;
                    float t = 0.f;
#pragma unroll
                    for (int r = 0; r < 4; ++r) { sa[r] = EX2(sa[r] - m); sb[r] = EX2(sb[r] - m); t += sa[r] + sb[r]; }
                    lsum[i] = lsum[i] * alpha + t;
                    const u32x4 pp = pack8(sa, sb);
                    o[2 * i] = M16(v0, pp, o[2 * i] * alpha);
                    o[2 * i + 1] = M16(v1, pp, o[2 * i + 1] * alpha);
                }
            }
        }
        asm volatile("s_waitcnt vmcnt(0)" ::: "memory");
#pragma unroll
        for (int i = 0; i < 4; ++i) lsum[i] = group_sum(lsum[i]);
    }
#pragma unroll
    for (int i = 0; i < 4; ++i) {
        const float r = gate[i] / lsum[i];
        store_wt(ws + WS_OF, S * D * 2, (u32)((head * 4 + g) * S + s0 + i * 16 + c) * 16u, pack8(o[2 * i] * r, o[2 * i + 1] * r));
    }
    publish(ws, CNT_O + st128);
}

__device__ __forceinline__ f32x2 gelu_tanh(f32x2 x) {
    constexpr float C0 = -2.f * 0.7978845608028654f * 1.4426950408889634f, C1 = C0 * 0.044715f;
    const f32x2 y = x * __builtin_elementwise_fma(x * x, f32x2{C1, C1}, f32x2{C0, C0});
    const f32x2 d = f32x2{EX2(y[0]), EX2(y[1])} + 1.f;
    return x * f32x2{__builtin_amdgcn_rcpf(d[0]), __builtin_amdgcn_rcpf(d[1])};
}

__device__ __forceinline__ void gemm1_tile(int b, const void* w1, const char* b1, char* ws, lds_ptr lds, int w, int lane) {
    const int x = b & 7, t = b >> 3;
    const int dt = (x & 1) * 4 + (t & 3), d0 = dt * 128, f0 = ((x >> 1) * 7 + (t >> 2)) * 128;
    const int g = lane >> 4, c = lane & 15;
    u32x2 bias[8];
#pragma unroll
    for (int j = 0; j < 8; ++j) bias[j] = *(const u32x2*)(b1 + (f0 + j * 16 + g * 4) * 2);
    OpReg<2, D, false> a; a.init(ws + WS_HF, S * D * 2, d0 + w * 32, 0, lane);
    OpTr<128, F * 2, 1, false, 8> bb; bb.init(w1, S * F * 2, f0, 0, lds, w, 0, lane);
    Engine<G1_NS, 2, 8> e;
    const u32 scratch = (u32)(uintptr_t)lds + G1_NS * 8192 + w * 256;
    gemm_run<8192, S / 32, 0>(e, a, bb, [] {}, [&](int r) { if (r == 1) touch<S * F * 2 - W1_PRE, MID_G1, 256>((const char*)w1 + W1_PRE, b, scratch); }, [](auto) {});
    auto& acc = e.acc;
#pragma unroll
    for (int jp = 0; jp < 4; ++jp)
#pragma unroll
        for (int i = 0; i < 2; ++i) {
            f32x4 u[2];
#pragma unroll
            for (int h = 0; h < 2; ++h) {
                const u32x2 bv = bias[2 * jp + h];
                const f32x4 v = acc[i][2 * jp + h] + f32x4{bf_lo(bv[0]), bf_hi(bv[0]), bf_lo(bv[1]), bf_hi(bv[1])};
                const f32x2 lo = gelu_tanh(f32x2{v[0], v[1]}), hi = gelu_tanh(f32x2{v[2], v[3]});
                u[h] = f32x4{lo[0], lo[1], hi[0], hi[1]};
            }
            store_wt(ws + WS_UF, F * D * 2, (u32)(((f0 / 32 + jp) * 4 + g) * D + d0 + w * 32 + i * 16 + c) * 16u, pack8(u[0], u[1]));
        }
    publish(ws, CNT_U + dt * 2 + (x >> 2));
}

constexpr int P1_NS = 8, P1_BASE = 8192, P2_NS = 6, P2_SLOT = 12288;

__device__ __forceinline__ void post_tile(int b, const char* w2, const char* b2, const char* wo, char* ws, char* out, lds_ptr lds, int w, int lane) {
    const int x = b & 7, t = b >> 3, h = t & 1;
    const int s0 = ((x >> 1) * 8 + (t & 7)) * 64, dt = (x & 1) * 4 + (t >> 3), d0 = dt * 128;
    const int g = lane >> 4, c = lane & 15;
    constexpr int N1 = F / 64, N2 = D / 32;
    static_assert(N1 % P1_NS == 0 && P1_BASE + (P1_NS - 1) * 8192 >= (P2_NS - 1) * P2_SLOT, "the last w2 slot lies in the last wo slot");
    float omg[4], bs[4];
#pragma unroll
    for (int j = 0; j < 4; ++j) {
        const int s = s0 + j * 16 + c;
        asm volatile("global_load_dword %0, %1, off" : "=v"(omg[j]) : "v"(ws + WS_G + s * 4) : "memory");
        asm volatile("global_load_ushort %0, %1, off" : "=v"(bs[j]) : "v"(b2 + s * 2) : "memory");
    }
    OpTr<128, D * 2, 1, false, 2> a2; a2.init(wo, D * D * 2, d0, 0, lds, w, w * 2, lane);
    OpFrag<64, S> b2o; b2o.init(ws + WS_OF, S * D * 2, s0, lds + 8192, w, 0, lane);
    Engine<P2_NS, 2, 4> e;
    auto& acc = e.acc;
    u32 seen = 0;
    {
        OpReg<2, D, true> a1; a1.init(ws + WS_UF, F * D * 2, d0 + w * 32, h * N1, lane);
        OpTr<128, S * 2, 1, false, 8> b1; b1.init(w2, F * S * 2, s0 & ~127, h * N1, lds + P1_BASE, w, 4 * h, lane);
        Engine<P1_NS, 2, 8> e1;
        constexpr int TAIL = Rounds<P1_NS, N1>::TAIL;
        gemm_run<8192, N1, 2>(e1, a1, b1,
            [&] { if (w == 0) await(ws, CNT_U + dt * 2 + h, F / 256); __builtin_amdgcn_s_barrier(); },
            [&](int r) { if (r == Rounds<P1_NS, N1>::N - 1) sample(seen, ws, CNT_O + (s0 >> 7)); },
            [&](auto p) { if constexpr (p.v == TAIL - 2) { confirm(seen, ws, CNT_O + (s0 >> 7), 16); gemm_fill<P2_SLOT>(a2, b2o, 0, P2_NS - 1); } });
#pragma unroll
        for (int j = 0; j < 4; ++j) {
            PIN(omg[j]); PIN(bs[j]);
            omg[j] = bf_round(1.f - omg[j]); bs[j] = __uint_as_float(__float_as_uint(bs[j]) << 16);
#pragma unroll
            for (int i = 0; i < 2; ++i) {
                store_wt(ws + WS_X, 256u << 15, (u32)(((b * 4 + w) * 8 + i * 4 + j) * 64 + lane) * 16u, __builtin_bit_cast(u32x4, e1.acc[i][4 + j]));
                acc[i][j] = (e1.acc[i][j] + bs[j]) * omg[j];
            }
        }
    }
    constexpr int PPW = 3, XW = 8;
    wait_bar<(P2_NS - 2) * PPW + XW>();
    gemm_fill<P2_SLOT>(a2, b2o, P2_NS - 1, P2_NS);
    gemm_first(e, a2, b2o);
    gemm_steps<0, P2_NS, MANY, P2_SLOT, 2, XW>(e, a2, b2o, P2_NS, [&](auto p) { if constexpr (p.v == P2_NS - 2) signal(ws, CNT_X + b); });
    for (int r = 1; r < 3; ++r) gemm_steps<0, P2_NS, MANY, P2_SLOT, 2, 0>(e, a2, b2o, (r + 1) * P2_NS, [](auto) {});
    seen = 0;
    u32x4 got[8];
    const u32x4 xsrd = make_srd(ws + WS_X, 256u << 15);
    gemm_steps<0, N2 - 3 * P2_NS, N2 - 3 * P2_NS, P2_SLOT, 2, 0>(e, a2, b2o, 4 * P2_NS, [&](auto p) {
        if constexpr (p.v == 0) sample(seen, ws, CNT_X + (b ^ 8));
        if constexpr (p.v == P2_NS - 2) {
            confirm(seen, ws, CNT_X + (b ^ 8), 1);
#pragma unroll
            for (int i = 0; i < 8; ++i) {
                got[i] = u32x4{};
                load_coh(got[i], lane * 16, xsrd, (u32)(((b ^ 8) * 4 + w) * 8 + i) * 1024u);
            }
        }
    });
    asm volatile("s_waitcnt vmcnt(0)" ::: "memory");
#pragma unroll
    for (int i = 0; i < 8; ++i) { PIN(got[i]); acc[i >> 2][i & 3] += __builtin_bit_cast(f32x4, got[i]) * omg[i & 3]; }
#pragma unroll
    for (int j = 0; j < 4; ++j) {
        const auto r0 = __builtin_amdgcn_permlane16_swap(cvtpk(acc[0][j][0], acc[0][j][1]), cvtpk(acc[1][j][0], acc[1][j][1]), false, false);
        const auto r1 = __builtin_amdgcn_permlane16_swap(cvtpk(acc[0][j][2], acc[0][j][3]), cvtpk(acc[1][j][2], acc[1][j][3]), false, false);
        const int s = s0 + j * 16 + c, d = d0 + w * 32 + (g & 1) * 16 + (g >> 1) * 8;
        *(u32x4*)(out + ((size_t)s * D + d) * 2) = u32x4{r0[0], r1[0], r0[1], r1[1]};
    }
}

constexpr int MAIN_LDS = Q_NS * Q_SLOT;
static_assert(MAIN_LDS >= P2_NS * P2_SLOT && MAIN_LDS >= P1_BASE + P1_NS * 8192 && MAIN_LDS >= 4 * KV_STAGE && 2 * MAIN_LDS <= 160 * 1024, "two workgroups per CU");

__global__ void __launch_bounds__(256, 2) k_main(const char* __restrict__ x, const char* __restrict__ wq, const char* __restrict__ w1, const char* __restrict__ b1, const char* __restrict__ w2, const char* __restrict__ b2,
                                                 const char* __restrict__ wo, char* __restrict__ ws, char* __restrict__ out) {
    extern __shared__ __attribute__((aligned(16))) char smem[];
    const int lane = threadIdx.x & 63, w = __builtin_amdgcn_readfirstlane(threadIdx.x >> 6), b = blockIdx.x;
    asm volatile("main_code_begin:");
    if (b < MID_ATT) {
        attention(b, x, wq, ws, (lds_ptr)smem, w, lane);
    } else {
        const int gb = b - MID_ATT;
        __builtin_amdgcn_s_setprio(1);
        if (gb < MID_G1) gemm1_tile(gb, w1, b1, ws, (lds_ptr)smem, w, lane);
        else {
            const u32 scratch = (u32)(uintptr_t)(lds_ptr)smem + w * 256;
            for (int i = 0; i < 5; ++i) __builtin_amdgcn_s_sleep(64);
            for (int i = 0; i < 4; ++i) {
                touch<F * S * 2, MID_PF * 4, 256>(w2, (gb - MID_G1) * 4 + i, scratch);
                asm volatile("s_waitcnt vmcnt(0)" ::: "memory");
            }
            touch<D * D * 2, MID_PF, 256>(wo, gb - MID_G1, scratch);
            wait_bar<0>();
        }
        __builtin_amdgcn_s_setprio(0);
        post_tile(gb, w2, b2, wo, ws, out, (lds_ptr)smem, w, lane);
    }
    asm volatile("main_code_end:");
}

extern "C" void kernel_launch(void* const* d_in, const int* in_sizes, int n_in, void* d_out, int out_size, void* d_ws, size_t ws_size,
                              hipStream_t stream) {
    const char* x = (const char*)d_in[0];
    const char *wq = (const char*)d_in[1], *wk = (const char*)d_in[2], *wv = (const char*)d_in[3], *wo = (const char*)d_in[4];
    const char *w1 = (const char*)d_in[6], *b1 = (const char*)d_in[7], *w2 = (const char*)d_in[8], *b2 = (const char*)d_in[9];
    const char* gw = (const char*)d_in[10];
    char* ws = (char*)d_ws;
    (void)hipFuncSetAttribute((const void*)k_pre, hipFuncAttributeMaxDynamicSharedMemorySize, PRE_LDS);
    (void)hipFuncSetAttribute((const void*)k_main, hipFuncAttributeMaxDynamicSharedMemorySize, MAIN_LDS);
    k_pre<<<PRE_KV + PRE_LN, 512, PRE_LDS, stream>>>(x, wq, wk, wv, gw, w1, ws);
    k_main<<<MID_ATT + MID_G1 + MID_PF, 256, MAIN_LDS, stream>>>(x, wq, w1, b1, w2, b2, wo, ws, (char*)d_out);
}
```
